```python
import torch

def _softmax(x, axis=-1):
    return torch.softmax(x, dim=axis)
def _gelu(x):
    return torch.nn.functional.gelu(x, approximate="tanh")
def _silu(x):
    return torch.nn.functional.silu(x)
def _sigmoid(x):
    return torch.sigmoid(x)
def _layernorm(x, axis=-1, eps=1e-5):
    m = x.mean(dim=axis, keepdim=True)
    v = x.var(dim=axis, keepdim=True, unbiased=False)
    return (x - m) / torch.sqrt(v + eps)
def _scatter_add(tgt, idx, src, axis=0):
    return tgt.clone().index_add_(axis, idx, src)
def _segment_sum(src, seg, n):
    out = torch.zeros((n,) + src.shape[1:], dtype=src.dtype)
    return out.index_add_(0, seg, src)
def _pad(x, pw):
    flat = tuple(v for pair in reversed(pw) for v in pair)
    return torch.nn.functional.pad(x, flat)

kernel_name = 'concat_heads__mha_block_cached__mha_block_cached__near_ridge'

def setup_inputs():

    _w = (lambda a: (lambda t: t.to(torch.bfloat16) if t.is_floating_point() else t)(torch.as_tensor(a)))
    def _A_setup():
        B, Sq, D = 8, 256, 576
        H, Sk, Dh = 32, 1024, 128
        return {
            "x": _w((lambda *s: torch.randn(*s, dtype=torch.bfloat16) * 0.8)(B, Sq, D)),
            "k_cache": _w((lambda *s: torch.randn(*s, dtype=torch.bfloat16) * 0.8)(B, H, Sk, Dh)),
            "v_cache": _w((lambda *s: torch.randn(*s, dtype=torch.bfloat16) * 0.05)(B, H, Sk, Dh)),
            "wq": _w((lambda *s: torch.randn(*s, dtype=torch.bfloat16) * 0.8)(D, H * Dh) * (D ** -0.5)),
            "wo": _w((lambda *s: torch.randn(*s, dtype=torch.bfloat16) * 0.05)(H * Dh, D) * ((H * Dh) ** -0.5)),
        }
    _A_in = _A_setup()

    def _B_setup():
        B, Sq, D = 8, 256, 576
        H, Sk, Dh = 32, 1024, 128
        return {
            "x": _w((lambda *s: torch.randn(*s, dtype=torch.bfloat16) * 0.8)(B, Sq, D)),
            "k_cache": _w((lambda *s: torch.randn(*s, dtype=torch.bfloat16) * 0.8)(B, H, Sk, Dh)),
            "v_cache": _w((lambda *s: torch.randn(*s, dtype=torch.bfloat16) * 0.05)(B, H, Sk, Dh)),
            "wq": _w((lambda *s: torch.randn(*s, dtype=torch.bfloat16) * 0.8)(D, H * Dh) * (D ** -0.5)),
            "wo": _w((lambda *s: torch.randn(*s, dtype=torch.bfloat16) * 0.05)(H * Dh, D) * ((H * Dh) ** -0.5)),
        }
    _B_in = _B_setup()

    return {
        "A_x": _A_in["x"],
        "A_k_cache": _A_in["k_cache"],
        "A_v_cache": _A_in["v_cache"],
        "A_wq": _A_in["wq"],
        "A_wo": _A_in["wo"],
        "B_x": _B_in["x"],
        "B_k_cache": _B_in["k_cache"],
        "B_v_cache": _B_in["v_cache"],
        "B_wq": _B_in["wq"],
        "B_wo": _B_in["wo"],
    }

def reference(A_x, A_k_cache, A_v_cache, A_wq, A_wo, B_x, B_k_cache, B_v_cache, B_wq, B_wo):
    def _A_ref(x, k_cache, v_cache, wq, wo):
        B, Sq, D = x.shape
        H, Dh = 32, 128
        q = (lambda x, *d: x.permute(*d))((x @ wq).reshape(B, Sq, H, Dh), 0, 2, 1, 3)
        scores = torch.einsum("bhqd,bhkd->bhqk", q, k_cache) * (Dh ** -0.5)
        p = _softmax(scores, axis=-1)
        o = torch.einsum("bhqk,bhkd->bhqd", p, v_cache)
        return (lambda x, *d: x.permute(*d))(o, 0, 2, 1, 3).reshape(B, Sq, H * Dh) @ wo

    def _B_ref(x, k_cache, v_cache, wq, wo):
        B, Sq, D = x.shape
        H, Dh = 32, 128
        q = (lambda x, *d: x.permute(*d))((x @ wq).reshape(B, Sq, H, Dh), 0, 2, 1, 3)
        scores = torch.einsum("bhqd,bhkd->bhqk", q, k_cache) * (Dh ** -0.5)
        p = _softmax(scores, axis=-1)
        o = torch.einsum("bhqk,bhkd->bhqd", p, v_cache)
        return (lambda x, *d: x.permute(*d))(o, 0, 2, 1, 3).reshape(B, Sq, H * Dh) @ wo

    a = _A_ref(A_x, A_k_cache, A_v_cache, A_wq, A_wo)
    b = _B_ref(B_x, B_k_cache, B_v_cache, B_wq, B_wo)
    return torch.concatenate([a, b], axis=-1)
```

```cpp
#include <hip/hip_runtime.h>
#include <hip/hip_bf16.h>
using bf16 = __hip_bfloat16;
using bf16x8 = __attribute__((ext_vector_type(8))) short;
using s16x4  = __attribute__((ext_vector_type(4))) short;
using f32x16 = __attribute__((ext_vector_type(16))) float;
using f32x4 = __attribute__((ext_vector_type(4))) float;
using u32x4  = __attribute__((ext_vector_type(4))) unsigned;
constexpr int D = 128, NW = 8, QBLK = 32, KVBLK = 64;
constexpr float SCALE = 0.088388347648318440f;
constexpr float THR = 8.f;
constexpr int SDEPTH = 2;
constexpr int LDQ = 4096, LDK = 128, LDO = 4096;
constexpr size_t SHM_V = KVBLK * D * 2, SHM_K = KVBLK * D * 2, SHM_ATTN = 2 * SHM_V + 2 * SHM_K + NW * 64 * 4;
#define KSWZ(row, colB) ((row) * 256 + ((colB) ^ (((row) & 7) << 4)))
#define SBAR() __builtin_amdgcn_sched_barrier(0)
__device__ __forceinline__ int crow(int r, int hi) { return (r & 3) + 8 * (r >> 2) + 4 * hi; }
__device__ __forceinline__ unsigned cvtpk(float lo, float hi) {
  unsigned r; asm volatile("v_cvt_pk_bf16_f32 %0, %1, %2" : "=v"(r) : "v"(lo), "v"(hi)); return r;
}
__device__ __forceinline__ void partialSM(f32x16& p0, f32x16& p1, float& m_reg, float& mn, float& alpha) {
  constexpr float C = SCALE * 1.4426950408889634f;
  float pmax = p0[0]; for (int r = 1; r < 16; ++r) pmax = fmaxf(pmax, p0[r]); for (int r = 0; r < 16; ++r) pmax = fmaxf(pmax, p1[r]);
  { auto rr = __builtin_amdgcn_permlane32_swap(__float_as_uint(pmax), __float_as_uint(pmax), false, false);
    pmax = fmaxf(__uint_as_float(rr[0]), __uint_as_float(rr[1])); }
  if (__builtin_expect(__all(pmax - m_reg <= THR / SCALE), 1)) { mn = m_reg; alpha = 1.f; }
  else { mn = fmaxf(m_reg, pmax); alpha = __builtin_amdgcn_exp2f((m_reg - mn) * C); m_reg = mn; }
  float mnC = -mn * C;
  for (int r = 0; r < 16; ++r) p0[r] = fmaf(p0[r], C, mnC); for (int r = 0; r < 16; ++r) p1[r] = fmaf(p1[r], C, mnC);
  for (int r = 0; r < 16; ++r) p0[r] = __builtin_amdgcn_exp2f(p0[r]);
}
__device__ __forceinline__ void finishSM(f32x16& p0, f32x16& p1, float alpha, float& l_reg, bf16x8& pa0, bf16x8& pa1, bf16x8& pa2, bf16x8& pa3) {
  for (int r = 0; r < 16; ++r) p1[r] = __builtin_amdgcn_exp2f(p1[r]);
  float ps = 0; for (int r = 0; r < 16; ++r) ps += p0[r]; for (int r = 0; r < 16; ++r) ps += p1[r];
  { auto rr = __builtin_amdgcn_permlane32_swap(__float_as_uint(ps), __float_as_uint(ps), false, false);
    ps = __uint_as_float(rr[0]) + __uint_as_float(rr[1]); }
  l_reg = l_reg * alpha + ps;
#define PK4(P, BASE, OUT) do { unsigned a0 = cvtpk(P[BASE + 0], P[BASE + 1]), a1 = cvtpk(P[BASE + 2], P[BASE + 3]);   \
    unsigned b0 = cvtpk(P[BASE + 4], P[BASE + 5]), b1 = cvtpk(P[BASE + 6], P[BASE + 7]);                              \
    auto r0 = __builtin_amdgcn_permlane32_swap(a0, b0, false, false); auto r1 = __builtin_amdgcn_permlane32_swap(a1, b1, false, false); \
    u32x4 w = {r0[0], r1[0], r0[1], r1[1]}; OUT = *reinterpret_cast<bf16x8*>(&w); } while (0)
  PK4(p0, 0, pa0); PK4(p0, 8, pa1); PK4(p1, 0, pa2); PK4(p1, 8, pa3);
#undef PK4
}
__device__ __forceinline__ void qkt(f32x16& p0, f32x16& p1, const bf16* Ks, const bf16x8* qr, int r32, int hi) {
  p0 = f32x16{}; p1 = f32x16{};
  for (int d0 = 0; d0 < 8; ++d0) { int cb = (d0 * 16 + hi * 8) * 2;
    bf16x8 b0 = *reinterpret_cast<const bf16x8*>((const char*)Ks + KSWZ(r32, cb));
    bf16x8 b1 = *reinterpret_cast<const bf16x8*>((const char*)Ks + KSWZ(32 + r32, cb));
    p0 = __builtin_amdgcn_mfma_f32_32x32x16_bf16(b0, qr[d0], p0, 0, 0, 0);
    p1 = __builtin_amdgcn_mfma_f32_32x32x16_bf16(b1, qr[d0], p1, 0, 0, 0); }
}
__device__ __forceinline__ int v_st(int k, int c) { const int kk = (k & ~0xC) | ((k & 4) << 1) | ((k & 8) >> 1); return ((kk >> 3) * 4 + (c >> 5)) * 512 + ((kk & 7) * 32 + (c & 31)) * 2; }
__device__ __forceinline__ int v_rd_base(int lane) { return ((lane & 3) << 3) | (((lane >> 2) & 3) << 6) | (((lane >> 4) & 1) << 5) | (((lane >> 5) & 1) << 8); }
constexpr int v_rd_off(int d0, int ks, int half) { return d0 * 512 + ks * 4096 + half * 2048; }
template <int OFF> __device__ __forceinline__ s16x4 tr_read(int vb) {
  s16x4 r; asm volatile("ds_read_b64_tr_b16 %0, %1 offset:%2" : "=&v"(r) : "v"(vb), "i"(OFF) : "memory"); return r;
}
template <int D0> __device__ __forceinline__ void pv_one(f32x16& od, int vb, bf16x8 pa0, bf16x8 pa1, bf16x8 pa2, bf16x8 pa3) {
  const s16x4 l0 = tr_read<v_rd_off(D0, 0, 0)>(vb), h0 = tr_read<v_rd_off(D0, 0, 1)>(vb), l1 = tr_read<v_rd_off(D0, 1, 0)>(vb), h1 = tr_read<v_rd_off(D0, 1, 1)>(vb);
  const s16x4 l2 = tr_read<v_rd_off(D0, 2, 0)>(vb), h2 = tr_read<v_rd_off(D0, 2, 1)>(vb), l3 = tr_read<v_rd_off(D0, 3, 0)>(vb), h3 = tr_read<v_rd_off(D0, 3, 1)>(vb);
  asm volatile("s_waitcnt lgkmcnt(0)" ::: "memory"); SBAR();
#define PK(L, H) (bf16x8){L[0], L[1], L[2], L[3], H[0], H[1], H[2], H[3]}
  od = __builtin_amdgcn_mfma_f32_32x32x16_bf16(pa0, PK(l0, h0), od, 0, 0, 0);
  od = __builtin_amdgcn_mfma_f32_32x32x16_bf16(pa1, PK(l1, h1), od, 0, 0, 0);
  od = __builtin_amdgcn_mfma_f32_32x32x16_bf16(pa2, PK(l2, h2), od, 0, 0, 0);
  od = __builtin_amdgcn_mfma_f32_32x32x16_bf16(pa3, PK(l3, h3), od, 0, 0, 0);
#undef PK
}
__device__ __forceinline__ void pv_d0(f32x16* o, int vb, bf16x8 pa0, bf16x8 pa1, bf16x8 pa2, bf16x8 pa3) {
  pv_one<0>(o[0], vb, pa0, pa1, pa2, pa3); pv_one<1>(o[1], vb, pa0, pa1, pa2, pa3); pv_one<2>(o[2], vb, pa0, pa1, pa2, pa3); pv_one<3>(o[3], vb, pa0, pa1, pa2, pa3);
}
__device__ __forceinline__ bf16x8 ld8(const bf16* p) { return *reinterpret_cast<const bf16x8*>(p); }
__device__ __forceinline__ void attn_dense_body(const bf16* __restrict__ Qb, const bf16* __restrict__ Kh, const bf16* __restrict__ Vh,
                                                bf16* __restrict__ Ob, int seq, char* lds) {
  const int tid = threadIdx.x, wid = tid >> 6, lane = tid & 63, r32 = lane & 31, hi = lane >> 5;
  bf16* V_lds = (bf16*)lds; bf16* K_lds = (bf16*)(lds + 2 * SHM_V);
  float* ws = (float*)(lds + 2 * SHM_V + 2 * SHM_K) + wid * 64; float* li_l = ws; float* al_l = ws + 32;
  float m_reg = -1e30f, l_reg = 0; f32x16 o[4] = {}; bf16x8 qr[8];
  const bf16* Qw = Qb + (long)(wid * QBLK + r32) * LDQ + hi * 8;
#pragma unroll
  for (int d0 = 0; d0 < 8; ++d0) qr[d0] = ld8(Qw + d0 * 16);
  const int sr = tid >> 4, sc = (tid & 15) * 8, vst0 = v_st(sr, sc), vst1 = v_st(32 + sr, sc);
  const int vb0 = (int)(uintptr_t)V_lds + v_rd_base(lane);
  struct { bf16x8 vs0, vs1, ks0, ks1; } sr_[SDEPTH];
#define SLOAD(i, k0) do { sr_[i].vs0 = ld8(&Vh[(long)((k0) + sr) * LDK + sc]); sr_[i].vs1 = ld8(&Vh[(long)((k0) + 32 + sr) * LDK + sc]); \
    sr_[i].ks0 = ld8(&Kh[(long)((k0) + sr) * LDK + sc]); sr_[i].ks1 = ld8(&Kh[(long)((k0) + 32 + sr) * LDK + sc]); } while (0)
#define SWRITE(b, i) do { *(bf16x8*)((char*)V_lds + (b) * SHM_V + vst0) = (sr_[i].vs0);          \
    *(bf16x8*)((char*)V_lds + (b) * SHM_V + vst1) = (sr_[i].vs1); int kc = sc * 2;               \
    *(bf16x8*)((char*)K_lds + (b) * SHM_K + KSWZ(sr, kc)) = (sr_[i].ks0);                       \
    *(bf16x8*)((char*)K_lds + (b) * SHM_K + KSWZ(32 + sr, kc)) = (sr_[i].ks1); } while (0)
#define SWAIT() do { asm volatile("s_waitcnt vmcnt(4)" ::: "memory"); } while (0)
#define RESC(a) do { if (__any((a) < 1.f)) { if (hi == 0) al_l[r32] = (a); asm volatile("s_waitcnt lgkmcnt(0)" ::: "memory"); \
    for (int d = 0; d < 4; ++d) for (int r = 0; r < 16; ++r) o[d][r] *= al_l[crow(r, hi)]; } } while (0)
  f32x16 pA0, pA1, pB0, pB1; float mnA, mnB, alA, alB; bf16x8 pa0, pa1, pa2, pa3; const int NT = seq / KVBLK;
  constexpr int SE = 0, SO = SDEPTH - 1;
  SLOAD(SE, 0); asm volatile("s_waitcnt vmcnt(0)" ::: "memory"); SWRITE(0, SE); __syncthreads();
  qkt(pA0, pA1, K_lds, qr, r32, hi); partialSM(pA0, pA1, m_reg, mnA, alA);
  SLOAD(SO, KVBLK); if (2 < NT) SLOAD(SE, 2 * KVBLK);
  SWAIT(); SWRITE(1, SO); __syncthreads();
  for (int j = 1; j + 1 < NT; j += 2) {
    SBAR(); qkt(pB0, pB1, (bf16*)((char*)K_lds + SHM_K), qr, r32, hi);
    finishSM(pA0, pA1, alA, l_reg, pa0, pa1, pa2, pa3); SBAR();
    SLOAD(SO, (j + SDEPTH) * KVBLK); SBAR();
    pv_d0(o, vb0, pa0, pa1, pa2, pa3); partialSM(pB0, pB1, m_reg, mnB, alB);
    __syncthreads(); SWAIT(); SWRITE(0, SE);
    RESC(alB); __syncthreads();
    SBAR(); qkt(pA0, pA1, K_lds, qr, r32, hi);
    finishSM(pB0, pB1, alB, l_reg, pa0, pa1, pa2, pa3); SBAR();
    if (j + 3 < NT) SLOAD(SE, (j + 1 + SDEPTH) * KVBLK); SBAR();
    pv_d0(o, vb0 + (int)SHM_V, pa0, pa1, pa2, pa3); partialSM(pA0, pA1, m_reg, mnA, alA);
    __syncthreads(); SWAIT(); SWRITE(1, SO);
    RESC(alA); __syncthreads();
  }
  SBAR(); qkt(pB0, pB1, (bf16*)((char*)K_lds + SHM_K), qr, r32, hi);
  finishSM(pA0, pA1, alA, l_reg, pa0, pa1, pa2, pa3); SBAR();
  pv_d0(o, vb0, pa0, pa1, pa2, pa3); partialSM(pB0, pB1, m_reg, mnB, alB);
  __syncthreads(); RESC(alB);
  finishSM(pB0, pB1, alB, l_reg, pa0, pa1, pa2, pa3); SBAR();
  pv_d0(o, vb0 + (int)SHM_V, pa0, pa1, pa2, pa3);
  if (hi == 0) li_l[r32] = l_reg; asm volatile("s_waitcnt lgkmcnt(0)" ::: "memory");
  float rli[16];
#pragma unroll
  for (int r = 0; r < 16; ++r) rli[r] = __builtin_amdgcn_rcpf(li_l[crow(r, hi)]);
  bf16* Ow = Ob + (long)(wid * QBLK) * LDO;
#pragma unroll
  for (int r = 0; r < 16; ++r) { int orow = crow(r, hi);
    for (int d0 = 0; d0 < 4; ++d0) Ow[(long)orow * LDO + d0 * 32 + r32] = __float2bfloat16(o[d0][r] * rli[r]); }
}
__global__ __launch_bounds__(64 * NW, 1)
void attn_fwd(const bf16* __restrict__ Q, const bf16* __restrict__ KA, const bf16* __restrict__ VA, const bf16* __restrict__ KB, const bf16* __restrict__ VB, bf16* __restrict__ O) {
  extern __shared__ __attribute__((aligned(16))) char shm[];
  const int h = blockIdx.x, b = blockIdx.y, br = blockIdx.z;
  const bf16* K = br ? KB : KA; const bf16* V = br ? VB : VA;
  const long q0 = ((long)(br * 8 + b) * 256) * 4096 + h * 128, k0 = (long)(b * 32 + h) * 1024 * 128;
  attn_dense_body(Q + q0, K + k0, V + k0, O + q0, 1024, shm);
}
__global__ __launch_bounds__(256) void gemm_simple(const bf16* A0, const bf16* A1, int lda, const bf16* W0, const bf16* W1, int ldw, bf16* C0, bf16* C1, int ldc, int K) {
  __shared__ __attribute__((aligned(16))) short As[64][72];
  __shared__ __attribute__((aligned(16))) short Ws[64][72];
  const bf16* A = blockIdx.z ? A1 : A0; const bf16* W = blockIdx.z ? W1 : W0; bf16* C = blockIdx.z ? C1 : C0;
  int m0 = blockIdx.y * 64, n0 = blockIdx.x * 64;
  int tid = threadIdx.x, wid = tid >> 6, lane = tid & 63, r32 = lane & 31, hi = lane >> 5;
  int wm = (wid >> 1) * 32, wn = (wid & 1) * 32;
  f32x16 acc = {};
  for (int k0 = 0; k0 < K; k0 += 64) {
    for (int i = 0; i < 2; ++i) { int idx = tid + i * 256; int r = idx >> 3, c = (idx & 7) * 8;
      *(bf16x8*)&As[r][c] = ld8(A + (long)(m0 + r) * lda + k0 + c);
      bf16x8 w = ld8(W + (long)(k0 + r) * ldw + n0 + c);
      for (int j = 0; j < 8; ++j) Ws[c + j][r] = w[j]; }
    __syncthreads();
    for (int kk = 0; kk < 4; ++kk) {
      bf16x8 a = *(bf16x8*)&As[wm + r32][kk * 16 + hi * 8];
      bf16x8 b = *(bf16x8*)&Ws[wn + r32][kk * 16 + hi * 8];
      acc = __builtin_amdgcn_mfma_f32_32x32x16_bf16(a, b, acc, 0, 0, 0);
    }
    __syncthreads();
  }
  for (int r = 0; r < 16; ++r) C[(long)(m0 + wm + crow(r, hi)) * ldc + n0 + wn + r32] = __float2bfloat16(acc[r]);
}
extern "C" void kernel_launch(void* const* d_in, const int* in_sizes, int n_in, void* d_out, int out_size, void* d_ws, size_t ws_size, hipStream_t stream) {
  const bf16* xA = (const bf16*)d_in[0]; const bf16* kA = (const bf16*)d_in[1]; const bf16* vA = (const bf16*)d_in[2]; const bf16* wqA = (const bf16*)d_in[3]; const bf16* woA = (const bf16*)d_in[4];
  const bf16* xB = (const bf16*)d_in[5]; const bf16* kB = (const bf16*)d_in[6]; const bf16* vB = (const bf16*)d_in[7]; const bf16* wqB = (const bf16*)d_in[8]; const bf16* woB = (const bf16*)d_in[9];
  bf16* Q = (bf16*)d_ws; bf16* O = Q + (size_t)2 * 2048 * 4096; bf16* out = (bf16*)d_out;
  hipFuncSetAttribute((const void*)attn_fwd, hipFuncAttributeMaxDynamicSharedMemorySize, (int)SHM_ATTN);
  gemm_simple<<<dim3(4096 / 64, 2048 / 64, 2), 256, 0, stream>>>(xA, xB, 576, wqA, wqB, 4096, Q, Q + (size_t)2048 * 4096, 4096, 576);
  attn_fwd<<<dim3(32, 8, 2), 512, SHM_ATTN, stream>>>(Q, kA, vA, kB, vB, O);
  gemm_simple<<<dim3(576 / 64, 2048 / 64, 2), 256, 0, stream>>>(O, O + (size_t)2048 * 4096, 4096, woA, woB, 576, out, out + 576, 1152, 4096);
}
```

```cpp
#include <hip/hip_runtime.h>

#define FULL_CLOBBER "v0", "v1", "v2", "v3", "v4", "v5", "v6", "v7", "v8", "v9", "v10", "v11", "v12", "v13", "v14", "v15", "v16", "v17", "v18", "v19", "v20", "v21", "v22", "v23", "v24", "v25", "v26", "v27", "v28", "v29", "v30", "v31", "v32", "v33", "v34", "v35", "v36", "v37", "v38", "v39", "v40", "v41", "v42", "v43", "v44", "v45", "v46", "v47", "v48", "v49", "v50", "v51", "v52", "v53", "v54", "v55", "v56", "v57", "v58", "v59", "v60", "v61", "v62", "v63", "v64", "v65", "v66", "v67", "v68", "v69", "v70", "v71", "v72", "v73", "v74", "v75", "v76", "v77", "v78", "v79", "v80", "v81", "v82", "v83", "v84", "v85", "v86", "v87", "v88", "v89", "v90", "v91", "v92", "v93", "v94", "v95", "v96", "v97", "v98", "v99", "v100", "v101", "v102", "v103", "v104", "v105", "v106", "v107", "v108", "v109", "v110", "v111", "v112", "v113", "v114", "v115", "v116", "v117", "v118", "v119", "v120", "v121", "v122", "v123", "v124", "v125", "v126", "v127", "v128", "v129", "v130", "v131", "v132", "v133", "v134", "v135", "v136", "v137", "v138", "v139", "v140", "v141", "v142", "v143", "v144", "v145", "v146", "v147", "v148", "v149", "v150", "v151", "v152", "v153", "v154", "v155", "v156", "v157", "v158", "v159", "v160", "v161", "v162", "v163", "v164", "v165", "v166", "v167", "v168", "v169", "v170", "v171", "v172", "v173", "v174", "v175", "v176", "v177", "v178", "v179", "v180", "v181", "v182", "v183", "v184", "v185", "v186", "v187", "v188", "v189", "v190", "v191", "v192", "v193", "v194", "v195", "v196", "v197", "v198", "v199", "v200", "v201", "v202", "v203", "v204", "v205", "v206", "v207", "v208", "v209", "v210", "v211", "v212", "v213", "v214", "v215", "v216", "v217", "v218", "v219", "v220", "v221", "v222", "v223", "v224", "v225", "v226", "v227", "v228", "v229", "v230", "v231", "v232", "v233", "v234", "v235", "v236", "v237", "v238", "v239", "v240", "v241", "v242", "v243", "v244", "v245", "v246", "v247", "v248", "v249", "v250", "v251", "v252", "v253", "v254", "v255", "a0", "a1", "a2", "a3", "a4", "a5", "a6", "a7", "a8", "a9", "a10", "a11", "a12", "a13", "a14", "a15", "a16", "a17", "a18", "a19", "a20", "a21", "a22", "a23", "a24", "a25", "a26", "a27", "a28", "a29", "a30", "a31", "a32", "a33", "a34", "a35", "a36", "a37", "a38", "a39", "a40", "a41", "a42", "a43", "a44", "a45", "a46", "a47", "a48", "a49", "a50", "a51", "a52", "a53", "a54", "a55", "a56", "a57", "a58", "a59", "a60", "a61", "a62", "a63", "a64", "a65", "a66", "a67", "a68", "a69", "a70", "a71", "a72", "a73", "a74", "a75", "a76", "a77", "a78", "a79", "a80", "a81", "a82", "a83", "a84", "a85", "a86", "a87", "a88", "a89", "a90", "a91", "a92", "a93", "a94", "a95", "a96", "a97", "a98", "a99", "a100", "a101", "a102", "a103", "a104", "a105", "a106", "a107", "a108", "a109", "a110", "a111", "a112", "a113", "a114", "a115", "a116", "a117", "a118", "a119", "a120", "a121", "a122", "a123", "a124", "a125", "a126", "a127", "a128", "a129", "a130", "a131", "a132", "a133", "a134", "a135", "a136", "a137", "a138", "a139", "a140", "a141", "a142", "a143", "a144", "a145", "a146", "a147", "a148", "a149", "a150", "a151", "a152", "a153", "a154", "a155", "a156", "a157", "a158", "a159", "a160", "a161", "a162", "a163", "a164", "a165", "a166", "a167", "a168", "a169", "a170", "a171", "a172", "a173", "a174", "a175", "a176", "a177", "a178", "a179", "a180", "a181", "a182", "a183", "a184", "a185", "a186", "a187", "a188", "a189", "a190", "a191", "a192", "a193", "a194", "a195", "a196", "a197", "a198", "a199", "a200", "a201", "a202", "a203", "a204", "a205", "a206", "a207", "a208", "a209", "a210", "a211", "a212", "a213", "a214", "a215", "a216", "a217", "a218", "a219", "a220", "a221", "a222", "a223", "a224", "a225", "a226", "a227", "a228", "a229", "a230", "a231", "a232", "a233", "a234", "a235", "a236", "a237", "a238", "a239", "a240", "a241", "a242", "a243", "a244", "a245", "a246", "a247", "a248", "a249", "a250", "a251", "a252", "a253", "a254", "a255", "s4", "s5", "s6", "s7", "s8", "s9", "s10", "s11", "s12", "s13", "s14", "s15", "s16", "s17", "s18", "s19", "s20", "s21", "s22", "s23", "s24", "s25", "s26", "s27", "s28", "s29", "s30", "s31", "s33", "s34", "s35", "s36", "s37", "s38", "s39", "s40", "s41", "s42", "s43", "s44", "s45", "s46", "s47", "s48", "s49", "s50", "s51", "s52", "s53", "s54", "s55", "s56", "s57", "s58", "s59", "s60", "s61", "s62", "s63", "s64", "s65", "s66", "s67", "s68", "s69", "s70", "s71", "s72", "s73", "s74", "s75", "s76", "s77", "s78", "s79", "s80", "s81", "s82", "s83", "s84", "s85", "s86", "s87", "s88", "s89", "s90", "s91", "s92", "s93", "s94", "s95", "s96", "s97", "s98", "s99", "vcc", "scc", "memory"

__global__ __launch_bounds__(256, 1) __attribute__((aligned(4096))) void mha_fused(const char* xA, const char* kA, const char* vA, const char* wqA, const char* woA,
                                                    const char* xB, const char* kB, const char* vB, const char* wqB, const char* woB, char* outp, char* ws) {
  const unsigned bid = blockIdx.x, wave = __builtin_amdgcn_readfirstlane(threadIdx.x >> 6);
  const auto ka = __builtin_amdgcn_kernarg_segment_ptr();
  asm volatile(
".macro MA f,d,g,a,h,b\n"
"v_mfma_f32_16x16x32_bf16 \\f[\\d:\\d+3],\\g[\\a:\\a+3],\\h[\\b:\\b+3],\\f[\\d:\\d+3]\n"
".endm\n"
".macro MZ f,d,g,a,h,b\n"
"v_mfma_f32_16x16x32_bf16 \\f[\\d:\\d+3],\\g[\\a:\\a+3],\\h[\\b:\\b+3],0\n"
".endm\n"
".macro MC d,a,b,c\n"
"v_mfma_f32_16x16x32_bf16 v[\\d:\\d+3],a[\\a:\\a+3],a[\\b:\\b+3],v[\\c:\\c+3]\n"
".endm\n"
".macro TR d,a,o\n"
"ds_read_b64_tr_b16 a[\\d:\\d+1],v\\a offset:\\o\n"
".endm\n"
".macro RD d,a,o\n"
"ds_read_b128 a[\\d:\\d+3],v\\a offset:\\o\n"
".endm\n"
".macro EX r\n"
"v_exp_f32 v\\r,v\\r\n"
".endm\n"
".macro AD l,r\n"
"v_add_f32 v\\l,v\\l,v\\r\n"
".endm\n"
".macro CV d,a,b\n"
"v_cvt_pk_bf16_f32 v\\d,v\\a,v\\b\n"
".endm\n"
".macro LG d,s\n"
"buffer_load_dwordx4 a[\\d:\\d+3],v255,s[\\s:\\s+3],s80 offen sc1\n"
".endm\n"
"s_bfe_u32 s80, %[bid], 0x10002\n"
"s_mul_i32 s80, s80, 40\n"
"s_load_dwordx8 s[4:11], %[ka], s80\n"
"s_add_u32 s79, s80, 32\n"
"s_load_dwordx2 s[12:13], %[ka], s79\n"
"s_load_dwordx4 s[16:19], %[ka], 80\n"
"s_mov_b32 s64, %[wave]\n"
"s_mov_b32 s74, 0\n"
"s_mov_b32 s75, 3\n"
"s_bfe_u32 s21, %[bid], 0x10001\n"
"s_lshr_b32 s82, %[bid], 6\n"
"s_lshl2_add_u32 s21, s21, s82\n"
"s_and_b32 s82, %[bid], 1\n"
"s_bfe_u32 s22, %[bid], 0x30003\n"
"s_lshl3_add_u32 s22, s82, s22\n"
"s_mul_i32 s82, s21, 294912\n"
"s_lshl_b32 s83, s22, 9\n"
"v_mbcnt_lo_u32_b32 v246, -1, 0\n"
"v_mbcnt_hi_u32_b32 v246, -1, v246\n"
"s_lshl_b32 s65, s64, 11\n"
"s_lshl_b32 s81, s64, 17\n"
"v_lshrrev_b32 v247, 4, v246\n"
"v_lshlrev_b32 v248, 2, v247\n"
"v_and_b32 v242, 15, v246\n"
"v_xor_b32 v242, v242, v248\n"
"v_lshlrev_b32 v242, 4, v242\n"
"v_lshl_add_u32 v242, v247, 13, v242\n"
"v_add_u32 v242, s81, v242\n"
"s_mov_b32 s66, 0\n"
"s_mul_i32 s81, s64, 73728\n"
"v_and_b32 v243, 7, v246\n"
"v_xor_b32 v243, v243, v247\n"
"v_lshlrev_b32 v243, 4, v243\n"
"v_xor_b32 v244, 64, v243\n"
"v_lshrrev_b32 v247, 3, v246\n"
"v_mul_u32_u24 v248, 1152, v247\n"
"v_add3_u32 v243, v243, v248, s81\n"
"v_add3_u32 v244, v244, v248, s81\n"
"s_lshl_b32 s67, s64, 13\n"
"s_add_u32 s67, s67, 65664\n"
"s_mov_b32 s68, 0\n"
"v_lshlrev_b32 v255, 4, v246\n"
"s_lshl_b32 s78, s64, 10\n"
"s_waitcnt lgkmcnt(0)\n"
"s_add_u32 s10, s10, s83\n"
"s_addc_u32 s11, s11, 0\n"
"s_add_u32 s4, s4, s82\n"
"s_addc_u32 s5, s5, 0\n"
"s_load_dwordx2 s[28:29], s[18:19], 49152\n"
"L_START2_%=:\n"
"s_mov_b64 s[36:37], s[10:11]\n"
"s_and_b32 s37, s37, 0xffff\n"
"s_mov_b32 s38, 4710912\n"
"s_mov_b32 s39, 0x20000\n"
"s_add_u32 m0, s65, 32800\n"
"s_nop 0\n"
"buffer_load_dwordx4 v242, s[36:39], s66 offen lds\n"
"s_add_u32 s66, s66, 32768\n"
"s_add_u32 m0, s65, 33824\n"
"s_nop 0\n"
"buffer_load_dwordx4 v242, s[36:39], s66 offen lds\n"
"s_add_u32 s66, s66, 32768\n"
"s_add_u32 m0, s65, 41000\n"
"s_nop 0\n"
"buffer_load_dwordx4 v242, s[36:39], s66 offen lds\n"
"s_add_u32 s66, s66, 32768\n"
"s_add_u32 m0, s65, 42024\n"
"s_nop 0\n"
"buffer_load_dwordx4 v242, s[36:39], s66 offen lds\n"
"s_sub_u32 s66, s66, 98048\n"
"s_mov_b64 s[40:41], s[4:5]\n"
"s_and_b32 s41, s41, 0xffff\n"
"s_mov_b32 s42, 294912\n"
"s_mov_b32 s43, 0x20000\n"
"s_add_u32 m0, s67, 0\n"
"s_nop 0\n"
"buffer_load_dwordx4 v243, s[40:43], s68 offen lds\n"
"s_add_u32 s68, s68, 9216\n"
"s_add_u32 m0, s67, 1024\n"
"s_nop 0\n"
"buffer_load_dwordx4 v244, s[40:43], s68 offen lds\n"
"s_add_u32 s68, s68, 9216\n"
"s_add_u32 m0, s67, 2048\n"
"s_nop 0\n"
"buffer_load_dwordx4 v243, s[40:43], s68 offen lds\n"
"s_add_u32 s68, s68, 9216\n"
"s_add_u32 m0, s67, 3072\n"
"s_nop 0\n"
"buffer_load_dwordx4 v244, s[40:43], s68 offen lds\n"
"s_add_u32 s68, s68, 9216\n"
"s_add_u32 m0, s67, 4096\n"
"s_nop 0\n"
"buffer_load_dwordx4 v243, s[40:43], s68 offen lds\n"
"s_add_u32 s68, s68, 9216\n"
"s_add_u32 m0, s67, 5120\n"
"s_nop 0\n"
"buffer_load_dwordx4 v244, s[40:43], s68 offen lds\n"
"s_add_u32 s68, s68, 9216\n"
"s_add_u32 m0, s67, 6144\n"
"s_nop 0\n"
"buffer_load_dwordx4 v243, s[40:43], s68 offen lds\n"
"s_add_u32 s68, s68, 9216\n"
"s_add_u32 m0, s67, 7168\n"
"s_nop 0\n"
"buffer_load_dwordx4 v244, s[40:43], s68 offen lds\n"
"s_sub_u32 s68, s68, 64384\n"
"v_and_b32 v247, 15, v246\n"
"v_lshrrev_b32 v248, 4, v246\n"
"v_lshrrev_b32 v249, 1, v247\n"
"v_xor_b32 v249, v249, v248\n"
"v_lshlrev_b32 v249, 4, v249\n"
"v_and_b32 v250, 7, v247\n"
"v_lshl_add_u32 v249, v250, 7, v249\n"
"v_lshrrev_b32 v251, 3, v247\n"
"v_lshl_add_u32 v240, v251, 10, v249\n"
"v_add_u32 v240, s67, v240\n"
"v_xor_b32 v241, 64, v240\n"
"v_lshl_add_u32 v249, v251, 11, v249\n"
"v_xor_b32 v250, 64, v249\n"
"v_bfe_u32 v247, v246, 2, 2\n"
"v_lshrrev_b32 v236, 1, v248\n"
"v_lshl_add_u32 v236, v236, 3, v247\n"
"v_lshlrev_b32 v236, 8, v236\n"
"v_and_b32 v245, 1, v248\n"
"v_mul_u32_u24 v245, 8200, v245\n"
"v_add_u32 v236, v236, v245\n"
"v_lshl_add_u32 v236, v247, 6, v236\n"
"v_and_b32 v245, 3, v246\n"
"v_lshl_add_u32 v236, v245, 4, v236\n"
"v_xor_b32 v237, 64, v236\n"
"v_xor_b32 v238, 128, v236\n"
"v_xor_b32 v239, 192, v236\n"
"s_add_u32 m0, s65, 49200\n"
"s_nop 0\n"
"buffer_load_dwordx4 v242, s[36:39], s66 offen lds\n"
"s_add_u32 s66, s66, 32768\n"
"s_add_u32 m0, s65, 50224\n"
"s_nop 0\n"
"buffer_load_dwordx4 v242, s[36:39], s66 offen lds\n"
"s_add_u32 s66, s66, 32768\n"
"s_add_u32 m0, s65, 57400\n"
"s_nop 0\n"
"buffer_load_dwordx4 v242, s[36:39], s66 offen lds\n"
"s_add_u32 s66, s66, 32768\n"
"s_add_u32 m0, s65, 58424\n"
"s_nop 0\n"
"buffer_load_dwordx4 v242, s[36:39], s66 offen lds\n"
"s_add_u32 s66, s66, 425728\n"
"s_getpc_b64 s[34:35]\n"
"L_PC_%=:\n"
"s_and_b32 s34, s34, 0xffffff80\n"
"s_lshr_b32 s79, %[bid], 3\n"
"s_lshl2_add_u32 s79, s79, s64\n"
"s_lshl_b32 s79, s79, 10\n"
"s_min_u32 s79, s79, (L_END_%=-L_PC_%=-1024)&0xffffff80\n"
"s_add_u32 s34, s34, s79\n"
"s_addc_u32 s35, s35, 0\n"
"global_load_dword a228, v255, s[34:35]\n"
"s_waitcnt vmcnt(5)\n"
"s_barrier\n"
"RD 64,240,0\n"
"RD 68,240,2048\n"
"RD 72,240,4096\n"
"RD 76,240,6144\n"
"RD 80,241,0\n"
"RD 84,241,2048\n"
"RD 88,241,4096\n"
"RD 92,241,6144\n"
"TR 128,236,32800\n"
"TR 130,236,33824\n"
"TR 132,236,36896\n"
"TR 134,236,37920\n"
"TR 136,236,32808\n"
"TR 138,236,33832\n"
"s_waitcnt lgkmcnt(4)\n"
"MZ v,128,a,128,a,64\n"
"s_waitcnt lgkmcnt(2)\n"
"s_bfe_u32 s24, %[bid], 0x10002\n"
"s_and_b32 s27, %[bid], 1\n"
"TR 140,236,36904\n"
"MA v,128,a,132,a,80\n"
"s_lshl4_add_u32 s25, s21, s22\n"
"s_lshl3_add_u32 s26, s24, s21\n"
"TR 142,236,37928\n"
"MZ v,132,a,132,a,84\n"
"s_add_u32 m0, s65, 0\n"
"s_bfe_u32 s82, %[bid], 0x10005\n"
"s_lshl1_add_u32 s82, s82, s27\n"
"MA v,132,a,128,a,68\n"
"s_bfe_u32 s83, %[bid], 0x20003\n"
"s_lshl_b32 s70, s25, 19\n"
"buffer_load_dwordx4 v242, s[36:39], s66 offen lds\n"
"MZ v,136,a,128,a,72\n"
"s_add_u32 s66, s66, 32768\n"
"s_lshl2_add_u32 s80, s24, s64\n"
"TR 144,237,32800\n"
"MA v,136,a,132,a,88\n"
"s_lshl_b32 s80, s80, 7\n"
"s_add_u32 s80, s80, s25\n"
"TR 146,237,33824\n"
"MZ v,140,a,132,a,92\n"
"s_add_u32 m0, s65, 1024\n"
"s_lshl_b32 s30, s80, 3\n"
"s_lshl2_add_u32 s80, s26, s64\n"
"MA v,140,a,128,a,76\n"
"s_waitcnt lgkmcnt(2)\n"
"s_lshl4_add_u32 s80, s80, s22\n"
"buffer_load_dwordx4 v242, s[36:39], s66 offen lds\n"
"MZ v,156,a,136,a,76\n"
"s_add_u32 s66, s66, 32768\n"
"s_lshl4_add_u32 s31, s80, 8192\n"
"TR 148,237,36896\n"
"MA v,156,a,140,a,92\n"
"s_lshl2_add_u32 s81, s27, s64\n"
"s_lshl2_add_u32 s80, s24, s83\n"
"TR 150,237,37920\n"
"MZ v,152,a,140,a,88\n"
"s_lshl3_add_u32 s80, s80, s81\n"
"s_lshl_b32 s20, s80, 7\n"
"TR 152,237,32808\n"
"MA v,152,a,136,a,72\n"
"s_add_u32 m0, s65, 8200\n"
"s_lshl2_add_u32 s80, s26, s82\n"
"s_lshl3_add_u32 s80, s80, s81\n"
"MZ v,148,a,136,a,68\n"
"s_lshl_b32 s80, s80, 21\n"
"s_add_u32 s80, s80, 536870912\n"
"buffer_load_dwordx4 v242, s[36:39], s66 offen lds\n"
"MA v,148,a,140,a,84\n"
"s_add_u32 s66, s66, 32768\n"
"s_or_b32 s20, s20, s80\n"
"TR 154,237,33832\n"
"MZ v,144,a,140,a,80\n"
"s_lshl2_add_u32 s14, %[bid], s64\n"
"s_mul_i32 s71, %[bid], 40960\n"
"TR 156,237,36904\n"
"MA v,144,a,136,a,64\n"
"s_waitcnt lgkmcnt(3)\n"
"s_and_b32 s71, s71, 0x3e000\n"
"s_mul_i32 s15, %[bid], 0\n"
"MZ v,160,a,144,a,64\n"
"s_add_u32 m0, s65, 9224\n"
"s_and_b32 s15, s15, 0x3c000\n"
"TR 158,237,37928\n"
"MA v,160,a,148,a,80\n"
"s_or_b32 s15, s15, 0x2000\n"
"s_lshl2_add_u32 s80, s21, s82\n"
"buffer_load_dwordx4 v242, s[36:39], s66 offen lds\n"
"MZ v,164,a,148,a,84\n"
"s_sub_u32 s66, s66, 98048\n"
"s_lshl2_add_u32 s80, s80, s64\n"
"TR 160,238,32800\n"
"MA v,164,a,144,a,68\n"
"s_mul_i32 s80, s80, 36864\n"
"s_lshl2_add_u32 s81, s24, s83\n"
"TR 162,238,33824\n"
"MZ v,168,a,144,a,72\n"
"s_mul_i32 s81, s81, 288\n"
"s_add_u32 s23, s80, s81\n"
"s_mul_i32 s80, s25, 36864\n"
"MA v,168,a,148,a,88\n"
"s_add_u32 m0, s67, 32768\n"
"s_mov_b32 s81, s70\n"
"TR 164,238,36896\n"
"MZ v,172,a,148,a,92\n"
"s_add_u32 s52, s12, s80\n"
"s_addc_u32 s53, s13, 0\n"
"buffer_load_dwordx4 v243, s[40:43], s68 offen lds\n"
"MA v,172,a,144,a,76\n"
"s_waitcnt lgkmcnt(3)\n"
"s_add_u32 s68, s68, 9216\n"
"TR 166,238,37920\n"
"MZ v,188,a,152,a,76\n"
"s_and_b32 s53, s53, 0xffff\n"
"s_mov_b32 s54, 36864\n"
"s_mov_b32 s55, 0x20000\n"
"MA v,188,a,156,a,92\n"
"s_add_u32 s44, s6, s81\n"
"s_addc_u32 s45, s7, 0\n"
"TR 168,238,32808\n"
"MZ v,184,a,156,a,88\n"
"s_add_u32 m0, s67, 33792\n"
"s_and_b32 s45, s45, 0xffff\n"
"s_mov_b32 s46, 524288\n"
"MA v,184,a,152,a,72\n"
"s_mov_b32 s47, 0x20000\n"
"buffer_load_dwordx4 v244, s[40:43], s68 offen lds\n"
"MZ v,180,a,152,a,68\n"
"s_add_u32 s68, s68, 9216\n"
"TR 170,238,33832\n"
"MA v,180,a,156,a,84\n"
"s_add_u32 s48, s8, s81\n"
"s_addc_u32 s49, s9, 0\n"
"TR 172,238,36904\n"
"MZ v,176,a,156,a,80\n"
"s_and_b32 s49, s49, 0xffff\n"
"s_mov_b32 s50, 524288\n"
"TR 174,238,37928\n"
"MA v,176,a,152,a,64\n"
"s_waitcnt lgkmcnt(4)\n"
"s_add_u32 m0, s67, 34816\n"
"s_mov_b32 s51, 0x20000\n"
"MZ v,192,a,160,a,64\n"
"s_lshl_b32 s80, s31, 11\n"
"s_sub_u32 s80, s80, 15728640\n"
"buffer_load_dwordx4 v243, s[40:43], s68 offen lds\n"
"MA v,192,a,164,a,80\n"
"s_add_u32 s68, s68, 9216\n"
"s_mul_i32 s81, s30, 1152\n"
"TR 176,239,32800\n"
"MZ v,196,a,164,a,84\n"
"s_add_u32 s81, s81, 34603008\n"
"TR 178,239,33824\n"
"MA v,196,a,160,a,68\n"
"s_add_u32 m0, s67, 35840\n"
"s_add_u32 s56, s18, s80\n"
"s_addc_u32 s57, s19, 0\n"
"MZ v,200,a,160,a,72\n"
"s_and_b32 s57, s57, 0xffff\n"
"s_mov_b32 s58, 0\n"
"buffer_load_dwordx4 v244, s[40:43], s68 offen lds\n"
"MA v,200,a,164,a,88\n"
"s_add_u32 s68, s68, 9216\n"
"s_mov_b32 s59, 0x20000\n"
"TR 180,239,36896\n"
"MZ v,204,a,164,a,92\n"
"s_add_u32 s60, s18, s81\n"
"s_addc_u32 s61, s19, 0\n"
"TR 182,239,37920\n"
"MA v,204,a,160,a,76\n"
"s_waitcnt lgkmcnt(4)\n"
"s_and_b32 s61, s61, 0xffff\n"
"TR 184,239,32808\n"
"MZ v,220,a,168,a,76\n"
"s_add_u32 m0, s67, 36864\n"
"s_mov_b32 s62, 9216\n"
"s_mov_b32 s63, 0x20000\n"
"MA v,220,a,172,a,92\n"
"buffer_load_dwordx4 v243, s[40:43], s68 offen lds\n"
"v_and_b32 v252, 1, v248\n"
"MZ v,216,a,172,a,88\n"
"s_add_u32 s68, s68, 9216\n"
"TR 186,239,33832\n"
"v_lshl_add_u32 v251, v252, 2, v247\n"
"MA v,216,a,168,a,72\n"
"TR 188,239,36904\n"
"v_lshlrev_b32 v251, 6, v251\n"
"MZ v,212,a,168,a,68\n"
"v_lshl_add_u32 v251, v252, 5, v251\n"
"v_lshl_add_u32 v251, v245, 3, v251\n"
"MA v,212,a,172,a,84\n"
"s_add_u32 m0, s67, 37888\n"
"TR 190,239,37928\n"
"v_lshrrev_b32 v252, 1, v248\n"
"MZ v,208,a,172,a,80\n"
"buffer_load_dwordx4 v244, s[40:43], s68 offen lds\n"
"v_lshl_add_u32 v251, v252, 11, v251\n"
"MA v,208,a,168,a,64\n"
"s_waitcnt lgkmcnt(4)\n"
"s_add_u32 s68, s68, 9216\n"
"v_add_u32 v251, 65536, v251\n"
"MZ v,224,a,176,a,64\n"
"v_xor_b32 v252, 32, v251\n"
"MA v,224,a,180,a,80\n"
"s_mul_i32 s81, s64, 288\n"
"s_add_u32 s81, s81, 131200\n"
"v_lshl_add_u32 v248, v248, 2, v247\n"
"MZ v,228,a,180,a,84\n"
"s_add_u32 m0, s67, 38912\n"
"s_waitcnt lgkmcnt(0)\n"
"v_mul_u32_u24 v248, 1152, v248\n"
"MA v,228,a,176,a,68\n"
"s_waitcnt vmcnt(11)\n"
"buffer_load_dwordx4 v243, s[40:43], s68 offen lds\n"
"v_lshlrev_b32 v245, 3, v245\n"
"MZ v,232,a,176,a,72\n"
"s_add_u32 s68, s68, 9216\n"
"s_barrier\n"
"v_add3_u32 v245, v245, v248, s81\n"
"MA v,232,a,180,a,88\n"
"s_lshl_b32 s69, s64, 11\n"
"s_and_b32 s80, s64, 1\n"
"TR 128,236,49200\n"
"MZ a,236,a,180,a,92\n"
"s_lshl_b32 s80, s80, 2\n"
"TR 130,236,50224\n"
"v_lshrrev_b32 v248, 4, v246\n"
"MA a,236,a,176,a,76\n"
"s_add_u32 m0, s67, 39936\n"
"TR 132,236,53296\n"
"v_or_b32 v248, s80, v248\n"
"MZ a,252,a,184,a,76\n"
"buffer_load_dwordx4 v244, s[40:43], s68 offen lds\n"
"v_and_b32 v253, 7, v246\n"
"MA a,252,a,188,a,92\n"
"s_sub_u32 s68, s68, 64384\n"
"TR 134,236,54320\n"
"v_xor_b32 v253, v253, v248\n"
"MZ a,248,a,188,a,88\n"
"v_lshlrev_b32 v253, 4, v253\n"
"v_lshrrev_b32 v247, 3, v246\n"
"MA a,248,a,184,a,72\n"
"TR 136,236,49208\n"
"v_lshl_add_u32 v253, v247, 8, v253\n"
"MZ a,244,a,184,a,68\n"
"TR 138,236,50232\n"
"v_add_u32 v253, s69, v253\n"
"MA a,244,a,188,a,84\n"
"v_bfe_u32 v247, v246, 2, 3\n"
"v_bfe_u32 v248, v246, 4, 1\n"
"MZ a,240,a,188,a,80\n"
"v_lshlrev_b32 v248, 1, v248\n"
"v_and_b32 v254, 3, v246\n"
"MA a,240,a,184,a,64\n"
"s_waitcnt lgkmcnt(2)\n"
"s_mov_b64 exec, 0\n"
"s_cmp_eq_u32 s64, 0\n"
"s_cbranch_scc1 L_ST1_%=\n"
"s_cmp_eq_u32 s64, 1\n"
"s_cbranch_scc1 L_ST2_%=\n"
"s_cmp_eq_u32 s64, 2\n"
"s_cbranch_scc1 L_ST3_%=\n"
"s_cmp_eq_u32 s64, 3\n"
"s_cbranch_scc1 L_ST4_%=\n"
"L_VB0_%=:\n"
"s_mov_b64 exec, -1\n"
"MZ v,0,a,128,a,64\n"
"TR 140,236,53304\n"
"v_xor_b32 v254, v254, v248\n"
"MA v,0,a,132,a,80\n"
"TR 142,236,54328\n"
"v_lshlrev_b32 v254, 4, v254\n"
"MZ v,4,a,132,a,84\n"
"s_add_u32 m0, s65, 16400\n"
"TR 144,237,49200\n"
"v_lshl_add_u32 v254, v247, 8, v254\n"
"MA v,4,a,128,a,68\n"
"buffer_load_dwordx4 v242, s[36:39], s66 offen lds\n"
"v_lshrrev_b32 v248, 5, v246\n"
"MZ v,8,a,128,a,72\n"
"s_add_u32 s66, s66, 32768\n"
"TR 146,237,50224\n"
"v_lshl_add_u32 v254, v248, 6, v254\n"
"MA v,8,a,132,a,88\n"
"TR 148,237,53296\n"
"v_add_u32 v254, s69, v254\n"
"MZ v,12,a,132,a,92\n"
"s_add_u32 m0, s65, 17424\n"
"s_mov_b32 s76, 0x3e0293ee\n"
"TR 150,237,54320\n"
"MA v,12,a,128,a,76\n"
"s_waitcnt lgkmcnt(4)\n"
"s_mov_b32 s77, 0x3e0293ee\n"
"buffer_load_dwordx4 v242, s[36:39], s66 offen lds\n"
"MZ v,28,a,136,a,76\n"
"s_add_u32 s66, s66, 32768\n"
"s_mov_b32 s70, 0\n"
"TR 152,237,49208\n"
"MA v,28,a,140,a,92\n"
"s_mov_b32 s84, 0x4e800000\n"
"s_mov_b32 s72, 2\n"
"TR 154,237,50232\n"
"MZ v,24,a,140,a,88\n"
"s_add_u32 s24, s70, s71\n"
"s_and_b32 s24, s24, 0x3ffff\n"
"TR 156,237,53304\n"
"MA v,24,a,136,a,72\n"
"s_add_u32 m0, s65, 24600\n"
"s_and_b32 s81, s70, 0xfffc0000\n"
"TR 158,237,54328\n"
"MZ v,20,a,136,a,68\n"
"s_or_b32 s24, s24, s81\n"
"s_add_u32 s70, s70, 8192\n"
"buffer_load_dwordx4 v242, s[36:39], s66 offen lds\n"
"MA v,20,a,140,a,84\n"
"s_add_u32 s66, s66, 32768\n"
"s_add_u32 s25, s70, s71\n"
"TR 160,238,49200\n"
"MZ v,16,a,140,a,80\n"
"s_and_b32 s25, s25, 0x3ffff\n"
"s_and_b32 s81, s70, 0xfffc0000\n"
"TR 162,238,50224\n"
"MA v,16,a,136,a,64\n"
"s_waitcnt lgkmcnt(6)\n"
"s_or_b32 s25, s25, s81\n"
"TR 164,238,53296\n"
"MZ v,32,a,144,a,64\n"
"s_add_u32 m0, s65, 25624\n"
"s_add_u32 s70, s70, 8192\n"
"TR 166,238,54320\n"
"MA v,32,a,148,a,80\n"
"s_add_u32 s26, s70, s71\n"
"s_and_b32 s26, s26, 0x3ffff\n"
"buffer_load_dwordx4 v242, s[36:39], s66 offen lds\n"
"MZ v,36,a,148,a,84\n"
"s_add_u32 s66, s66, 425728\n"
"s_and_b32 s81, s70, 0xfffc0000\n"
"TR 168,238,49208\n"
"MA v,36,a,144,a,68\n"
"s_or_b32 s26, s26, s81\n"
"s_add_u32 s70, s70, 8192\n"
"TR 170,238,50232\n"
"MZ v,40,a,144,a,72\n"
"s_add_u32 s27, s70, s71\n"
"s_and_b32 s27, s27, 0x3ffff\n"
"TR 172,238,53304\n"
"MA v,40,a,148,a,88\n"
"s_add_u32 m0, s67, 0\n"
"s_and_b32 s81, s70, 0xfffc0000\n"
"TR 174,238,54328\n"
"MZ v,44,a,148,a,92\n"
"s_or_b32 s27, s27, s81\n"
"s_add_u32 s70, s70, 8192\n"
"buffer_load_dwordx4 v243, s[40:43], s68 offen lds\n"
"MA v,44,a,144,a,76\n"
"s_waitcnt lgkmcnt(8)\n"
"s_add_u32 s68, s68, 9216\n"
"TR 176,239,49200\n"
"MZ v,60,a,152,a,76\n"
"TR 178,239,50224\n"
"MA v,60,a,156,a,92\n"
"TR 180,239,53296\n"
"MZ v,56,a,156,a,88\n"
"s_add_u32 m0, s67, 1024\n"
"TR 182,239,54320\n"
"MA v,56,a,152,a,72\n"
"buffer_load_dwordx4 v244, s[40:43], s68 offen lds\n"
"MZ v,52,a,152,a,68\n"
"s_add_u32 s68, s68, 9216\n"
"TR 184,239,49208\n"
"MA v,52,a,156,a,84\n"
"TR 186,239,50232\n"
"MZ v,48,a,156,a,80\n"
"TR 188,239,53304\n"
"MA v,48,a,152,a,64\n"
"s_waitcnt lgkmcnt(11)\n"
"s_add_u32 m0, s67, 2048\n"
"MZ v,64,a,160,a,64\n"
"buffer_load_dwordx4 v243, s[40:43], s68 offen lds\n"
"MA v,64,a,164,a,80\n"
"s_add_u32 s68, s68, 9216\n"
"TR 190,239,54328\n"
"MZ v,68,a,164,a,84\n"
"s_cmp_eq_u32 s74, 0\n"
"s_cselect_b32 s81, 0x7f4a7c15, 0\n"
"s_cselect_b32 s80, 0x9e3779b9, 0\n"
"MA v,68,a,160,a,68\n"
"s_add_u32 m0, s67, 3072\n"
"s_add_u32 s28, s28, s81\n"
"s_addc_u32 s29, s29, s80\n"
"MZ v,72,a,160,a,72\n"
"buffer_load_dwordx4 v244, s[40:43], s68 offen lds\n"
"MA v,72,a,164,a,88\n"
"s_add_u32 s68, s68, 9216\n"
"s_waitcnt vmcnt(8)\n"
"RD 96,240,32768\n"
"MZ v,76,a,164,a,92\n"
"RD 100,240,34816\n"
"MA v,76,a,160,a,76\n"
"s_waitcnt lgkmcnt(10)\n"
"RD 104,240,36864\n"
"MZ v,92,a,168,a,76\n"
"s_add_u32 m0, s65, 32800\n"
"RD 108,240,38912\n"
"MA v,92,a,172,a,92\n"
"buffer_load_dwordx4 v242, s[36:39], s66 offen lds\n"
"MZ v,88,a,172,a,88\n"
"s_add_u32 s66, s66, 32768\n"
"RD 112,241,32768\n"
"MA v,88,a,168,a,72\n"
"RD 116,241,34816\n"
"MZ v,84,a,168,a,68\n"
"RD 120,241,36864\n"
"MA v,84,a,172,a,84\n"
"s_add_u32 m0, s65, 33824\n"
"RD 124,241,38912\n"
"MZ v,80,a,172,a,80\n"
"buffer_load_dwordx4 v242, s[36:39], s66 offen lds\n"
"MA v,80,a,168,a,64\n"
"s_waitcnt lgkmcnt(12)\n"
"s_add_u32 s66, s66, 32768\n"
"MZ v,96,a,176,a,64\n"
"MA v,96,a,180,a,80\n"
"MZ v,100,a,180,a,84\n"
"s_add_u32 m0, s65, 41000\n"
"s_waitcnt lgkmcnt(0)\n"
"MA v,100,a,176,a,68\n"
"s_waitcnt vmcnt(63)\n"
"buffer_load_dwordx4 v242, s[36:39], s66 offen lds\n"
"MZ v,104,a,176,a,72\n"
"s_add_u32 s66, s66, 32768\n"
"s_barrier\n"
"MA v,104,a,180,a,88\n"
"TR 128,236,0\n"
"MZ v,108,a,180,a,92\n"
"TR 130,236,1024\n"
"MA v,108,a,176,a,76\n"
"s_add_u32 m0, s65, 42024\n"
"TR 132,236,4096\n"
"MZ v,124,a,184,a,76\n"
"buffer_load_dwordx4 v242, s[36:39], s66 offen lds\n"
"MA v,124,a,188,a,92\n"
"s_sub_u32 s66, s66, 98048\n"
"TR 134,236,5120\n"
"MZ v,120,a,188,a,88\n"
"L_ST1_%=:\n"
"s_cbranch_execz L_VB0_%=\n"
"MA v,120,a,184,a,72\n"
"TR 136,236,8\n"
"MZ v,116,a,184,a,68\n"
"TR 138,236,1032\n"
"MA v,116,a,188,a,84\n"
"MZ v,112,a,188,a,80\n"
"MA v,112,a,184,a,64\n"
"s_waitcnt lgkmcnt(2)\n"
"MA v,128,a,128,a,96\n"
"TR 140,236,4104\n"
"MA v,128,a,132,a,112\n"
"TR 142,236,5128\n"
"MA v,132,a,132,a,116\n"
"s_add_u32 m0, s67, 4096\n"
"MA v,132,a,128,a,100\n"
"buffer_load_dwordx4 v243, s[40:43], s68 offen lds\n"
"MA v,136,a,128,a,104\n"
"s_add_u32 s68, s68, 9216\n"
"TR 144,237,0\n"
"MA v,136,a,132,a,120\n"
"TR 146,237,1024\n"
"MA v,140,a,132,a,124\n"
"s_add_u32 m0, s67, 5120\n"
"MA v,140,a,128,a,108\n"
"s_waitcnt lgkmcnt(2)\n"
"buffer_load_dwordx4 v244, s[40:43], s68 offen lds\n"
"MA v,156,a,136,a,108\n"
"s_add_u32 s68, s68, 9216\n"
"TR 148,237,4096\n"
"MA v,156,a,140,a,124\n"
"TR 150,237,5120\n"
"MA v,152,a,140,a,120\n"
"s_add_u32 m0, s67, 6144\n"
"TR 152,237,8\n"
"MA v,152,a,136,a,104\n"
"buffer_load_dwordx4 v243, s[40:43], s68 offen lds\n"
"MA v,148,a,136,a,100\n"
"s_add_u32 s68, s68, 9216\n"
"TR 154,237,1032\n"
"MA v,148,a,140,a,116\n"
"MA v,144,a,140,a,112\n"
"s_add_u32 m0, s67, 7168\n"
"TR 156,237,4104\n"
"MA v,144,a,136,a,96\n"
"s_waitcnt lgkmcnt(3)\n"
"buffer_load_dwordx4 v244, s[40:43], s68 offen lds\n"
"MA v,160,a,144,a,96\n"
"s_sub_u32 s68, s68, 64384\n"
"TR 158,237,5128\n"
"MA v,160,a,148,a,112\n"
"MA v,164,a,148,a,116\n"
"s_add_u32 m0, s67, 32768\n"
"TR 160,238,0\n"
"MA v,164,a,144,a,100\n"
"buffer_load_dwordx4 v243, s[40:43], s68 offen lds\n"
"MA v,168,a,144,a,104\n"
"s_add_u32 s68, s68, 9216\n"
"TR 162,238,1024\n"
"MA v,168,a,148,a,120\n"
"TR 164,238,4096\n"
"MA v,172,a,148,a,124\n"
"s_add_u32 m0, s67, 33792\n"
"MA v,172,a,144,a,108\n"
"s_waitcnt lgkmcnt(3)\n"
"buffer_load_dwordx4 v244, s[40:43], s68 offen lds\n"
"MA v,188,a,152,a,108\n"
"s_add_u32 s68, s68, 9216\n"
"TR 166,238,5120\n"
"MA v,188,a,156,a,124\n"
"TR 168,238,8\n"
"MA v,184,a,156,a,120\n"
"s_add_u32 m0, s67, 34816\n"
"MA v,184,a,152,a,104\n"
"buffer_load_dwordx4 v243, s[40:43], s68 offen lds\n"
"MA v,180,a,152,a,100\n"
"s_add_u32 s68, s68, 9216\n"
"TR 170,238,1032\n"
"MA v,180,a,156,a,116\n"
"TR 172,238,4104\n"
"MA v,176,a,156,a,112\n"
"TR 174,238,5128\n"
"MA v,176,a,152,a,96\n"
"s_waitcnt lgkmcnt(4)\n"
"s_add_u32 m0, s67, 35840\n"
"MA v,192,a,160,a,96\n"
"buffer_load_dwordx4 v244, s[40:43], s68 offen lds\n"
"MA v,192,a,164,a,112\n"
"s_add_u32 s68, s68, 9216\n"
"TR 176,239,0\n"
"MA v,196,a,164,a,116\n"
"TR 178,239,1024\n"
"MA v,196,a,160,a,100\n"
"s_add_u32 m0, s65, 49200\n"
"MA v,200,a,160,a,104\n"
"buffer_load_dwordx4 v242, s[36:39], s66 offen lds\n"
"MA v,200,a,164,a,120\n"
"s_add_u32 s66, s66, 32768\n"
"TR 180,239,4096\n"
"MA v,204,a,164,a,124\n"
"TR 182,239,5120\n"
"MA v,204,a,160,a,108\n"
"s_waitcnt lgkmcnt(4)\n"
"s_add_u32 m0, s65, 50224\n"
"TR 184,239,8\n"
"MA v,220,a,168,a,108\n"
"buffer_load_dwordx4 v242, s[36:39], s66 offen lds\n"
"MA v,220,a,172,a,124\n"
"s_add_u32 s66, s66, 32768\n"
"TR 186,239,1032\n"
"MA v,216,a,172,a,120\n"
"MA v,216,a,168,a,104\n"
"s_add_u32 m0, s65, 57400\n"
"TR 188,239,4104\n"
"MA v,212,a,168,a,100\n"
"buffer_load_dwordx4 v242, s[36:39], s66 offen lds\n"
"MA v,212,a,172,a,116\n"
"s_add_u32 s66, s66, 32768\n"
"TR 190,239,5128\n"
"MA v,208,a,172,a,112\n"
"MA v,208,a,168,a,96\n"
"s_waitcnt lgkmcnt(4)\n"
"s_add_u32 m0, s65, 58424\n"
"MA v,224,a,176,a,96\n"
"buffer_load_dwordx4 v242, s[36:39], s66 offen lds\n"
"MA v,224,a,180,a,112\n"
"s_add_u32 s66, s66, 425728\n"
"MA v,228,a,180,a,116\n"
"s_add_u32 m0, s78, 143488\n"
"s_waitcnt lgkmcnt(0)\n"
"MA v,228,a,176,a,100\n"
"s_add_u32 s79, s78, 12288\n"
"s_waitcnt vmcnt(20)\n"
"MA v,232,a,176,a,104\n"
"s_barrier\n"
"buffer_load_dwordx4 v255, s[52:55], s79 offen lds\n"
"MA v,232,a,180,a,120\n"
"TR 128,236,16400\n"
"MA a,236,a,180,a,124\n"
"s_add_u32 m0, s78, 147584\n"
"TR 130,236,17424\n"
"MA a,236,a,176,a,108\n"
"s_add_u32 s79, s78, 16384\n"
"TR 132,236,20496\n"
"MA a,252,a,184,a,108\n"
"buffer_load_dwordx4 v255, s[52:55], s79 offen lds\n"
"MA a,252,a,188,a,124\n"
"TR 134,236,21520\n"
"MA a,248,a,188,a,120\n"
"MA a,248,a,184,a,104\n"
"TR 136,236,16408\n"
"MA a,244,a,184,a,100\n"
"TR 138,236,17432\n"
"MA a,244,a,188,a,116\n"
"MA a,240,a,188,a,112\n"
"MA a,240,a,184,a,96\n"
"s_waitcnt lgkmcnt(2)\n"
"MA v,0,a,128,a,96\n"
"TR 140,236,20504\n"
"MA v,0,a,132,a,112\n"
"TR 142,236,21528\n"
"MA v,4,a,132,a,116\n"
"s_add_u32 m0, s67, 36864\n"
"TR 144,237,16400\n"
"MA v,4,a,128,a,100\n"
"buffer_load_dwordx4 v243, s[40:43], s68 offen lds\n"
"MA v,8,a,128,a,104\n"
"s_add_u32 s68, s68, 9216\n"
"TR 146,237,17424\n"
"MA v,8,a,132,a,120\n"
"TR 148,237,20496\n"
"MA v,12,a,132,a,124\n"
"TR 150,237,21520\n"
"MA v,12,a,128,a,108\n"
"s_waitcnt lgkmcnt(4)\n"
"s_add_u32 m0, s67, 37888\n"
"TR 152,237,16408\n"
"MA v,28,a,136,a,108\n"
"buffer_load_dwordx4 v244, s[40:43], s68 offen lds\n"
"MA v,28,a,140,a,124\n"
"s_add_u32 s68, s68, 9216\n"
"TR 154,237,17432\n"
"MA v,24,a,140,a,120\n"
"TR 156,237,20504\n"
"MA v,24,a,136,a,104\n"
"TR 158,237,21528\n"
"MA v,20,a,136,a,100\n"
"s_add_u32 m0, s67, 38912\n"
"TR 160,238,16400\n"
"MA v,20,a,140,a,116\n"
"buffer_load_dwordx4 v243, s[40:43], s68 offen lds\n"
"MA v,16,a,140,a,112\n"
"s_add_u32 s68, s68, 9216\n"
"TR 162,238,17424\n"
"MA v,16,a,136,a,96\n"
"s_waitcnt lgkmcnt(6)\n"
"TR 164,238,20496\n"
"MA v,32,a,144,a,96\n"
"TR 166,238,21520\n"
"MA v,32,a,148,a,112\n"
"s_add_u32 m0, s67, 39936\n"
"TR 168,238,16408\n"
"MA v,36,a,148,a,116\n"
"buffer_load_dwordx4 v244, s[40:43], s68 offen lds\n"
"MA v,36,a,144,a,100\n"
"s_sub_u32 s68, s68, 64384\n"
"TR 170,238,17432\n"
"MA v,40,a,144,a,104\n"
"TR 172,238,20504\n"
"MA v,40,a,148,a,120\n"
"TR 174,238,21528\n"
"MA v,44,a,148,a,124\n"
"MA v,44,a,144,a,108\n"
"s_waitcnt lgkmcnt(8)\n"
"s_add_u32 m0, s65, 0\n"
"TR 176,239,16400\n"
"MA v,60,a,152,a,108\n"
"buffer_load_dwordx4 v242, s[36:39], s66 offen lds\n"
"MA v,60,a,156,a,124\n"
"s_add_u32 s66, s66, 32768\n"
"TR 178,239,17424\n"
"MA v,56,a,156,a,120\n"
"TR 180,239,20496\n"
"MA v,56,a,152,a,104\n"
"TR 182,239,21520\n"
"MA v,52,a,152,a,100\n"
"s_add_u32 m0, s65, 1024\n"
"TR 184,239,16408\n"
"MA v,52,a,156,a,116\n"
"buffer_load_dwordx4 v242, s[36:39], s66 offen lds\n"
"MA v,48,a,156,a,112\n"
"s_add_u32 s66, s66, 32768\n"
"TR 186,239,17432\n"
"MA v,48,a,152,a,96\n"
"s_waitcnt lgkmcnt(10)\n"
"TR 188,239,20504\n"
"MA v,64,a,160,a,96\n"
"TR 190,239,21528\n"
"MA v,64,a,164,a,112\n"
"s_add_u32 m0, s65, 8200\n"
"MA v,68,a,164,a,116\n"
"buffer_load_dwordx4 v242, s[36:39], s66 offen lds\n"
"MA v,68,a,160,a,100\n"
"s_add_u32 s66, s66, 32768\n"
"MA v,72,a,160,a,104\n"
"MA v,72,a,164,a,120\n"
"s_waitcnt vmcnt(17)\n"
"RD 64,240,0\n"
"MA v,76,a,164,a,124\n"
"s_add_u32 m0, s65, 9224\n"
"RD 68,240,2048\n"
"MA v,76,a,160,a,108\n"
"s_waitcnt lgkmcnt(10)\n"
"buffer_load_dwordx4 v242, s[36:39], s66 offen lds\n"
"MA v,92,a,168,a,108\n"
"s_sub_u32 s66, s66, 98048\n"
"RD 72,240,4096\n"
"MA v,92,a,172,a,124\n"
"RD 76,240,6144\n"
"MA v,88,a,172,a,120\n"
"RD 80,241,0\n"
"MA v,88,a,168,a,104\n"
"s_add_u32 m0, s78, 131200\n"
"RD 84,241,2048\n"
"MA v,84,a,168,a,100\n"
"s_add_u32 s79, s78, 0\n"
"RD 88,241,4096\n"
"MA v,84,a,172,a,116\n"
"buffer_load_dwordx4 v255, s[52:55], s79 offen lds\n"
"MA v,80,a,172,a,112\n"
"RD 92,241,6144\n"
"MA v,80,a,168,a,96\n"
"s_waitcnt lgkmcnt(12)\n"
"MA v,96,a,176,a,96\n"
"s_add_u32 m0, s78, 135296\n"
"MA v,96,a,180,a,112\n"
"s_add_u32 s79, s78, 4096\n"
"MA v,100,a,180,a,116\n"
"s_waitcnt lgkmcnt(0)\n"
"buffer_load_dwordx4 v255, s[52:55], s79 offen lds\n"
"MA v,100,a,176,a,100\n"
"s_waitcnt vmcnt(63)\n"
"MA v,104,a,176,a,104\n"
"s_barrier\n"
"MA v,104,a,180,a,120\n"
"s_add_u32 m0, s78, 139392\n"
"TR 128,236,32800\n"
"MA v,108,a,180,a,124\n"
"s_add_u32 s79, s78, 8192\n"
"TR 130,236,33824\n"
"MA v,108,a,176,a,108\n"
"buffer_load_dwordx4 v255, s[52:55], s79 offen lds\n"
"MA v,124,a,184,a,108\n"
"TR 132,236,36896\n"
"MA v,124,a,188,a,124\n"
"TR 134,236,37920\n"
"MA v,120,a,188,a,120\n"
"MA v,120,a,184,a,104\n"
"TR 136,236,32808\n"
"MA v,116,a,184,a,100\n"
"TR 138,236,33832\n"
"MA v,116,a,188,a,116\n"
"MA v,112,a,188,a,112\n"
"MA v,112,a,184,a,96\n"
"s_waitcnt lgkmcnt(2)\n"
"s_mov_b64 exec, 0\n"
"s_cmp_eq_u32 s64, 0\n"
"s_cbranch_scc1 L_ST5_%=\n"
"s_cmp_eq_u32 s64, 1\n"
"s_cbranch_scc1 L_ST6_%=\n"
"s_cmp_eq_u32 s64, 2\n"
"s_cbranch_scc1 L_ST7_%=\n"
"s_cmp_eq_u32 s64, 3\n"
"s_cbranch_scc1 L_ST8_%=\n"
"L_VB1_%=:\n"
"s_mov_b64 exec, -1\n"
"L_G0_%=:\n"
"MA v,128,a,128,a,64\n"
"TR 140,236,36904\n"
"MA v,128,a,132,a,80\n"
"TR 142,236,37928\n"
"MA v,132,a,132,a,84\n"
"s_add_u32 m0, s67, 0\n"
"MA v,132,a,128,a,68\n"
"buffer_load_dwordx4 v243, s[40:43], s68 offen lds\n"
"MA v,136,a,128,a,72\n"
"s_add_u32 s68, s68, 9216\n"
"TR 144,237,32800\n"
"MA v,136,a,132,a,88\n"
"TR 146,237,33824\n"
"MA v,140,a,132,a,92\n"
"MA v,140,a,128,a,76\n"
"s_waitcnt lgkmcnt(2)\n"
"TR 148,237,36896\n"
"MA v,156,a,136,a,76\n"
"MA v,156,a,140,a,92\n"
"s_add_u32 m0, s67, 1024\n"
"TR 150,237,37920\n"
"MA v,152,a,140,a,88\n"
"buffer_load_dwordx4 v244, s[40:43], s68 offen lds\n"
"MA v,152,a,136,a,72\n"
"s_add_u32 s68, s68, 9216\n"
"TR 152,237,32808\n"
"MA v,148,a,136,a,68\n"
"TR 154,237,33832\n"
"MA v,148,a,140,a,84\n"
"MA v,144,a,140,a,80\n"
"TR 156,237,36904\n"
"MA v,144,a,136,a,64\n"
"s_waitcnt lgkmcnt(3)\n"
"MA v,160,a,144,a,64\n"
"s_add_u32 m0, s67, 2048\n"
"TR 158,237,37928\n"
"MA v,160,a,148,a,80\n"
"buffer_load_dwordx4 v243, s[40:43], s68 offen lds\n"
"MA v,164,a,148,a,84\n"
"s_add_u32 s68, s68, 9216\n"
"TR 160,238,32800\n"
"MA v,164,a,144,a,68\n"
"TR 162,238,33824\n"
"MA v,168,a,144,a,72\n"
"MA v,168,a,148,a,88\n"
"TR 164,238,36896\n"
"MA v,172,a,148,a,92\n"
"MA v,172,a,144,a,76\n"
"s_waitcnt lgkmcnt(3)\n"
"s_add_u32 m0, s67, 3072\n"
"TR 166,238,37920\n"
"MA v,188,a,152,a,76\n"
"buffer_load_dwordx4 v244, s[40:43], s68 offen lds\n"
"MA v,188,a,156,a,92\n"
"s_add_u32 s68, s68, 9216\n"
"TR 168,238,32808\n"
"MA v,184,a,156,a,88\n"
"MA v,184,a,152,a,72\n"
"TR 170,238,33832\n"
"MA v,180,a,152,a,68\n"
"TR 172,238,36904\n"
"MA v,180,a,156,a,84\n"
"MA v,176,a,156,a,80\n"
"TR 174,238,37928\n"
"MA v,176,a,152,a,64\n"
"s_waitcnt lgkmcnt(4)\n"
"s_add_u32 m0, s65, 16400\n"
"MA v,192,a,160,a,64\n"
"buffer_load_dwordx4 v242, s[36:39], s66 offen lds\n"
"MA v,192,a,164,a,80\n"
"s_add_u32 s66, s66, 32768\n"
"TR 176,239,32800\n"
"MA v,196,a,164,a,84\n"
"TR 178,239,33824\n"
"MA v,196,a,160,a,68\n"
"MA v,200,a,160,a,72\n"
"TR 180,239,36896\n"
"MA v,200,a,164,a,88\n"
"TR 182,239,37920\n"
"MA v,204,a,164,a,92\n"
"s_add_u32 m0, s65, 17424\n"
"MA v,204,a,160,a,76\n"
"s_waitcnt lgkmcnt(4)\n"
"buffer_load_dwordx4 v242, s[36:39], s66 offen lds\n"
"MA v,220,a,168,a,76\n"
"s_add_u32 s66, s66, 32768\n"
"TR 184,239,32808\n"
"MA v,220,a,172,a,92\n"
"TR 186,239,33832\n"
"MA v,216,a,172,a,88\n"
"MA v,216,a,168,a,72\n"
"TR 188,239,36904\n"
"MA v,212,a,168,a,68\n"
"MA v,212,a,172,a,84\n"
"s_add_u32 m0, s65, 24600\n"
"TR 190,239,37928\n"
"MA v,208,a,172,a,80\n"
"buffer_load_dwordx4 v242, s[36:39], s66 offen lds\n"
"MA v,208,a,168,a,64\n"
"s_waitcnt lgkmcnt(4)\n"
"s_add_u32 s66, s66, 32768\n"
"MA v,224,a,176,a,64\n"
"MA v,224,a,180,a,80\n"
"MA v,228,a,180,a,84\n"
"s_waitcnt lgkmcnt(0)\n"
"MA v,228,a,176,a,68\n"
"s_waitcnt vmcnt(11)\n"
"MA v,232,a,176,a,72\n"
"s_add_u32 m0, s65, 25624\n"
"s_barrier\n"
"MA v,232,a,180,a,88\n"
"buffer_load_dwordx4 v242, s[36:39], s66 offen lds\n"
"MA a,236,a,180,a,92\n"
"s_add_u32 s66, s66, 425728\n"
"TR 128,236,49200\n"
"MA a,236,a,176,a,76\n"
"TR 130,236,50224\n"
"MA a,252,a,184,a,76\n"
"TR 132,236,53296\n"
"MA a,252,a,188,a,92\n"
"TR 134,236,54320\n"
"MA a,248,a,188,a,88\n"
"MA a,248,a,184,a,72\n"
"TR 136,236,49208\n"
"MA a,244,a,184,a,68\n"
"TR 138,236,50232\n"
"MA a,244,a,188,a,84\n"
"MA a,240,a,188,a,80\n"
"MA a,240,a,184,a,64\n"
"s_waitcnt lgkmcnt(2)\n"
"MA v,0,a,128,a,64\n"
"TR 140,236,53304\n"
"MA v,0,a,132,a,80\n"
"TR 142,236,54328\n"
"MA v,4,a,132,a,84\n"
"s_add_u32 m0, s67, 4096\n"
"RD 96,240,32768\n"
"MA v,4,a,128,a,68\n"
"buffer_load_dwordx4 v243, s[40:43], s68 offen lds\n"
"MA v,8,a,128,a,72\n"
"s_add_u32 s68, s68, 9216\n"
"TR 144,237,49200\n"
"MA v,8,a,132,a,88\n"
"TR 146,237,50224\n"
"MA v,12,a,132,a,92\n"
"RD 100,240,34816\n"
"MA v,12,a,128,a,76\n"
"s_waitcnt lgkmcnt(4)\n"
"TR 148,237,53296\n"
"MA v,28,a,136,a,76\n"
"RD 104,240,36864\n"
"MA v,28,a,140,a,92\n"
"s_add_u32 m0, s67, 5120\n"
"TR 150,237,54320\n"
"MA v,24,a,140,a,88\n"
"buffer_load_dwordx4 v244, s[40:43], s68 offen lds\n"
"MA v,24,a,136,a,72\n"
"s_add_u32 s68, s68, 9216\n"
"TR 152,237,49208\n"
"MA v,20,a,136,a,68\n"
"TR 154,237,50232\n"
"MA v,20,a,140,a,84\n"
"RD 108,240,38912\n"
"MA v,16,a,140,a,80\n"
"TR 156,237,53304\n"
"MA v,16,a,136,a,64\n"
"s_waitcnt lgkmcnt(4)\n"
"RD 112,241,32768\n"
"MA v,32,a,144,a,64\n"
"s_add_u32 m0, s67, 6144\n"
"TR 158,237,54328\n"
"MA v,32,a,148,a,80\n"
"buffer_load_dwordx4 v243, s[40:43], s68 offen lds\n"
"MA v,36,a,148,a,84\n"
"s_add_u32 s68, s68, 9216\n"
"TR 160,238,49200\n"
"MA v,36,a,144,a,68\n"
"TR 162,238,50224\n"
"MA v,40,a,144,a,72\n"
"RD 116,241,34816\n"
"MA v,40,a,148,a,88\n"
"TR 164,238,53296\n"
"MA v,44,a,148,a,92\n"
"RD 120,241,36864\n"
"MA v,44,a,144,a,76\n"
"s_waitcnt lgkmcnt(5)\n"
"s_add_u32 m0, s67, 7168\n"
"TR 166,238,54320\n"
"MA v,60,a,152,a,76\n"
"buffer_load_dwordx4 v244, s[40:43], s68 offen lds\n"
"MA v,60,a,156,a,92\n"
"s_sub_u32 s68, s68, 64384\n"
"TR 168,238,49208\n"
"MA v,56,a,156,a,88\n"
"RD 124,241,38912\n"
"MA v,56,a,152,a,72\n"
"TR 170,238,50232\n"
"MA v,52,a,152,a,68\n"
"TR 172,238,53304\n"
"MA v,52,a,156,a,84\n"
"MA v,48,a,156,a,80\n"
"TR 174,238,54328\n"
"MA v,48,a,152,a,64\n"
"s_waitcnt lgkmcnt(5)\n"
"s_add_u32 m0, s65, 32800\n"
"MA v,64,a,160,a,64\n"
"buffer_load_dwordx4 v242, s[36:39], s66 offen lds\n"
"MA v,64,a,164,a,80\n"
"s_add_u32 s66, s66, 32768\n"
"TR 176,239,49200\n"
"MA v,68,a,164,a,84\n"
"TR 178,239,50224\n"
"MA v,68,a,160,a,68\n"
"MA v,72,a,160,a,72\n"
"TR 180,239,53296\n"
"MA v,72,a,164,a,88\n"
"TR 182,239,54320\n"
"MA v,76,a,164,a,92\n"
"s_add_u32 m0, s65, 33824\n"
"MA v,76,a,160,a,76\n"
"s_waitcnt lgkmcnt(4)\n"
"buffer_load_dwordx4 v242, s[36:39], s66 offen lds\n"
"MA v,92,a,168,a,76\n"
"s_add_u32 s66, s66, 32768\n"
"TR 184,239,49208\n"
"MA v,92,a,172,a,92\n"
"L_ST2_%=:\n"
"s_cbranch_execz L_VB0_%=\n"
"TR 186,239,50232\n"
"MA v,88,a,172,a,88\n"
"MA v,88,a,168,a,72\n"
"TR 188,239,53304\n"
"MA v,84,a,168,a,68\n"
"MA v,84,a,172,a,84\n"
"s_add_u32 m0, s65, 41000\n"
"TR 190,239,54328\n"
"MA v,80,a,172,a,80\n"
"buffer_load_dwordx4 v242, s[36:39], s66 offen lds\n"
"MA v,80,a,168,a,64\n"
"s_waitcnt lgkmcnt(4)\n"
"s_add_u32 s66, s66, 32768\n"
"MA v,96,a,176,a,64\n"
"MA v,96,a,180,a,80\n"
"MA v,100,a,180,a,84\n"
"s_waitcnt lgkmcnt(0)\n"
"MA v,100,a,176,a,68\n"
"s_waitcnt vmcnt(15)\n"
"MA v,104,a,176,a,72\n"
"s_add_u32 m0, s65, 42024\n"
"s_barrier\n"
"MA v,104,a,180,a,88\n"
"buffer_load_dwordx4 v242, s[36:39], s66 offen lds\n"
"MA v,108,a,180,a,92\n"
"s_sub_u32 s66, s66, 98048\n"
"TR 128,236,0\n"
"MA v,108,a,176,a,76\n"
"TR 130,236,1024\n"
"MA v,124,a,184,a,76\n"
"TR 132,236,4096\n"
"MA v,124,a,188,a,92\n"
"TR 134,236,5120\n"
"MA v,120,a,188,a,88\n"
"MA v,120,a,184,a,72\n"
"TR 136,236,8\n"
"MA v,116,a,184,a,68\n"
"TR 138,236,1032\n"
"MA v,116,a,188,a,84\n"
"MA v,112,a,188,a,80\n"
"MA v,112,a,184,a,64\n"
"s_waitcnt lgkmcnt(2)\n"
"MA v,128,a,128,a,96\n"
"TR 140,236,4104\n"
"MA v,128,a,132,a,112\n"
"TR 142,236,5128\n"
"MA v,132,a,132,a,116\n"
"s_add_u32 m0, s67, 32768\n"
"MA v,132,a,128,a,100\n"
"buffer_load_dwordx4 v243, s[40:43], s68 offen lds\n"
"MA v,136,a,128,a,104\n"
"s_add_u32 s68, s68, 9216\n"
"TR 144,237,0\n"
"MA v,136,a,132,a,120\n"
"TR 146,237,1024\n"
"MA v,140,a,132,a,124\n"
"MA v,140,a,128,a,108\n"
"s_waitcnt lgkmcnt(2)\n"
"TR 148,237,4096\n"
"MA v,156,a,136,a,108\n"
"s_add_u32 m0, s67, 33792\n"
"MA v,156,a,140,a,124\n"
"buffer_load_dwordx4 v244, s[40:43], s68 offen lds\n"
"MA v,152,a,140,a,120\n"
"s_add_u32 s68, s68, 9216\n"
"TR 150,237,5120\n"
"MA v,152,a,136,a,104\n"
"TR 152,237,8\n"
"MA v,148,a,136,a,100\n"
"TR 154,237,1032\n"
"MA v,148,a,140,a,116\n"
"MA v,144,a,140,a,112\n"
"s_add_u32 m0, s67, 34816\n"
"TR 156,237,4104\n"
"MA v,144,a,136,a,96\n"
"s_waitcnt lgkmcnt(3)\n"
"buffer_load_dwordx4 v243, s[40:43], s68 offen lds\n"
"MA v,160,a,144,a,96\n"
"s_add_u32 s68, s68, 9216\n"
"TR 158,237,5128\n"
"MA v,160,a,148,a,112\n"
"MA v,164,a,148,a,116\n"
"TR 160,238,0\n"
"MA v,164,a,144,a,100\n"
"TR 162,238,1024\n"
"MA v,168,a,144,a,104\n"
"MA v,168,a,148,a,120\n"
"s_add_u32 m0, s67, 35840\n"
"TR 164,238,4096\n"
"MA v,172,a,148,a,124\n"
"buffer_load_dwordx4 v244, s[40:43], s68 offen lds\n"
"MA v,172,a,144,a,108\n"
"s_waitcnt lgkmcnt(3)\n"
"s_add_u32 s68, s68, 9216\n"
"TR 166,238,5120\n"
"MA v,188,a,152,a,108\n"
"MA v,188,a,156,a,124\n"
"TR 168,238,8\n"
"MA v,184,a,156,a,120\n"
"MA v,184,a,152,a,104\n"
"s_add_u32 m0, s65, 49200\n"
"TR 170,238,1032\n"
"MA v,180,a,152,a,100\n"
"buffer_load_dwordx4 v242, s[36:39], s66 offen lds\n"
"MA v,180,a,156,a,116\n"
"s_add_u32 s66, s66, 32768\n"
"TR 172,238,4104\n"
"MA v,176,a,156,a,112\n"
"TR 174,238,5128\n"
"MA v,176,a,152,a,96\n"
"s_waitcnt lgkmcnt(4)\n"
"MA v,192,a,160,a,96\n"
"TR 176,239,0\n"
"MA v,192,a,164,a,112\n"
"MA v,196,a,164,a,116\n"
"s_add_u32 m0, s65, 50224\n"
"TR 178,239,1024\n"
"MA v,196,a,160,a,100\n"
"buffer_load_dwordx4 v242, s[36:39], s66 offen lds\n"
"MA v,200,a,160,a,104\n"
"s_add_u32 s66, s66, 32768\n"
"TR 180,239,4096\n"
"MA v,200,a,164,a,120\n"
"TR 182,239,5120\n"
"MA v,204,a,164,a,124\n"
"MA v,204,a,160,a,108\n"
"s_waitcnt lgkmcnt(4)\n"
"TR 184,239,8\n"
"MA v,220,a,168,a,108\n"
"s_add_u32 m0, s65, 57400\n"
"MA v,220,a,172,a,124\n"
"buffer_load_dwordx4 v242, s[36:39], s66 offen lds\n"
"MA v,216,a,172,a,120\n"
"s_add_u32 s66, s66, 32768\n"
"TR 186,239,1032\n"
"MA v,216,a,168,a,104\n"
"TR 188,239,4104\n"
"MA v,212,a,168,a,100\n"
"MA v,212,a,172,a,116\n"
"TR 190,239,5128\n"
"MA v,208,a,172,a,112\n"
"MA v,208,a,168,a,96\n"
"s_waitcnt lgkmcnt(4)\n"
"s_add_u32 m0, s65, 58424\n"
"MA v,224,a,176,a,96\n"
"buffer_load_dwordx4 v242, s[36:39], s66 offen lds\n"
"MA v,224,a,180,a,112\n"
"s_add_u32 s66, s66, 425728\n"
"MA v,228,a,180,a,116\n"
"s_waitcnt lgkmcnt(0)\n"
"MA v,228,a,176,a,100\n"
"s_waitcnt vmcnt(12)\n"
"MA v,232,a,176,a,104\n"
"s_barrier\n"
"MA v,232,a,180,a,120\n"
"TR 128,236,16400\n"
"MA a,236,a,180,a,124\n"
"global_load_dword a229, v255, s[34:35]\n"
"MA a,236,a,176,a,108\n"
"TR 130,236,17424\n"
"MA a,252,a,184,a,108\n"
"TR 132,236,20496\n"
"MA a,252,a,188,a,124\n"
"TR 134,236,21520\n"
"MA a,248,a,188,a,120\n"
"MA a,248,a,184,a,104\n"
"TR 136,236,16408\n"
"MA a,244,a,184,a,100\n"
"TR 138,236,17432\n"
"MA a,244,a,188,a,116\n"
"MA a,240,a,188,a,112\n"
"MA a,240,a,184,a,96\n"
"s_waitcnt lgkmcnt(2)\n"
"MA v,0,a,128,a,96\n"
"TR 140,236,20504\n"
"MA v,0,a,132,a,112\n"
"TR 142,236,21528\n"
"MA v,4,a,132,a,116\n"
"s_add_u32 m0, s67, 36864\n"
"RD 64,240,0\n"
"MA v,4,a,128,a,100\n"
"buffer_load_dwordx4 v243, s[40:43], s68 offen lds\n"
"MA v,8,a,128,a,104\n"
"s_add_u32 s68, s68, 9216\n"
"TR 144,237,16400\n"
"MA v,8,a,132,a,120\n"
"TR 146,237,17424\n"
"MA v,12,a,132,a,124\n"
"RD 68,240,2048\n"
"MA v,12,a,128,a,108\n"
"s_waitcnt lgkmcnt(4)\n"
"TR 148,237,20496\n"
"MA v,28,a,136,a,108\n"
"RD 72,240,4096\n"
"MA v,28,a,140,a,124\n"
"s_add_u32 m0, s67, 37888\n"
"TR 150,237,21520\n"
"MA v,24,a,140,a,120\n"
"buffer_load_dwordx4 v244, s[40:43], s68 offen lds\n"
"MA v,24,a,136,a,104\n"
"s_add_u32 s68, s68, 9216\n"
"TR 152,237,16408\n"
"MA v,20,a,136,a,100\n"
"TR 154,237,17432\n"
"MA v,20,a,140,a,116\n"
"RD 76,240,6144\n"
"MA v,16,a,140,a,112\n"
"TR 156,237,20504\n"
"MA v,16,a,136,a,96\n"
"s_waitcnt lgkmcnt(4)\n"
"RD 80,241,0\n"
"MA v,32,a,144,a,96\n"
"s_add_u32 m0, s67, 38912\n"
"TR 158,237,21528\n"
"MA v,32,a,148,a,112\n"
"buffer_load_dwordx4 v243, s[40:43], s68 offen lds\n"
"MA v,36,a,148,a,116\n"
"s_add_u32 s68, s68, 9216\n"
"TR 160,238,16400\n"
"MA v,36,a,144,a,100\n"
"TR 162,238,17424\n"
"MA v,40,a,144,a,104\n"
"RD 84,241,2048\n"
"MA v,40,a,148,a,120\n"
"TR 164,238,20496\n"
"MA v,44,a,148,a,124\n"
"RD 88,241,4096\n"
"MA v,44,a,144,a,108\n"
"s_waitcnt lgkmcnt(5)\n"
"s_add_u32 m0, s67, 39936\n"
"TR 166,238,21520\n"
"MA v,60,a,152,a,108\n"
"buffer_load_dwordx4 v244, s[40:43], s68 offen lds\n"
"MA v,60,a,156,a,124\n"
"s_sub_u32 s68, s68, 64384\n"
"TR 168,238,16408\n"
"MA v,56,a,156,a,120\n"
"RD 92,241,6144\n"
"MA v,56,a,152,a,104\n"
"TR 170,238,17432\n"
"MA v,52,a,152,a,100\n"
"TR 172,238,20504\n"
"MA v,52,a,156,a,116\n"
"MA v,48,a,156,a,112\n"
"TR 174,238,21528\n"
"MA v,48,a,152,a,96\n"
"s_waitcnt lgkmcnt(5)\n"
"s_add_u32 m0, s65, 0\n"
"MA v,64,a,160,a,96\n"
"buffer_load_dwordx4 v242, s[36:39], s66 offen lds\n"
"MA v,64,a,164,a,112\n"
"s_add_u32 s66, s66, 32768\n"
"TR 176,239,16400\n"
"MA v,68,a,164,a,116\n"
"TR 178,239,17424\n"
"MA v,68,a,160,a,100\n"
"MA v,72,a,160,a,104\n"
"TR 180,239,20496\n"
"MA v,72,a,164,a,120\n"
"TR 182,239,21520\n"
"MA v,76,a,164,a,124\n"
"s_add_u32 m0, s65, 1024\n"
"MA v,76,a,160,a,108\n"
"s_waitcnt lgkmcnt(4)\n"
"buffer_load_dwordx4 v242, s[36:39], s66 offen lds\n"
"MA v,92,a,168,a,108\n"
"s_add_u32 s66, s66, 32768\n"
"TR 184,239,16408\n"
"MA v,92,a,172,a,124\n"
"TR 186,239,17432\n"
"MA v,88,a,172,a,120\n"
"MA v,88,a,168,a,104\n"
"TR 188,239,20504\n"
"MA v,84,a,168,a,100\n"
"MA v,84,a,172,a,116\n"
"s_add_u32 m0, s65, 8200\n"
"TR 190,239,21528\n"
"MA v,80,a,172,a,112\n"
"buffer_load_dwordx4 v242, s[36:39], s66 offen lds\n"
"MA v,80,a,168,a,96\n"
"s_waitcnt lgkmcnt(4)\n"
"s_add_u32 s66, s66, 32768\n"
"MA v,96,a,176,a,96\n"
"MA v,96,a,180,a,112\n"
"MA v,100,a,180,a,116\n"
"s_waitcnt lgkmcnt(0)\n"
"MA v,100,a,176,a,100\n"
"s_waitcnt vmcnt(16)\n"
"MA v,104,a,176,a,104\n"
"s_add_u32 m0, s65, 9224\n"
"s_barrier\n"
"MA v,104,a,180,a,120\n"
"buffer_load_dwordx4 v242, s[36:39], s66 offen lds\n"
"MA v,108,a,180,a,124\n"
"s_sub_u32 s66, s66, 98048\n"
"TR 128,236,32800\n"
"MA v,108,a,176,a,108\n"
"TR 130,236,33824\n"
"MA v,124,a,184,a,108\n"
"TR 132,236,36896\n"
"MA v,124,a,188,a,124\n"
"TR 134,236,37920\n"
"MA v,120,a,188,a,120\n"
"MA v,120,a,184,a,104\n"
"TR 136,236,32808\n"
"MA v,116,a,184,a,100\n"
"TR 138,236,33832\n"
"MA v,116,a,188,a,116\n"
"s_sub_u32 s72, s72, 1\n"
"MA v,112,a,188,a,112\n"
"s_cmp_lg_u32 s72, 0\n"
"MA v,112,a,184,a,96\n"
"s_waitcnt lgkmcnt(2)\n"
"s_cbranch_scc1 L_G0_%=\n"
"MA v,128,a,128,a,64\n"
"TR 140,236,36904\n"
"MA v,128,a,132,a,80\n"
"TR 142,236,37928\n"
"MA v,132,a,132,a,84\n"
"s_add_u32 m0, s67, 0\n"
"TR 192,245,0\n"
"MA v,132,a,128,a,68\n"
"buffer_load_dwordx4 v243, s[40:43], s68 offen lds\n"
"MA v,136,a,128,a,72\n"
"s_add_u32 s68, s68, 9216\n"
"TR 144,237,32800\n"
"MA v,136,a,132,a,88\n"
"TR 146,237,33824\n"
"MA v,140,a,132,a,92\n"
"TR 196,245,32\n"
"MA v,140,a,128,a,76\n"
"s_waitcnt lgkmcnt(4)\n"
"TR 148,237,36896\n"
"MA v,156,a,136,a,76\n"
"TR 200,245,64\n"
"MA v,156,a,140,a,92\n"
"s_add_u32 m0, s67, 1024\n"
"TR 150,237,37920\n"
"MA v,152,a,140,a,88\n"
"buffer_load_dwordx4 v244, s[40:43], s68 offen lds\n"
"MA v,152,a,136,a,72\n"
"s_add_u32 s68, s68, 9216\n"
"TR 152,237,32808\n"
"MA v,148,a,136,a,68\n"
"TR 154,237,33832\n"
"MA v,148,a,140,a,84\n"
"TR 204,245,96\n"
"MA v,144,a,140,a,80\n"
"TR 156,237,36904\n"
"MA v,144,a,136,a,64\n"
"s_waitcnt lgkmcnt(4)\n"
"TR 208,245,128\n"
"MA v,160,a,144,a,64\n"
"s_add_u32 m0, s67, 2048\n"
"TR 158,237,37928\n"
"MA v,160,a,148,a,80\n"
"buffer_load_dwordx4 v243, s[40:43], s68 offen lds\n"
"MA v,164,a,148,a,84\n"
"s_add_u32 s68, s68, 9216\n"
"TR 160,238,32800\n"
"MA v,164,a,144,a,68\n"
"TR 162,238,33824\n"
"MA v,168,a,144,a,72\n"
"TR 212,245,160\n"
"MA v,168,a,148,a,88\n"
"TR 164,238,36896\n"
"MA v,172,a,148,a,92\n"
"TR 216,245,192\n"
"MA v,172,a,144,a,76\n"
"s_waitcnt lgkmcnt(5)\n"
"s_add_u32 m0, s67, 3072\n"
"TR 166,238,37920\n"
"MA v,188,a,152,a,76\n"
"buffer_load_dwordx4 v244, s[40:43], s68 offen lds\n"
"MA v,188,a,156,a,92\n"
"s_add_u32 s68, s68, 9216\n"
"TR 168,238,32808\n"
"MA v,184,a,156,a,88\n"
"TR 220,245,224\n"
"MA v,184,a,152,a,72\n"
"TR 170,238,33832\n"
"MA v,180,a,152,a,68\n"
"TR 172,238,36904\n"
"MA v,180,a,156,a,84\n"
"TR 224,245,256\n"
"MA v,176,a,156,a,80\n"
"s_mov_b32 s66, 4194304\n"
"TR 174,238,37928\n"
"MA v,176,a,152,a,64\n"
"s_waitcnt lgkmcnt(6)\n"
"s_add_u32 m0, s65, 16400\n"
"MA v,192,a,160,a,64\n"
"buffer_load_dwordx4 v242, s[36:39], s66 offen lds\n"
"MA v,192,a,164,a,80\n"
"s_add_u32 s66, s66, 32768\n"
"TR 176,239,32800\n"
"MA v,196,a,164,a,84\n"
"TR 178,239,33824\n"
"MA v,196,a,160,a,68\n"
"MA v,200,a,160,a,72\n"
"TR 180,239,36896\n"
"MA v,200,a,164,a,88\n"
"TR 182,239,37920\n"
"MA v,204,a,164,a,92\n"
"s_add_u32 m0, s65, 17424\n"
"MA v,204,a,160,a,76\n"
"s_waitcnt lgkmcnt(4)\n"
"buffer_load_dwordx4 v242, s[36:39], s66 offen lds\n"
"MA v,220,a,168,a,76\n"
"s_add_u32 s66, s66, 32768\n"
"TR 184,239,32808\n"
"MA v,220,a,172,a,92\n"
"TR 186,239,33832\n"
"MA v,216,a,172,a,88\n"
"MA v,216,a,168,a,72\n"
"TR 188,239,36904\n"
"MA v,212,a,168,a,68\n"
"MA v,212,a,172,a,84\n"
"s_add_u32 m0, s65, 24600\n"
"TR 190,239,37928\n"
"MA v,208,a,172,a,80\n"
"buffer_load_dwordx4 v242, s[36:39], s66 offen lds\n"
"MA v,208,a,168,a,64\n"
"s_waitcnt lgkmcnt(4)\n"
"s_add_u32 s66, s66, 32768\n"
"MA v,224,a,176,a,64\n"
"MA v,224,a,180,a,80\n"
"MA v,228,a,180,a,84\n"
"s_waitcnt lgkmcnt(0)\n"
"MA v,228,a,176,a,68\n"
"s_waitcnt vmcnt(11)\n"
"MA v,232,a,176,a,72\n"
"s_add_u32 m0, s65, 25624\n"
"s_barrier\n"
"MA v,232,a,180,a,88\n"
"buffer_load_dwordx4 v242, s[36:39], s66 offen lds\n"
"MA a,236,a,180,a,92\n"
"TR 128,236,49200\n"
"MA a,236,a,176,a,76\n"
"TR 130,236,50224\n"
"MA a,252,a,184,a,76\n"
"TR 132,236,53296\n"
"MA a,252,a,188,a,92\n"
"TR 134,236,54320\n"
"MA a,248,a,188,a,88\n"
"MA a,248,a,184,a,72\n"
"TR 136,236,49208\n"
"MA a,244,a,184,a,68\n"
"TR 138,236,50232\n"
"MA a,244,a,188,a,84\n"
"MA a,240,a,188,a,80\n"
"MA a,240,a,184,a,64\n"
"s_waitcnt lgkmcnt(2)\n"
"MA v,0,a,128,a,64\n"
"TR 140,236,53304\n"
"MA v,0,a,132,a,80\n"
"TR 142,236,54328\n"
"MA v,4,a,132,a,84\n"
"s_add_u32 m0, s67, 4096\n"
"RD 96,240,32768\n"
"MA v,4,a,128,a,68\n"
"buffer_load_dwordx4 v243, s[40:43], s68 offen lds\n"
"MA v,8,a,128,a,72\n"
"s_add_u32 s68, s68, 9216\n"
"TR 144,237,49200\n"
"MA v,8,a,132,a,88\n"
"TR 146,237,50224\n"
"MA v,12,a,132,a,92\n"
"RD 100,240,34816\n"
"MA v,12,a,128,a,76\n"
"s_waitcnt lgkmcnt(4)\n"
"s_add_u32 m0, s67, 5120\n"
"TR 148,237,53296\n"
"MA v,28,a,136,a,76\n"
"buffer_load_dwordx4 v244, s[40:43], s68 offen lds\n"
"MA v,28,a,140,a,92\n"
"s_add_u32 s68, s68, 9216\n"
"TR 150,237,54320\n"
"MA v,24,a,140,a,88\n"
"TR 152,237,49208\n"
"MA v,24,a,136,a,72\n"
"RD 104,240,36864\n"
"MA v,20,a,136,a,68\n"
"s_add_u32 m0, s67, 6144\n"
"TR 154,237,50232\n"
"MA v,20,a,140,a,84\n"
"buffer_load_dwordx4 v243, s[40:43], s68 offen lds\n"
"MA v,16,a,140,a,80\n"
"s_add_u32 s68, s68, 9216\n"
"TR 156,237,53304\n"
"MA v,16,a,136,a,64\n"
"s_waitcnt lgkmcnt(4)\n"
"RD 108,240,38912\n"
"MA v,32,a,144,a,64\n"
"TR 158,237,54328\n"
"MA v,32,a,148,a,80\n"
"s_add_u32 m0, s67, 7168\n"
"RD 112,241,32768\n"
"MA v,36,a,148,a,84\n"
"buffer_load_dwordx4 v244, s[40:43], s68 offen lds\n"
"MA v,36,a,144,a,68\n"
"s_sub_u32 s68, s68, 64384\n"
"TR 160,238,49200\n"
"MA v,40,a,144,a,72\n"
"TR 162,238,50224\n"
"MA v,40,a,148,a,88\n"
"TR 164,238,53296\n"
"MA v,44,a,148,a,92\n"
"s_mov_b32 s66, 3670272\n"
"RD 116,241,34816\n"
"MA v,44,a,144,a,76\n"
"s_waitcnt lgkmcnt(5)\n"
"s_add_u32 m0, s65, 32800\n"
"TR 166,238,54320\n"
"MA v,60,a,152,a,76\n"
"buffer_load_dwordx4 v242, s[36:39], s66 offen lds\n"
"MA v,60,a,156,a,92\n"
"s_add_u32 s66, s66, 32768\n"
"TR 168,238,49208\n"
"MA v,56,a,156,a,88\n"
"RD 120,241,36864\n"
"MA v,56,a,152,a,72\n"
"TR 170,238,50232\n"
"MA v,52,a,152,a,68\n"
"s_add_u32 m0, s65, 33824\n"
"TR 172,238,53304\n"
"MA v,52,a,156,a,84\n"
"buffer_load_dwordx4 v242, s[36:39], s66 offen lds\n"
"MA v,48,a,156,a,80\n"
"s_add_u32 s66, s66, 32768\n"
"TR 174,238,54328\n"
"MA v,48,a,152,a,64\n"
"s_waitcnt lgkmcnt(5)\n"
"RD 124,241,38912\n"
"MA v,64,a,160,a,64\n"
"L_ST3_%=:\n"
"s_cbranch_execz L_VB0_%=\n"
"TR 176,239,49200\n"
"MA v,64,a,164,a,80\n"
"s_add_u32 m0, s65, 41000\n"
"MA v,68,a,164,a,84\n"
"buffer_load_dwordx4 v242, s[36:39], s66 offen lds\n"
"MA v,68,a,160,a,68\n"
"s_add_u32 s66, s66, 32768\n"
"TR 178,239,50224\n"
"MA v,72,a,160,a,72\n"
"TR 180,239,53296\n"
"MA v,72,a,164,a,88\n"
"TR 182,239,54320\n"
"MA v,76,a,164,a,92\n"
"s_add_u32 m0, s65, 42024\n"
"MA v,76,a,160,a,76\n"
"s_waitcnt lgkmcnt(5)\n"
"buffer_load_dwordx4 v242, s[36:39], s66 offen lds\n"
"MA v,92,a,168,a,76\n"
"TR 184,239,49208\n"
"MA v,92,a,172,a,92\n"
"TR 186,239,50232\n"
"MA v,88,a,172,a,88\n"
"MA v,88,a,168,a,72\n"
"s_add_u32 m0, s78, 131200\n"
"TR 188,239,53304\n"
"MA v,84,a,168,a,68\n"
"s_add_u32 s79, s78, 18432\n"
"MA v,84,a,172,a,84\n"
"buffer_load_dwordx4 v255, s[52:55], s79 offen lds\n"
"MA v,80,a,172,a,80\n"
"TR 190,239,54328\n"
"MA v,80,a,168,a,64\n"
"s_waitcnt lgkmcnt(4)\n"
"MA v,96,a,176,a,64\n"
"s_add_u32 m0, s78, 135296\n"
"MA v,96,a,180,a,80\n"
"s_add_u32 s79, s78, 22528\n"
"MA v,100,a,180,a,84\n"
"s_waitcnt lgkmcnt(0)\n"
"buffer_load_dwordx4 v255, s[52:55], s79 offen lds\n"
"MA v,100,a,176,a,68\n"
"s_waitcnt vmcnt(18)\n"
"MA v,104,a,176,a,72\n"
"s_barrier\n"
"MA v,104,a,180,a,88\n"
"s_add_u32 m0, s78, 139392\n"
"TR 128,236,0\n"
"MA v,108,a,180,a,92\n"
"s_add_u32 s79, s78, 26624\n"
"TR 130,236,1024\n"
"MA v,108,a,176,a,76\n"
"buffer_load_dwordx4 v255, s[52:55], s79 offen lds\n"
"MA v,124,a,184,a,76\n"
"TR 132,236,4096\n"
"MA v,124,a,188,a,92\n"
"TR 134,236,5120\n"
"MA v,120,a,188,a,88\n"
"MA v,120,a,184,a,72\n"
"TR 136,236,8\n"
"MA v,116,a,184,a,68\n"
"TR 138,236,1032\n"
"MA v,116,a,188,a,84\n"
"MA v,112,a,188,a,80\n"
"MA v,112,a,184,a,64\n"
"s_waitcnt lgkmcnt(2)\n"
"MA v,128,a,128,a,96\n"
"TR 140,236,4104\n"
"MA v,128,a,132,a,112\n"
"s_mov_b32 s66, 4194560\n"
"TR 142,236,5128\n"
"MA v,132,a,132,a,116\n"
"s_add_u32 m0, s65, 49200\n"
"TR 144,237,0\n"
"MA v,132,a,128,a,100\n"
"buffer_load_dwordx4 v242, s[36:39], s66 offen lds\n"
"MA v,136,a,128,a,104\n"
"s_add_u32 s66, s66, 32768\n"
"TR 146,237,1024\n"
"MA v,136,a,132,a,120\n"
"TR 148,237,4096\n"
"MA v,140,a,132,a,124\n"
"TR 150,237,5120\n"
"MA v,140,a,128,a,108\n"
"s_waitcnt lgkmcnt(4)\n"
"s_add_u32 m0, s65, 50224\n"
"TR 152,237,8\n"
"MA v,156,a,136,a,108\n"
"buffer_load_dwordx4 v242, s[36:39], s66 offen lds\n"
"MA v,156,a,140,a,124\n"
"s_add_u32 s66, s66, 32768\n"
"TR 154,237,1032\n"
"MA v,152,a,140,a,120\n"
"TR 156,237,4104\n"
"MA v,152,a,136,a,104\n"
"TR 158,237,5128\n"
"MA v,148,a,136,a,100\n"
"TR 160,238,0\n"
"MA v,148,a,140,a,116\n"
"s_add_u32 m0, s65, 57400\n"
"MA v,144,a,140,a,112\n"
"buffer_load_dwordx4 v242, s[36:39], s66 offen lds\n"
"MA v,144,a,136,a,96\n"
"s_waitcnt lgkmcnt(5)\n"
"s_add_u32 s66, s66, 32768\n"
"TR 162,238,1024\n"
"MA v,160,a,144,a,96\n"
"TR 164,238,4096\n"
"MA v,160,a,148,a,112\n"
"TR 166,238,5120\n"
"MA v,164,a,148,a,116\n"
"TR 168,238,8\n"
"MA v,164,a,144,a,100\n"
"s_add_u32 m0, s65, 58424\n"
"TR 170,238,1032\n"
"MA v,168,a,144,a,104\n"
"buffer_load_dwordx4 v242, s[36:39], s66 offen lds\n"
"MA v,168,a,148,a,120\n"
"TR 172,238,4104\n"
"MA v,172,a,148,a,124\n"
"TR 174,238,5128\n"
"MA v,172,a,144,a,108\n"
"s_waitcnt lgkmcnt(8)\n"
"TR 176,239,0\n"
"MA v,188,a,152,a,108\n"
"s_add_u32 m0, s78, 143488\n"
"TR 178,239,1024\n"
"MA v,188,a,156,a,124\n"
"s_add_u32 s79, s78, 30720\n"
"TR 180,239,4096\n"
"MA v,184,a,156,a,120\n"
"buffer_load_dwordx4 v255, s[52:55], s79 offen lds\n"
"MA v,184,a,152,a,104\n"
"TR 182,239,5120\n"
"MA v,180,a,152,a,100\n"
"TR 184,239,8\n"
"MA v,180,a,156,a,116\n"
"TR 186,239,1032\n"
"MA v,176,a,156,a,112\n"
"s_add_u32 m0, s78, 147584\n"
"TR 188,239,4104\n"
"MA v,176,a,152,a,96\n"
"s_waitcnt lgkmcnt(11)\n"
"s_add_u32 s79, s78, 34816\n"
"MA v,192,a,160,a,96\n"
"buffer_load_dwordx4 v255, s[52:55], s79 offen lds\n"
"MA v,192,a,164,a,112\n"
"TR 190,239,5128\n"
"MA v,196,a,164,a,116\n"
"MA v,196,a,160,a,100\n"
"MA v,200,a,160,a,104\n"
"s_add_u32 m0, s69, 106496\n"
"MA v,200,a,164,a,120\n"
"buffer_load_dwordx4 v254, s[48:51], s25 offen nt lds\n"
"MA v,204,a,164,a,124\n"
"s_waitcnt vmcnt(14)\n"
"RD 64,240,0\n"
"MA v,204,a,160,a,108\n"
"s_waitcnt lgkmcnt(9)\n"
"RD 68,240,2048\n"
"MA v,220,a,168,a,108\n"
"RD 72,240,4096\n"
"MA v,220,a,172,a,124\n"
"RD 76,240,6144\n"
"MA v,216,a,172,a,120\n"
"s_add_u32 m0, s69, 107392\n"
"RD 80,241,0\n"
"MA v,216,a,168,a,104\n"
"buffer_load_dwordx4 v254, s[48:51], s25 offen offset:128 nt lds\n"
"MA v,212,a,168,a,100\n"
"RD 84,241,2048\n"
"MA v,212,a,172,a,116\n"
"RD 88,241,4096\n"
"MA v,208,a,172,a,112\n"
"RD 92,241,6144\n"
"MA v,208,a,168,a,96\n"
"s_waitcnt lgkmcnt(12)\n"
"MA v,224,a,176,a,96\n"
"s_add_u32 m0, s69, 114688\n"
"MA v,224,a,180,a,112\n"
"buffer_load_dwordx4 v254, s[48:51], s26 offen nt lds\n"
"MA v,228,a,180,a,116\n"
"s_waitcnt lgkmcnt(0)\n"
"MA v,228,a,176,a,100\n"
"s_waitcnt vmcnt(63)\n"
"MA v,232,a,176,a,104\n"
"s_barrier\n"
"MA v,232,a,180,a,120\n"
"TR 128,236,16400\n"
"MA a,236,a,180,a,124\n"
"s_add_u32 m0, s69, 115584\n"
"TR 130,236,17424\n"
"MA a,236,a,176,a,108\n"
"buffer_load_dwordx4 v254, s[48:51], s26 offen offset:128 nt lds\n"
"MA a,252,a,184,a,108\n"
"TR 132,236,20496\n"
"MA a,252,a,188,a,124\n"
"TR 134,236,21520\n"
"MA a,248,a,188,a,120\n"
"MA a,248,a,184,a,104\n"
"TR 136,236,16408\n"
"MA a,244,a,184,a,100\n"
"TR 138,236,17432\n"
"MA a,244,a,188,a,116\n"
"MA a,240,a,188,a,112\n"
"MA a,240,a,184,a,96\n"
"s_waitcnt lgkmcnt(2)\n"
"MA v,128,a,128,a,64\n"
"TR 140,236,20504\n"
"MA v,128,a,132,a,80\n"
"TR 142,236,21528\n"
"MA v,132,a,132,a,84\n"
"MA v,132,a,128,a,68\n"
"TR 144,237,16400\n"
"MA v,136,a,128,a,72\n"
"s_add_u32 m0, s69, 0\n"
"MA v,136,a,132,a,88\n"
"buffer_load_dwordx4 v253, s[44:47], s24 offen nt lds\n"
"MA v,140,a,132,a,92\n"
"TR 146,237,17424\n"
"MA v,140,a,128,a,76\n"
"s_waitcnt lgkmcnt(2)\n"
"TR 148,237,20496\n"
"MA v,156,a,136,a,76\n"
"MA v,156,a,140,a,92\n"
"TR 150,237,21520\n"
"MA v,152,a,140,a,88\n"
"TR 152,237,16408\n"
"MA v,152,a,136,a,72\n"
"MA v,148,a,136,a,68\n"
"TR 154,237,17432\n"
"MA v,148,a,140,a,84\n"
"MA v,144,a,140,a,80\n"
"TR 156,237,20504\n"
"MA v,144,a,136,a,64\n"
"s_waitcnt lgkmcnt(3)\n"
"MA v,160,a,144,a,64\n"
"TR 158,237,21528\n"
"MA v,160,a,148,a,80\n"
"MA v,164,a,148,a,84\n"
"s_add_u32 m0, s69, 896\n"
"TR 160,238,16400\n"
"MA v,164,a,144,a,68\n"
"buffer_load_dwordx4 v253, s[44:47], s24 offen offset:128 nt lds\n"
"MA v,168,a,144,a,72\n"
"TR 162,238,17424\n"
"MA v,168,a,148,a,88\n"
"TR 164,238,20496\n"
"MA v,172,a,148,a,92\n"
"MA v,172,a,144,a,76\n"
"s_waitcnt lgkmcnt(3)\n"
"TR 166,238,21520\n"
"MA v,188,a,152,a,76\n"
"MA v,188,a,156,a,92\n"
"TR 168,238,16408\n"
"MA v,184,a,156,a,88\n"
"MA v,184,a,152,a,72\n"
"TR 170,238,17432\n"
"MA v,180,a,152,a,68\n"
"TR 172,238,20504\n"
"MA v,180,a,156,a,84\n"
"MA v,176,a,156,a,80\n"
"TR 174,238,21528\n"
"MA v,176,a,152,a,64\n"
"s_waitcnt lgkmcnt(4)\n"
"MA v,192,a,160,a,64\n"
"s_add_u32 m0, s69, 8192\n"
"TR 176,239,16400\n"
"MA v,192,a,164,a,80\n"
"buffer_load_dwordx4 v253, s[44:47], s25 offen lds\n"
"MA v,196,a,164,a,84\n"
"TR 178,239,17424\n"
"MA v,196,a,160,a,68\n"
"MA v,200,a,160,a,72\n"
"TR 180,239,20496\n"
"MA v,200,a,164,a,88\n"
"TR 182,239,21520\n"
"MA v,204,a,164,a,92\n"
"MA v,204,a,160,a,76\n"
"s_waitcnt lgkmcnt(4)\n"
"TR 184,239,16408\n"
"MA v,220,a,168,a,76\n"
"MA v,220,a,172,a,92\n"
"TR 186,239,17432\n"
"MA v,216,a,172,a,88\n"
"MA v,216,a,168,a,72\n"
"TR 188,239,20504\n"
"MA v,212,a,168,a,68\n"
"MA v,212,a,172,a,84\n"
"TR 190,239,21528\n"
"MA v,208,a,172,a,80\n"
"s_add_u32 m0, s69, 9088\n"
"MA v,208,a,168,a,64\n"
"s_waitcnt lgkmcnt(4)\n"
"buffer_load_dwordx4 v253, s[44:47], s25 offen offset:128 lds\n"
"MA v,224,a,176,a,64\n"
"MA v,224,a,180,a,80\n"
"MA v,228,a,180,a,84\n"
"s_waitcnt lgkmcnt(0)\n"
"MA v,228,a,176,a,68\n"
"s_waitcnt vmcnt(17)\n"
"MA v,232,a,176,a,72\n"
"s_barrier\n"
"MA v,232,a,180,a,88\n"
"TR 128,236,32800\n"
"MA a,236,a,180,a,92\n"
"TR 130,236,33824\n"
"MA a,236,a,176,a,76\n"
"TR 132,236,36896\n"
"MA a,252,a,184,a,76\n"
"TR 134,236,37920\n"
"MA a,252,a,188,a,92\n"
"MA a,248,a,188,a,88\n"
"TR 136,236,32808\n"
"MA a,248,a,184,a,72\n"
"TR 138,236,33832\n"
"MA a,244,a,184,a,68\n"
"MA a,244,a,188,a,84\n"
"MA a,240,a,188,a,80\n"
"MA a,240,a,184,a,64\n"
"s_waitcnt lgkmcnt(2)\n"
"MA v,0,a,128,a,96\n"
"TR 140,236,36904\n"
"MA v,0,a,132,a,112\n"
"TR 142,236,37928\n"
"MA v,4,a,132,a,116\n"
"s_add_u32 m0, s69, 98304\n"
"MA v,4,a,128,a,100\n"
"buffer_load_dwordx4 v254, s[48:51], s24 offen nt lds\n"
"MA v,8,a,128,a,104\n"
"TR 144,237,32800\n"
"v_mul_f32 v128, s76, v128\n"
"MA v,8,a,132,a,120\n"
"TR 146,237,33824\n"
"v_mul_f32 v129, s76, v129\n"
"MA v,12,a,132,a,124\n"
"v_mul_f32 v130, s76, v130\n"
"v_mul_f32 v131, s76, v131\n"
"MA v,12,a,128,a,108\n"
"s_waitcnt lgkmcnt(2)\n"
"TR 148,237,36896\n"
"CV 128,128,129\n"
"MA v,28,a,136,a,108\n"
"CV 130,130,131\n"
"v_accvgpr_write_b32 a0, v128\n"
"MA v,28,a,140,a,124\n"
"s_add_u32 m0, s69, 99200\n"
"TR 150,237,37920\n"
"v_accvgpr_write_b32 a1, v130\n"
"MA v,24,a,140,a,120\n"
"buffer_load_dwordx4 v254, s[48:51], s24 offen offset:128 nt lds\n"
"v_mul_f32 v144, s76, v144\n"
"MA v,24,a,136,a,104\n"
"TR 152,237,32808\n"
"v_mul_f32 v145, s76, v145\n"
"MA v,20,a,136,a,100\n"
"s_add_u32 s24, s70, s71\n"
"TR 154,237,33832\n"
"v_mul_f32 v146, s76, v146\n"
"MA v,20,a,140,a,116\n"
"s_and_b32 s24, s24, 0x3ffff\n"
"v_mul_f32 v147, s76, v147\n"
"CV 144,144,145\n"
"MA v,16,a,140,a,112\n"
"s_and_b32 s81, s70, 0xfffc0000\n"
"TR 156,237,36904\n"
"CV 146,146,147\n"
"MA v,16,a,136,a,96\n"
"s_waitcnt lgkmcnt(3)\n"
"v_accvgpr_write_b32 a2, v144\n"
"v_accvgpr_write_b32 a3, v146\n"
"MA v,32,a,144,a,96\n"
"s_add_u32 m0, s69, 16384\n"
"TR 158,237,37928\n"
"v_mul_f32 v132, s76, v132\n"
"MA v,32,a,148,a,112\n"
"s_or_b32 s24, s24, s81\n"
"buffer_load_dwordx4 v253, s[44:47], s26 offen lds\n"
"v_mul_f32 v133, s76, v133\n"
"MA v,36,a,148,a,116\n"
"s_add_u32 s70, s70, 8192\n"
"TR 160,238,32800\n"
"v_mul_f32 v134, s76, v134\n"
"MA v,36,a,144,a,100\n"
"s_add_u32 s25, s70, s71\n"
"TR 162,238,33824\n"
"v_mul_f32 v135, s76, v135\n"
"MA v,40,a,144,a,104\n"
"s_and_b32 s25, s25, 0x3ffff\n"
"CV 132,132,133\n"
"CV 134,134,135\n"
"MA v,40,a,148,a,120\n"
"s_and_b32 s81, s70, 0xfffc0000\n"
"TR 164,238,36896\n"
"v_accvgpr_write_b32 a16, v132\n"
"MA v,44,a,148,a,124\n"
"s_or_b32 s25, s25, s81\n"
"v_accvgpr_write_b32 a17, v134\n"
"v_mul_f32 v148, s76, v148\n"
"MA v,44,a,144,a,108\n"
"s_waitcnt lgkmcnt(3)\n"
"s_add_u32 m0, s69, 17280\n"
"TR 166,238,37920\n"
"v_mul_f32 v149, s76, v149\n"
"MA v,60,a,152,a,108\n"
"s_add_u32 s70, s70, 8192\n"
"buffer_load_dwordx4 v253, s[44:47], s26 offen offset:128 lds\n"
"v_mul_f32 v150, s76, v150\n"
"MA v,60,a,156,a,124\n"
"TR 168,238,32808\n"
"v_mul_f32 v151, s76, v151\n"
"MA v,56,a,156,a,120\n"
"CV 148,148,149\n"
"CV 150,150,151\n"
"MA v,56,a,152,a,104\n"
"TR 170,238,33832\n"
"v_accvgpr_write_b32 a18, v148\n"
"MA v,52,a,152,a,100\n"
"TR 172,238,36904\n"
"v_accvgpr_write_b32 a19, v150\n"
"MA v,52,a,156,a,116\n"
"v_mul_f32 v136, s76, v136\n"
"v_mul_f32 v137, s76, v137\n"
"MA v,48,a,156,a,112\n"
"TR 174,238,37928\n"
"v_mul_f32 v138, s76, v138\n"
"MA v,48,a,152,a,96\n"
"s_waitcnt lgkmcnt(4)\n"
"v_mul_f32 v139, s76, v139\n"
"CV 136,136,137\n"
"MA v,64,a,160,a,96\n"
"global_load_dword a230, v255, s[34:35]\n"
"CV 138,138,139\n"
"MA v,64,a,164,a,112\n"
"TR 176,239,32800\n"
"v_accvgpr_write_b32 a32, v136\n"
"MA v,68,a,164,a,116\n"
"TR 178,239,33824\n"
"v_accvgpr_write_b32 a33, v138\n"
"MA v,68,a,160,a,100\n"
"v_mul_f32 v152, s76, v152\n"
"v_mul_f32 v153, s76, v153\n"
"MA v,72,a,160,a,104\n"
"TR 180,239,36896\n"
"v_mul_f32 v154, s76, v154\n"
"MA v,72,a,164,a,120\n"
"TR 182,239,37920\n"
"v_mul_f32 v155, s76, v155\n"
"MA v,76,a,164,a,124\n"
"CV 152,152,153\n"
"CV 154,154,155\n"
"MA v,76,a,160,a,108\n"
"s_waitcnt lgkmcnt(4)\n"
"TR 184,239,32808\n"
"v_accvgpr_write_b32 a34, v152\n"
"MA v,92,a,168,a,108\n"
"v_accvgpr_write_b32 a35, v154\n"
"v_mul_f32 v140, s76, v140\n"
"MA v,92,a,172,a,124\n"
"TR 186,239,33832\n"
"v_mul_f32 v141, s76, v141\n"
"MA v,88,a,172,a,120\n"
"v_mul_f32 v142, s76, v142\n"
"v_mul_f32 v143, s76, v143\n"
"MA v,88,a,168,a,104\n"
"TR 188,239,36904\n"
"CV 140,140,141\n"
"MA v,84,a,168,a,100\n"
"CV 142,142,143\n"
"v_accvgpr_write_b32 a48, v140\n"
"MA v,84,a,172,a,116\n"
"TR 190,239,37928\n"
"v_accvgpr_write_b32 a49, v142\n"
"MA v,80,a,172,a,112\n"
"v_mul_f32 v156, s76, v156\n"
"v_mul_f32 v157, s76, v157\n"
"MA v,80,a,168,a,96\n"
"s_waitcnt lgkmcnt(4)\n"
"v_mul_f32 v158, s76, v158\n"
"v_mul_f32 v159, s76, v159\n"
"MA v,96,a,176,a,96\n"
"CV 156,156,157\n"
"CV 158,158,159\n"
"MA v,96,a,180,a,112\n"
"v_accvgpr_write_b32 a50, v156\n"
"v_accvgpr_write_b32 a51, v158\n"
"MA v,100,a,180,a,116\n"
"s_waitcnt lgkmcnt(0)\n"
"v_mul_f32 v160, s76, v160\n"
"v_mul_f32 v161, s76, v161\n"
"MA v,100,a,176,a,100\n"
"s_waitcnt vmcnt(13)\n"
"v_mul_f32 v162, s76, v162\n"
"v_mul_f32 v163, s76, v163\n"
"MA v,104,a,176,a,104\n"
"s_barrier\n"
"CV 160,160,161\n"
"CV 162,162,163\n"
"MA v,104,a,180,a,120\n"
"TR 128,236,49200\n"
"v_accvgpr_write_b32 a4, v160\n"
"MA v,108,a,180,a,124\n"
"TR 130,236,50224\n"
"v_accvgpr_write_b32 a5, v162\n"
"MA v,108,a,176,a,108\n"
"TR 132,236,53296\n"
"v_mul_f32 v176, s76, v176\n"
"MA v,124,a,184,a,108\n"
"TR 134,236,54320\n"
"v_mul_f32 v177, s76, v177\n"
"MA v,124,a,188,a,124\n"
"v_mul_f32 v178, s76, v178\n"
"v_mul_f32 v179, s76, v179\n"
"MA v,120,a,188,a,120\n"
"TR 136,236,49208\n"
"CV 176,176,177\n"
"MA v,120,a,184,a,104\n"
"TR 138,236,50232\n"
"CV 178,178,179\n"
"MA v,116,a,184,a,100\n"
"v_accvgpr_write_b32 a6, v176\n"
"v_accvgpr_write_b32 a7, v178\n"
"MA v,116,a,188,a,116\n"
"v_mul_f32 v164, s76, v164\n"
"v_mul_f32 v165, s76, v165\n"
"MA v,112,a,188,a,112\n"
"v_mul_f32 v166, s76, v166\n"
"v_mul_f32 v167, s76, v167\n"
"MA v,112,a,184,a,96\n"
"s_waitcnt lgkmcnt(2)\n"
"CV 164,164,165\n"
"CV 166,166,167\n"
"MA v,0,a,128,a,64\n"
"TR 140,236,53304\n"
"MA v,0,a,132,a,80\n"
"TR 142,236,54328\n"
"MA v,4,a,132,a,84\n"
"TR 194,245,0\n"
"MA v,4,a,128,a,68\n"
"TR 144,237,49200\n"
"MA v,8,a,128,a,72\n"
"s_add_u32 m0, s69, 24576\n"
"TR 198,245,32\n"
"v_accvgpr_write_b32 a20, v164\n"
"MA v,8,a,132,a,88\n"
"buffer_load_dwordx4 v253, s[44:47], s27 offen lds\n"
"v_accvgpr_write_b32 a21, v166\n"
"MA v,12,a,132,a,92\n"
"TR 146,237,50224\n"
"v_mul_f32 v180, s76, v180\n"
"MA v,12,a,128,a,76\n"
"s_waitcnt lgkmcnt(4)\n"
"TR 148,237,53296\n"
"v_mul_f32 v181, s76, v181\n"
"MA v,28,a,136,a,76\n"
"L_ST4_%=:\n"
"s_cbranch_execz L_VB0_%=\n"
"TR 202,245,64\n"
"v_mul_f32 v182, s76, v182\n"
"MA v,28,a,140,a,92\n"
"TR 150,237,54320\n"
"v_mul_f32 v183, s76, v183\n"
"MA v,24,a,140,a,88\n"
"TR 152,237,49208\n"
"CV 180,180,181\n"
"MA v,24,a,136,a,72\n"
"TR 206,245,96\n"
"CV 182,182,183\n"
"MA v,20,a,136,a,68\n"
"TR 154,237,50232\n"
"v_accvgpr_write_b32 a22, v180\n"
"MA v,20,a,140,a,84\n"
"TR 210,245,128\n"
"v_accvgpr_write_b32 a23, v182\n"
"MA v,16,a,140,a,80\n"
"TR 156,237,53304\n"
"v_mul_f32 v168, s76, v168\n"
"MA v,16,a,136,a,64\n"
"s_waitcnt lgkmcnt(5)\n"
"TR 214,245,160\n"
"v_mul_f32 v169, s76, v169\n"
"MA v,32,a,144,a,64\n"
"TR 158,237,54328\n"
"v_mul_f32 v170, s76, v170\n"
"MA v,32,a,148,a,80\n"
"TR 218,245,192\n"
"v_mul_f32 v171, s76, v171\n"
"MA v,36,a,148,a,84\n"
"s_add_u32 m0, s69, 25472\n"
"TR 160,238,49200\n"
"CV 168,168,169\n"
"MA v,36,a,144,a,68\n"
"buffer_load_dwordx4 v253, s[44:47], s27 offen offset:128 lds\n"
"CV 170,170,171\n"
"MA v,40,a,144,a,72\n"
"TR 162,238,50224\n"
"v_accvgpr_write_b32 a36, v168\n"
"MA v,40,a,148,a,88\n"
"TR 164,238,53296\n"
"v_accvgpr_write_b32 a37, v170\n"
"MA v,44,a,148,a,92\n"
"TR 222,245,224\n"
"v_mul_f32 v184, s76, v184\n"
"MA v,44,a,144,a,76\n"
"s_waitcnt lgkmcnt(5)\n"
"TR 166,238,54320\n"
"v_mul_f32 v185, s76, v185\n"
"MA v,60,a,152,a,76\n"
"TR 226,245,256\n"
"v_mul_f32 v186, s76, v186\n"
"MA v,60,a,156,a,92\n"
"TR 168,238,49208\n"
"v_mul_f32 v187, s76, v187\n"
"MA v,56,a,156,a,88\n"
"CV 184,184,185\n"
"CV 186,186,187\n"
"MA v,56,a,152,a,72\n"
"TR 170,238,50232\n"
"v_accvgpr_write_b32 a38, v184\n"
"MA v,52,a,152,a,68\n"
"TR 172,238,53304\n"
"v_accvgpr_write_b32 a39, v186\n"
"MA v,52,a,156,a,84\n"
"v_mul_f32 v172, s76, v172\n"
"v_mul_f32 v173, s76, v173\n"
"MA v,48,a,156,a,80\n"
"TR 174,238,54328\n"
"v_mul_f32 v174, s76, v174\n"
"MA v,48,a,152,a,64\n"
"s_waitcnt lgkmcnt(5)\n"
"v_mul_f32 v175, s76, v175\n"
"CV 172,172,173\n"
"MA v,64,a,160,a,64\n"
"s_add_u32 m0, s69, 32768\n"
"TR 176,239,49200\n"
"CV 174,174,175\n"
"MA v,64,a,164,a,80\n"
"buffer_load_dwordx4 v253, s[44:47], s24 offen lds\n"
"v_accvgpr_write_b32 a52, v172\n"
"MA v,68,a,164,a,84\n"
"TR 178,239,50224\n"
"v_accvgpr_write_b32 a53, v174\n"
"MA v,68,a,160,a,68\n"
"v_mul_f32 v188, s76, v188\n"
"v_mul_f32 v189, s76, v189\n"
"MA v,72,a,160,a,72\n"
"TR 180,239,53296\n"
"v_mul_f32 v190, s76, v190\n"
"MA v,72,a,164,a,88\n"
"TR 182,239,54320\n"
"v_mul_f32 v191, s76, v191\n"
"MA v,76,a,164,a,92\n"
"CV 188,188,189\n"
"CV 190,190,191\n"
"MA v,76,a,160,a,76\n"
"s_waitcnt lgkmcnt(4)\n"
"TR 184,239,49208\n"
"v_accvgpr_write_b32 a54, v188\n"
"MA v,92,a,168,a,76\n"
"v_accvgpr_write_b32 a55, v190\n"
"v_mul_f32 v192, s76, v192\n"
"MA v,92,a,172,a,92\n"
"TR 186,239,50232\n"
"v_mul_f32 v193, s76, v193\n"
"MA v,88,a,172,a,88\n"
"v_mul_f32 v194, s76, v194\n"
"v_mul_f32 v195, s76, v195\n"
"MA v,88,a,168,a,72\n"
"TR 188,239,53304\n"
"CV 192,192,193\n"
"MA v,84,a,168,a,68\n"
"CV 194,194,195\n"
"v_accvgpr_write_b32 a8, v192\n"
"MA v,84,a,172,a,84\n"
"TR 190,239,54328\n"
"v_accvgpr_write_b32 a9, v194\n"
"MA v,80,a,172,a,80\n"
"s_add_u32 m0, s69, 33664\n"
"v_mul_f32 v208, s76, v208\n"
"v_mul_f32 v209, s76, v209\n"
"MA v,80,a,168,a,64\n"
"s_waitcnt lgkmcnt(4)\n"
"buffer_load_dwordx4 v253, s[44:47], s24 offen offset:128 lds\n"
"v_mul_f32 v210, s76, v210\n"
"MA v,96,a,176,a,64\n"
"v_mul_f32 v211, s76, v211\n"
"CV 208,208,209\n"
"MA v,96,a,180,a,80\n"
"CV 210,210,211\n"
"v_accvgpr_write_b32 a10, v208\n"
"MA v,100,a,180,a,84\n"
"s_waitcnt lgkmcnt(0)\n"
"v_accvgpr_write_b32 a11, v210\n"
"v_mul_f32 v196, s76, v196\n"
"MA v,100,a,176,a,68\n"
"s_waitcnt vmcnt(11)\n"
"v_mul_f32 v197, s76, v197\n"
"v_mul_f32 v198, s76, v198\n"
"MA v,104,a,176,a,72\n"
"s_barrier\n"
"v_mul_f32 v199, s76, v199\n"
"CV 196,196,197\n"
"MA v,104,a,180,a,88\n"
"CV 198,198,199\n"
"v_accvgpr_write_b32 a24, v196\n"
"MA v,108,a,180,a,92\n"
"v_accvgpr_write_b32 a25, v198\n"
"v_mul_f32 v212, s76, v212\n"
"MA v,108,a,176,a,76\n"
"v_mul_f32 v213, s76, v213\n"
"v_mul_f32 v214, s76, v214\n"
"MA v,124,a,184,a,76\n"
"v_mul_f32 v215, s76, v215\n"
"CV 212,212,213\n"
"MA v,124,a,188,a,92\n"
"CV 214,214,215\n"
"v_accvgpr_write_b32 a26, v212\n"
"MA v,120,a,188,a,88\n"
"v_accvgpr_write_b32 a27, v214\n"
"v_mul_f32 v200, s76, v200\n"
"MA v,120,a,184,a,72\n"
"v_mul_f32 v201, s76, v201\n"
"v_mul_f32 v202, s76, v202\n"
"MA v,116,a,184,a,68\n"
"v_mul_f32 v203, s76, v203\n"
"CV 200,200,201\n"
"MA v,116,a,188,a,84\n"
"CV 202,202,203\n"
"v_accvgpr_write_b32 a40, v200\n"
"MA v,112,a,188,a,80\n"
"v_accvgpr_write_b32 a41, v202\n"
"v_mul_f32 v216, s76, v216\n"
"MA v,112,a,184,a,64\n"
"v_mul_f32 v217, s76, v217\n"
"v_mul_f32 v218, s76, v218\n"
"RD 128,249,0\n"
"RD 132,250,0\n"
"RD 136,249,1024\n"
"RD 140,250,1024\n"
"RD 144,249,4096\n"
"RD 148,250,4096\n"
"RD 152,249,5120\n"
"RD 156,250,5120\n"
"v_mul_f32 v219, s76, v219\n"
"CV 216,216,217\n"
"CV 218,218,219\n"
"v_accvgpr_write_b32 a42, v216\n"
"v_accvgpr_write_b32 a43, v218\n"
"v_mul_f32 v204, s76, v204\n"
"v_mul_f32 v205, s76, v205\n"
"v_mul_f32 v206, s76, v206\n"
"v_mul_f32 v207, s76, v207\n"
"CV 204,204,205\n"
"CV 206,206,207\n"
"v_accvgpr_write_b32 a56, v204\n"
"v_accvgpr_write_b32 a57, v206\n"
"v_mul_f32 v220, s76, v220\n"
"v_mul_f32 v221, s76, v221\n"
"v_mul_f32 v222, s76, v222\n"
"v_mul_f32 v223, s76, v223\n"
"CV 220,220,221\n"
"CV 222,222,223\n"
"v_accvgpr_write_b32 a58, v220\n"
"v_accvgpr_write_b32 a59, v222\n"
"s_add_u32 s26, s70, s71\n"
"s_and_b32 s26, s26, 0x3ffff\n"
"s_and_b32 s81, s70, 0xfffc0000\n"
"s_or_b32 s26, s26, s81\n"
"s_add_u32 s70, s70, 8192\n"
"s_add_u32 m0, s69, 122880\n"
"s_nop 0\n"
"buffer_load_dwordx4 v254, s[48:51], s27 offen nt lds\n"
"v_pk_mul_f32 v[224:225], v[224:225], s[76:77]\n"
"v_pk_mul_f32 v[226:227], v[226:227], s[76:77]\n"
"CV 224,224,225\n"
"CV 226,226,227\n"
"v_accvgpr_write_b32 a12, v224\n"
"v_accvgpr_write_b32 a13, v226\n"
"v_accvgpr_read_b32 v132, a240\n"
"v_accvgpr_read_b32 v133, a241\n"
"v_accvgpr_read_b32 v134, a242\n"
"v_accvgpr_read_b32 v135, a243\n"
"v_pk_mul_f32 v[132:133], v[132:133], s[76:77]\n"
"v_pk_mul_f32 v[134:135], v[134:135], s[76:77]\n"
"CV 132,132,133\n"
"CV 134,134,135\n"
"v_accvgpr_write_b32 a14, v132\n"
"v_accvgpr_write_b32 a15, v134\n"
"v_pk_mul_f32 v[228:229], v[228:229], s[76:77]\n"
"v_pk_mul_f32 v[230:231], v[230:231], s[76:77]\n"
"CV 228,228,229\n"
"CV 230,230,231\n"
"v_accvgpr_write_b32 a28, v228\n"
"v_accvgpr_write_b32 a29, v230\n"
"v_accvgpr_read_b32 v140, a244\n"
"v_accvgpr_read_b32 v141, a245\n"
"v_accvgpr_read_b32 v142, a246\n"
"v_accvgpr_read_b32 v143, a247\n"
"v_pk_mul_f32 v[140:141], v[140:141], s[76:77]\n"
"v_pk_mul_f32 v[142:143], v[142:143], s[76:77]\n"
"CV 140,140,141\n"
"CV 142,142,143\n"
"v_accvgpr_write_b32 a30, v140\n"
"v_accvgpr_write_b32 a31, v142\n"
"s_add_u32 m0, s69, 123776\n"
"s_nop 0\n"
"buffer_load_dwordx4 v254, s[48:51], s27 offen offset:128 nt lds\n"
"v_pk_mul_f32 v[232:233], v[232:233], s[76:77]\n"
"v_pk_mul_f32 v[234:235], v[234:235], s[76:77]\n"
"CV 232,232,233\n"
"CV 234,234,235\n"
"v_accvgpr_write_b32 a44, v232\n"
"v_accvgpr_write_b32 a45, v234\n"
"v_accvgpr_read_b32 v148, a248\n"
"v_accvgpr_read_b32 v149, a249\n"
"v_accvgpr_read_b32 v150, a250\n"
"v_accvgpr_read_b32 v151, a251\n"
"v_pk_mul_f32 v[148:149], v[148:149], s[76:77]\n"
"v_pk_mul_f32 v[150:151], v[150:151], s[76:77]\n"
"CV 148,148,149\n"
"CV 150,150,151\n"
"v_accvgpr_write_b32 a46, v148\n"
"v_accvgpr_write_b32 a47, v150\n"
"v_accvgpr_read_b32 v152, a236\n"
"v_accvgpr_read_b32 v153, a237\n"
"v_accvgpr_read_b32 v154, a238\n"
"v_accvgpr_read_b32 v155, a239\n"
"v_pk_mul_f32 v[152:153], v[152:153], s[76:77]\n"
"v_pk_mul_f32 v[154:155], v[154:155], s[76:77]\n"
"CV 152,152,153\n"
"CV 154,154,155\n"
"v_accvgpr_write_b32 a60, v152\n"
"v_accvgpr_write_b32 a61, v154\n"
"v_accvgpr_read_b32 v156, a252\n"
"v_accvgpr_read_b32 v157, a253\n"
"v_accvgpr_read_b32 v158, a254\n"
"v_accvgpr_read_b32 v159, a255\n"
"v_pk_mul_f32 v[156:157], v[156:157], s[76:77]\n"
"v_pk_mul_f32 v[158:159], v[158:159], s[76:77]\n"
"CV 156,156,157\n"
"CV 158,158,159\n"
"v_accvgpr_write_b32 a62, v156\n"
"v_accvgpr_write_b32 a63, v158\n"
"s_add_u32 m0, s69, 40960\n"
"s_nop 0\n"
"buffer_load_dwordx4 v253, s[44:47], s25 offen lds\n"
"s_add_u32 m0, s69, 41856\n"
"s_nop 0\n"
"buffer_load_dwordx4 v253, s[44:47], s25 offen offset:128 lds\n"
"s_add_u32 s27, s70, s71\n"
"s_and_b32 s27, s27, 0x3ffff\n"
"s_and_b32 s81, s70, 0xfffc0000\n"
"s_or_b32 s27, s27, s81\n"
"s_add_u32 s70, s70, 8192\n"
"v_mov_b32 v128, s28\n"
"v_mov_b32 v129, s29\n"
"v_accvgpr_write_b32 a252, v128\n"
"v_accvgpr_write_b32 a253, v129\n"
"s_cmp_eq_u32 s74, 0\n"
"v_mov_b32 v128, 0xff800000\n"
"v_accvgpr_write_b32 a228, v128\n"
"v_accvgpr_write_b32 a229, v128\n"
"s_waitcnt vmcnt(8)\n"
"v_accvgpr_write_b32 a230, v128\n"
"v_accvgpr_write_b32 a231, v128\n"
"v_accvgpr_write_b32 a232, v128\n"
"v_accvgpr_write_b32 a233, v128\n"
"v_accvgpr_write_b32 a234, v128\n"
"v_accvgpr_write_b32 a235, v128\n"
"s_cbranch_scc0 L_SWEEP0_%=\n"
"L_NOSWEEP_%=:\n"
"s_nop 1\n"
"s_waitcnt lgkmcnt(7)\n"
"MZ v,128,a,128,a,0\n"
"s_waitcnt lgkmcnt(6)\n"
"v_bfe_u32 v176, v255, 4, 4\n"
"v_mov_b32 v177, 0x3f803f80\n"
"MA v,128,a,132,a,4\n"
"s_waitcnt lgkmcnt(5)\n"
"v_cmp_eq_u32 vcc, 0, v176\n"
"v_mul_f32 v0, s76, v0\n"
"MA v,128,a,136,a,8\n"
"s_waitcnt lgkmcnt(4)\n"
"s_add_u32 m0, s69, 65536\n"
"v_mul_f32 v1, s76, v1\n"
"v_cndmask_b32 v178, 0, v177, vcc\n"
"MA v,128,a,140,a,12\n"
"buffer_load_dwordx4 v254, s[48:51], s24 offen nt lds\n"
"v_cmp_eq_u32 vcc, 1, v176\n"
"MZ v,132,a,140,a,28\n"
"v_accvgpr_write_b32 a236, v178\n"
"v_accvgpr_write_b32 a237, v178\n"
"MA v,132,a,136,a,24\n"
"v_accvgpr_write_b32 a238, v178\n"
"v_accvgpr_write_b32 a239, v178\n"
"MA v,132,a,132,a,20\n"
"v_cndmask_b32 v178, 0, v177, vcc\n"
"v_cmp_eq_u32 vcc, 2, v176\n"
"MA v,132,a,128,a,16\n"
"v_accvgpr_write_b32 a240, v178\n"
"v_accvgpr_write_b32 a241, v178\n"
"MZ v,136,a,128,a,32\n"
"v_accvgpr_write_b32 a242, v178\n"
"v_accvgpr_write_b32 a243, v178\n"
"MA v,136,a,132,a,36\n"
"v_cndmask_b32 v178, 0, v177, vcc\n"
"v_cmp_eq_u32 vcc, 3, v176\n"
"MA v,136,a,136,a,40\n"
"s_add_u32 m0, s69, 66432\n"
"v_accvgpr_write_b32 a244, v178\n"
"v_accvgpr_write_b32 a245, v178\n"
"MA v,136,a,140,a,44\n"
"buffer_load_dwordx4 v254, s[48:51], s24 offen offset:128 nt lds\n"
"v_accvgpr_write_b32 a246, v178\n"
"MZ v,140,a,140,a,60\n"
"v_accvgpr_write_b32 a247, v178\n"
"v_cndmask_b32 v178, 0, v177, vcc\n"
"MA v,140,a,136,a,56\n"
"v_accvgpr_write_b32 a248, v178\n"
"v_accvgpr_write_b32 a249, v178\n"
"MA v,140,a,132,a,52\n"
"v_accvgpr_write_b32 a250, v178\n"
"v_accvgpr_write_b32 a251, v178\n"
"MA v,140,a,128,a,48\n"
"s_waitcnt lgkmcnt(3)\n"
"v_mul_f32 v2, s76, v2\n"
"v_mul_f32 v3, s76, v3\n"
"MZ v,156,a,144,a,48\n"
"s_waitcnt lgkmcnt(2)\n"
"CV 0,0,1\n"
"CV 2,2,3\n"
"MA v,156,a,148,a,52\n"
"s_waitcnt lgkmcnt(1)\n"
"v_accvgpr_write_b32 a64, v0\n"
"v_accvgpr_write_b32 a65, v2\n"
"MA v,156,a,152,a,56\n"
"s_waitcnt lgkmcnt(0)\n"
"s_add_u32 m0, s69, 49152\n"
"v_mul_f32 v16, s76, v16\n"
"v_mul_f32 v17, s76, v17\n"
"MA v,156,a,156,a,60\n"
"buffer_load_dwordx4 v253, s[44:47], s26 offen lds\n"
"v_mul_f32 v18, s76, v18\n"
"MZ v,152,a,156,a,44\n"
"v_max3_f32 v224, v128, v129, v130\n"
"v_max3_f32 v228, v132, v133, v134\n"
"MA v,152,a,152,a,40\n"
"v_max3_f32 v232, v136, v137, v138\n"
"v_max3_f32 v236, v140, v141, v142\n"
"MA v,152,a,148,a,36\n"
"v_accvgpr_read_b32 v225, a228\n"
"v_accvgpr_read_b32 v229, a229\n"
"MA v,152,a,144,a,32\n"
"v_accvgpr_read_b32 v233, a230\n"
"v_accvgpr_read_b32 v237, a231\n"
"MZ v,148,a,144,a,16\n"
"v_max3_f32 v224, v224, v131, v225\n"
"v_max3_f32 v228, v228, v135, v229\n"
"MA v,148,a,148,a,20\n"
"v_max3_f32 v232, v232, v139, v233\n"
"v_max3_f32 v236, v236, v143, v237\n"
"MA v,148,a,152,a,24\n"
"s_add_u32 m0, s69, 50048\n"
"v_mov_b32 v225, v224\n"
"v_mov_b32 v229, v228\n"
"MA v,148,a,156,a,28\n"
"buffer_load_dwordx4 v253, s[44:47], s26 offen offset:128 lds\n"
"v_mov_b32 v233, v232\n"
"MZ v,144,a,156,a,12\n"
"v_mov_b32 v237, v236\n"
"v_permlane32_swap_b32 v225, v224\n"
"MA v,144,a,152,a,8\n"
"v_permlane32_swap_b32 v229, v228\n"
"v_permlane32_swap_b32 v233, v232\n"
"MA v,144,a,148,a,4\n"
"v_permlane32_swap_b32 v237, v236\n"
"v_max_f32 v224, v224, v225\n"
"MA v,144,a,144,a,0\n"
"v_max_f32 v228, v228, v229\n"
"v_max_f32 v232, v232, v233\n"
"v_max_f32 v236, v236, v237\n"
"v_mov_b32 v225, v224\n"
"v_mov_b32 v229, v228\n"
"v_mov_b32 v233, v232\n"
"v_mov_b32 v237, v236\n"
"v_permlane16_swap_b32 v225, v224\n"
"v_permlane16_swap_b32 v229, v228\n"
"v_permlane16_swap_b32 v233, v232\n"
"v_permlane16_swap_b32 v237, v236\n"
"v_max_f32 v224, v224, v225\n"
"v_max_f32 v228, v228, v229\n"
"v_max_f32 v232, v232, v233\n"
"v_max_f32 v236, v236, v237\n"
"v_sub_f32 v227, 0, v224\n"
"v_sub_f32 v231, 0, v228\n"
"v_sub_f32 v235, 0, v232\n"
"v_sub_f32 v239, 0, v236\n"
"v_sub_f32 v226, 0, v224\n"
"v_sub_f32 v230, 0, v228\n"
"v_sub_f32 v234, 0, v232\n"
"v_sub_f32 v238, 0, v236\n"
"v_sub_f32 v225, 0, v224\n"
"v_sub_f32 v229, 0, v228\n"
"v_sub_f32 v233, 0, v232\n"
"v_sub_f32 v237, 0, v236\n"
"v_sub_f32 v224, 0, v224\n"
"v_sub_f32 v228, 0, v228\n"
"v_sub_f32 v232, 0, v232\n"
"v_sub_f32 v236, 0, v236\n"
"s_waitcnt vmcnt(63)\n"
"s_barrier\n"
"RD 128,249,8192\n"
"RD 132,250,8192\n"
"RD 136,249,9216\n"
"RD 140,250,9216\n"
"AD 128,224\n"
"AD 129,224\n"
"AD 130,224\n"
"AD 131,224\n"
"AD 132,228\n"
"AD 133,228\n"
"AD 134,228\n"
"AD 135,228\n"
"AD 136,232\n"
"AD 137,232\n"
"AD 138,232\n"
"AD 139,232\n"
"AD 140,236\n"
"AD 141,236\n"
"AD 142,236\n"
"AD 143,236\n"
"AD 156,236\n"
"AD 157,236\n"
"AD 158,236\n"
"AD 159,236\n"
"AD 152,232\n"
"AD 153,232\n"
"AD 154,232\n"
"AD 155,232\n"
"AD 148,228\n"
"AD 149,228\n"
"AD 150,228\n"
"AD 151,228\n"
"AD 144,224\n"
"AD 145,224\n"
"AD 146,224\n"
"AD 147,224\n"
"EX 128\n"
"EX 129\n"
"EX 130\n"
"EX 131\n"
"EX 132\n"
"EX 133\n"
"EX 134\n"
"EX 135\n"
"EX 136\n"
"EX 137\n"
"EX 138\n"
"EX 139\n"
"EX 140\n"
"EX 141\n"
"EX 142\n"
"EX 143\n"
"EX 156\n"
"EX 157\n"
"EX 158\n"
"EX 159\n"
"EX 152\n"
"EX 153\n"
"EX 154\n"
"EX 155\n"
"EX 148\n"
"EX 149\n"
"EX 150\n"
"EX 151\n"
"CV 160,128,129\n"
"CV 161,130,131\n"
"CV 164,132,133\n"
"CV 165,134,135\n"
"CV 168,136,137\n"
"CV 169,138,139\n"
"CV 172,140,141\n"
"CV 173,142,143\n"
"CV 174,156,157\n"
"CV 175,158,159\n"
"CV 170,152,153\n"
"CV 171,154,155\n"
"CV 166,148,149\n"
"CV 167,150,151\n"
"s_mov_b32 s73, 0\n"
"s_waitcnt lgkmcnt(3)\n"
"MC 128,128,0,224\n"
"s_waitcnt lgkmcnt(2)\n"
"EX 144\n"
"MA v,128,a,132,a,4\n"
"s_waitcnt lgkmcnt(1)\n"
"RD 144,249,12288\n"
"v_mul_f32 v19, s76, v19\n"
"MA v,128,a,136,a,8\n"
"s_waitcnt lgkmcnt(1)\n"
"EX 145\n"
"MA v,128,a,140,a,12\n"
"RD 148,250,12288\n"
"CV 162,144,145\n"
"MC 132,140,28,228\n"
"EX 146\n"
"MA v,132,a,136,a,24\n"
"RD 152,249,13312\n"
"CV 16,16,17\n"
"MA v,132,a,132,a,20\n"
"EX 147\n"
"MA v,132,a,128,a,16\n"
"RD 156,250,13312\n"
"CV 163,146,147\n"
"MC 136,128,32,232\n"
"s_add_u32 m0, s69, 73728\n"
"EX 128\n"
"MA v,136,a,132,a,36\n"
"buffer_load_dwordx4 v254, s[48:51], s25 offen nt lds\n"
"CV 18,18,19\n"
"MA v,136,a,136,a,40\n"
"EX 129\n"
"MA v,136,a,140,a,44\n"
"CV 176,128,129\n"
"v_accvgpr_write_b32 a66, v16\n"
"MC 140,140,60,236\n"
"EX 130\n"
"MA v,140,a,136,a,56\n"
"v_accvgpr_write_b32 a67, v18\n"
"v_mul_f32 v4, s76, v4\n"
"MA v,140,a,132,a,52\n"
"EX 131\n"
"MA v,140,a,128,a,48\n"
"s_waitcnt lgkmcnt(0)\n"
"CV 177,130,131\n"
"v_mul_f32 v5, s76, v5\n"
"MC 156,144,48,236\n"
"EX 132\n"
"MA v,156,a,148,a,52\n"
"v_mul_f32 v6, s76, v6\n"
"v_mul_f32 v7, s76, v7\n"
"MA v,156,a,152,a,56\n"
"EX 133\n"
"MA v,156,a,156,a,60\n"
"CV 180,132,133\n"
"CV 4,4,5\n"
"MC 152,156,44,232\n"
"s_add_u32 m0, s69, 74624\n"
"EX 134\n"
"MA v,152,a,152,a,40\n"
"buffer_load_dwordx4 v254, s[48:51], s25 offen offset:128 nt lds\n"
"CV 6,6,7\n"
"MA v,152,a,148,a,36\n"
"s_add_u32 s24, s70, s71\n"
"EX 135\n"
"MA v,152,a,144,a,32\n"
"CV 181,134,135\n"
"v_accvgpr_write_b32 a80, v4\n"
"MC 148,144,16,228\n"
"s_and_b32 s24, s24, 0x3ffff\n"
"EX 136\n"
"MA v,148,a,148,a,20\n"
"v_accvgpr_write_b32 a81, v6\n"
"v_mul_f32 v20, s76, v20\n"
"MA v,148,a,152,a,24\n"
"s_and_b32 s81, s70, 0xfffc0000\n"
"EX 137\n"
"MA v,148,a,156,a,28\n"
"CV 184,136,137\n"
"v_mul_f32 v21, s76, v21\n"
"MC 144,156,12,224\n"
"EX 138\n"
"MA v,144,a,152,a,8\n"
"v_mul_f32 v22, s76, v22\n"
"v_mul_f32 v23, s76, v23\n"
"MA v,144,a,148,a,4\n"
"s_or_b32 s24, s24, s81\n"
"EX 139\n"
"MA v,144,a,144,a,0\n"
"CV 185,138,139\n"
"CV 20,20,21\n"
"MZ v,240,v,160,a,236\n"
"L_ST5_%=:\n"
"s_cbranch_execz L_VB1_%=\n"
"EX 140\n"
"MA v,240,v,164,a,240\n"
"CV 22,22,23\n"
"v_accvgpr_write_b32 a82, v20\n"
"MA v,240,v,168,a,244\n"
"s_add_u32 s70, s70, 8192\n"
"EX 141\n"
"MA v,240,v,172,a,248\n"
"CV 188,140,141\n"
"v_accvgpr_write_b32 a83, v22\n"
"EX 142\n"
"EX 143\n"
"EX 156\n"
"EX 157\n"
"s_add_u32 m0, s69, 57344\n"
"s_nop 0\n"
"buffer_load_dwordx4 v253, s[44:47], s27 offen lds\n"
"s_add_u32 m0, s69, 58240\n"
"s_nop 0\n"
"buffer_load_dwordx4 v253, s[44:47], s27 offen offset:128 lds\n"
"s_memrealtime vcc\n"
"s_waitcnt lgkmcnt(0)\n"
"s_mov_b32 s77, 153\n"
"s_add_u32 s72, vcc_lo, s77\n"
"s_mov_b32 s15, 0\n"
"s_waitcnt vmcnt(14)\n"
"s_barrier\n"
"RD 128,249,16384\n"
"RD 132,250,16384\n"
"RD 136,249,17408\n"
"RD 140,250,17408\n"
"EX 158\n"
"EX 159\n"
"EX 152\n"
"EX 153\n"
"EX 154\n"
"EX 155\n"
"EX 148\n"
"EX 149\n"
"EX 150\n"
"EX 151\n"
"CV 189,142,143\n"
"CV 190,156,157\n"
"CV 191,158,159\n"
"CV 186,152,153\n"
"CV 187,154,155\n"
"CV 182,148,149\n"
"CV 183,150,151\n"
"s_waitcnt lgkmcnt(3)\n"
"MC 128,128,0,224\n"
"s_waitcnt lgkmcnt(2)\n"
"EX 144\n"
"v_mul_f32 v8, s76, v8\n"
"v_mul_f32 v9, s76, v9\n"
"MA v,128,a,132,a,4\n"
"s_waitcnt lgkmcnt(1)\n"
"RD 144,249,20480\n"
"v_mul_f32 v10, s76, v10\n"
"v_mul_f32 v11, s76, v11\n"
"CV 8,8,9\n"
"MA v,128,a,136,a,8\n"
"s_waitcnt lgkmcnt(1)\n"
"EX 145\n"
"CV 10,10,11\n"
"v_accvgpr_write_b32 a96, v8\n"
"MA v,128,a,140,a,12\n"
"RD 148,250,20480\n"
"CV 178,144,145\n"
"v_accvgpr_write_b32 a97, v10\n"
"v_mul_f32 v24, s76, v24\n"
"MC 132,140,28,228\n"
"EX 146\n"
"v_mul_f32 v25, s76, v25\n"
"v_mul_f32 v26, s76, v26\n"
"MA v,132,a,136,a,24\n"
"RD 152,249,21504\n"
"v_mul_f32 v27, s76, v27\n"
"CV 24,24,25\n"
"CV 26,26,27\n"
"MA v,132,a,132,a,20\n"
"EX 147\n"
"v_accvgpr_write_b32 a98, v24\n"
"v_accvgpr_write_b32 a99, v26\n"
"MA v,132,a,128,a,16\n"
"RD 156,250,21504\n"
"CV 179,146,147\n"
"v_mul_f32 v12, s76, v12\n"
"v_mul_f32 v13, s76, v13\n"
"MC 136,128,32,232\n"
"s_add_u32 m0, s69, 81920\n"
"EX 128\n"
"v_mul_f32 v14, s76, v14\n"
"v_mul_f32 v15, s76, v15\n"
"MA v,136,a,132,a,36\n"
"buffer_load_dwordx4 v254, s[48:51], s26 offen nt lds\n"
"CV 12,12,13\n"
"CV 14,14,15\n"
"v_accvgpr_write_b32 a112, v12\n"
"MA v,136,a,136,a,40\n"
"EX 129\n"
"v_accvgpr_write_b32 a113, v14\n"
"v_mul_f32 v28, s76, v28\n"
"MA v,136,a,140,a,44\n"
"TR 160,251,32768\n"
"CV 192,128,129\n"
"v_mul_f32 v29, s76, v29\n"
"v_mul_f32 v30, s76, v30\n"
"MC 140,140,60,236\n"
"EX 130\n"
"v_mul_f32 v31, s76, v31\n"
"CV 28,28,29\n"
"MA v,140,a,136,a,56\n"
"TR 162,251,36864\n"
"CV 30,30,31\n"
"v_accvgpr_write_b32 a114, v28\n"
"v_accvgpr_write_b32 a115, v30\n"
"MA v,140,a,132,a,52\n"
"EX 131\n"
"v_mul_f32 v32, s76, v32\n"
"v_mul_f32 v33, s76, v33\n"
"MA v,140,a,128,a,48\n"
"s_waitcnt lgkmcnt(2)\n"
"TR 164,251,40960\n"
"CV 193,130,131\n"
"v_mul_f32 v34, s76, v34\n"
"v_mul_f32 v35, s76, v35\n"
"MC 156,144,48,236\n"
"EX 132\n"
"CV 32,32,33\n"
"CV 34,34,35\n"
"MA v,156,a,148,a,52\n"
"TR 166,251,45056\n"
"v_accvgpr_write_b32 a68, v32\n"
"v_accvgpr_write_b32 a69, v34\n"
"v_mul_f32 v48, s76, v48\n"
"MA v,156,a,152,a,56\n"
"EX 133\n"
"v_mul_f32 v49, s76, v49\n"
"v_mul_f32 v50, s76, v50\n"
"MA v,156,a,156,a,60\n"
"TR 168,252,32768\n"
"CV 196,132,133\n"
"v_mul_f32 v51, s76, v51\n"
"CV 48,48,49\n"
"MC 152,156,44,232\n"
"s_add_u32 m0, s69, 82816\n"
"EX 134\n"
"CV 50,50,51\n"
"v_accvgpr_write_b32 a70, v48\n"
"MA v,152,a,152,a,40\n"
"buffer_load_dwordx4 v254, s[48:51], s26 offen offset:128 nt lds\n"
"v_accvgpr_write_b32 a71, v50\n"
"v_mul_f32 v36, s76, v36\n"
"v_mul_f32 v37, s76, v37\n"
"MA v,152,a,148,a,36\n"
"s_add_u32 s25, s70, s71\n"
"EX 135\n"
"v_mul_f32 v38, s76, v38\n"
"v_mul_f32 v39, s76, v39\n"
"MA v,152,a,144,a,32\n"
"TR 170,252,36864\n"
"CV 197,134,135\n"
"CV 36,36,37\n"
"CV 38,38,39\n"
"MC 148,144,16,228\n"
"s_and_b32 s25, s25, 0x3ffff\n"
"EX 136\n"
"v_accvgpr_write_b32 a84, v36\n"
"v_accvgpr_write_b32 a85, v38\n"
"MA v,148,a,148,a,20\n"
"TR 172,252,40960\n"
"v_mul_f32 v52, s76, v52\n"
"v_mul_f32 v53, s76, v53\n"
"v_mul_f32 v54, s76, v54\n"
"MA v,148,a,152,a,24\n"
"s_and_b32 s81, s70, 0xfffc0000\n"
"EX 137\n"
"v_mul_f32 v55, s76, v55\n"
"CV 52,52,53\n"
"MA v,148,a,156,a,28\n"
"TR 174,252,45056\n"
"CV 200,136,137\n"
"CV 54,54,55\n"
"v_accvgpr_write_b32 a86, v52\n"
"MC 144,156,12,224\n"
"EX 138\n"
"v_accvgpr_write_b32 a87, v54\n"
"v_mul_f32 v40, s76, v40\n"
"MA v,144,a,152,a,8\n"
"TR 176,251,33280\n"
"v_mul_f32 v41, s76, v41\n"
"v_mul_f32 v42, s76, v42\n"
"v_mul_f32 v43, s76, v43\n"
"MA v,144,a,148,a,4\n"
"s_or_b32 s25, s25, s81\n"
"EX 139\n"
"CV 40,40,41\n"
"CV 42,42,43\n"
"MA v,144,a,144,a,0\n"
"s_waitcnt lgkmcnt(5)\n"
"TR 178,251,37376\n"
"CV 201,138,139\n"
"v_accvgpr_write_b32 a100, v40\n"
"v_accvgpr_write_b32 a101, v42\n"
"MZ v,0,v,160,a,160\n"
"EX 140\n"
"v_mul_f32 v56, s76, v56\n"
"v_mul_f32 v57, s76, v57\n"
"MA v,0,v,176,a,164\n"
"TR 180,251,41472\n"
"v_mul_f32 v58, s76, v58\n"
"v_mul_f32 v59, s76, v59\n"
"CV 56,56,57\n"
"MZ v,4,v,180,a,164\n"
"s_add_u32 s70, s70, 8192\n"
"EX 141\n"
"CV 58,58,59\n"
"v_accvgpr_write_b32 a102, v56\n"
"MA v,4,v,164,a,160\n"
"TR 182,251,45568\n"
"CV 204,140,141\n"
"v_accvgpr_write_b32 a103, v58\n"
"v_mul_f32 v44, s76, v44\n"
"MZ v,8,v,168,a,160\n"
"s_add_u32 m0, s69, 0\n"
"EX 142\n"
"v_mul_f32 v45, s76, v45\n"
"v_mul_f32 v46, s76, v46\n"
"MA v,8,v,184,a,164\n"
"buffer_load_dwordx4 v253, s[44:47], s24 offen nt lds\n"
"v_mul_f32 v47, s76, v47\n"
"CV 44,44,45\n"
"CV 46,46,47\n"
"MZ v,12,v,188,a,164\n"
"EX 143\n"
"v_accvgpr_write_b32 a116, v44\n"
"v_accvgpr_write_b32 a117, v46\n"
"MA v,12,v,172,a,160\n"
"s_waitcnt lgkmcnt(4)\n"
"TR 184,252,33280\n"
"CV 205,142,143\n"
"v_mul_f32 v60, s76, v60\n"
"v_mul_f32 v61, s76, v61\n"
"MZ v,28,v,172,a,168\n"
"EX 156\n"
"v_mul_f32 v62, s76, v62\n"
"v_mul_f32 v63, s76, v63\n"
"MA v,28,v,188,a,172\n"
"TR 186,252,37376\n"
"CV 60,60,61\n"
"CV 62,62,63\n"
"v_accvgpr_write_b32 a118, v60\n"
"MZ v,24,v,184,a,172\n"
"EX 157\n"
"v_accvgpr_write_b32 a119, v62\n"
"v_mul_f32 v64, s76, v64\n"
"MA v,24,v,168,a,168\n"
"CV 206,156,157\n"
"v_mul_f32 v65, s76, v65\n"
"v_mul_f32 v66, s76, v66\n"
"v_mul_f32 v67, s76, v67\n"
"MZ v,20,v,164,a,168\n"
"EX 158\n"
"CV 64,64,65\n"
"CV 66,66,67\n"
"MA v,20,v,180,a,172\n"
"TR 188,252,41472\n"
"v_accvgpr_write_b32 a72, v64\n"
"v_accvgpr_write_b32 a73, v66\n"
"v_mul_f32 v80, s76, v80\n"
"MZ v,16,v,176,a,172\n"
"EX 159\n"
"v_mul_f32 v81, s76, v81\n"
"v_mul_f32 v82, s76, v82\n"
"MA v,16,v,160,a,168\n"
"s_waitcnt lgkmcnt(3)\n"
"TR 190,252,45568\n"
"CV 207,158,159\n"
"v_mul_f32 v83, s76, v83\n"
"CV 80,80,81\n"
"MZ v,32,v,160,a,176\n"
"s_add_u32 m0, s69, 896\n"
"EX 152\n"
"CV 82,82,83\n"
"v_accvgpr_write_b32 a74, v80\n"
"MA v,32,v,176,a,180\n"
"buffer_load_dwordx4 v253, s[44:47], s24 offen offset:128 nt lds\n"
"v_accvgpr_write_b32 a75, v82\n"
"v_mul_f32 v68, s76, v68\n"
"v_mul_f32 v69, s76, v69\n"
"MZ v,36,v,180,a,180\n"
"EX 153\n"
"v_mul_f32 v70, s76, v70\n"
"v_mul_f32 v71, s76, v71\n"
"MA v,36,v,164,a,176\n"
"CV 202,152,153\n"
"CV 68,68,69\n"
"CV 70,70,71\n"
"v_accvgpr_write_b32 a88, v68\n"
"MZ v,40,v,168,a,176\n"
"EX 154\n"
"v_accvgpr_write_b32 a89, v70\n"
"v_mul_f32 v84, s76, v84\n"
"MA v,40,v,184,a,180\n"
"v_mul_f32 v85, s76, v85\n"
"v_mul_f32 v86, s76, v86\n"
"v_mul_f32 v87, s76, v87\n"
"CV 84,84,85\n"
"MZ v,44,v,188,a,180\n"
"EX 155\n"
"CV 86,86,87\n"
"v_accvgpr_write_b32 a90, v84\n"
"MA v,44,v,172,a,176\n"
"s_waitcnt lgkmcnt(0)\n"
"RD 128,249,24576\n"
"CV 203,154,155\n"
"v_accvgpr_write_b32 a91, v86\n"
"v_mul_f32 v72, s76, v72\n"
"MZ v,60,v,172,a,184\n"
"EX 148\n"
"v_mul_f32 v73, s76, v73\n"
"v_mul_f32 v74, s76, v74\n"
"MA v,60,v,188,a,188\n"
"RD 132,250,24576\n"
"v_mul_f32 v75, s76, v75\n"
"CV 72,72,73\n"
"CV 74,74,75\n"
"MZ v,56,v,184,a,188\n"
"EX 149\n"
"v_accvgpr_write_b32 a104, v72\n"
"v_accvgpr_write_b32 a105, v74\n"
"MA v,56,v,168,a,184\n"
"RD 136,249,25600\n"
"CV 198,148,149\n"
"MZ v,52,v,164,a,184\n"
"EX 150\n"
"MA v,52,v,180,a,188\n"
"RD 140,250,25600\n"
"MZ v,48,v,176,a,188\n"
"EX 151\n"
"MA v,48,v,160,a,184\n"
"CV 199,150,151\n"
"MA v,240,v,176,a,236\n"
"MA v,240,v,180,a,240\n"
"MA v,240,v,184,a,244\n"
"MA v,240,v,188,a,248\n"
"s_waitcnt lgkmcnt(0)\n"
"MC 128,128,0,224\n"
"EX 144\n"
"v_mul_f32 v88, s76, v88\n"
"v_mul_f32 v89, s76, v89\n"
"MA v,128,a,132,a,4\n"
"RD 144,249,28672\n"
"v_mul_f32 v90, s76, v90\n"
"v_mul_f32 v91, s76, v91\n"
"CV 88,88,89\n"
"MA v,128,a,136,a,8\n"
"EX 145\n"
"CV 90,90,91\n"
"v_accvgpr_write_b32 a106, v88\n"
"MA v,128,a,140,a,12\n"
"RD 148,250,28672\n"
"CV 194,144,145\n"
"v_accvgpr_write_b32 a107, v90\n"
"v_mul_f32 v76, s76, v76\n"
"MC 132,140,28,228\n"
"EX 146\n"
"v_mul_f32 v77, s76, v77\n"
"v_mul_f32 v78, s76, v78\n"
"MA v,132,a,136,a,24\n"
"RD 152,249,29696\n"
"v_mul_f32 v79, s76, v79\n"
"CV 76,76,77\n"
"CV 78,78,79\n"
"MA v,132,a,132,a,20\n"
"EX 147\n"
"v_accvgpr_write_b32 a120, v76\n"
"v_accvgpr_write_b32 a121, v78\n"
"MA v,132,a,128,a,16\n"
"RD 156,250,29696\n"
"CV 195,146,147\n"
"v_mul_f32 v92, s76, v92\n"
"v_mul_f32 v93, s76, v93\n"
"MC 136,128,32,232\n"
"s_add_u32 m0, s69, 90112\n"
"EX 128\n"
"v_mul_f32 v94, s76, v94\n"
"v_mul_f32 v95, s76, v95\n"
"MA v,136,a,132,a,36\n"
"buffer_load_dwordx4 v254, s[48:51], s27 offen nt lds\n"
"CV 92,92,93\n"
"CV 94,94,95\n"
"v_accvgpr_write_b32 a122, v92\n"
"MA v,136,a,136,a,40\n"
"EX 129\n"
"v_accvgpr_write_b32 a123, v94\n"
"v_mul_f32 v96, s76, v96\n"
"MA v,136,a,140,a,44\n"
"TR 160,251,33792\n"
"CV 208,128,129\n"
"v_mul_f32 v97, s76, v97\n"
"v_mul_f32 v98, s76, v98\n"
"MC 140,140,60,236\n"
"EX 130\n"
"v_mul_f32 v99, s76, v99\n"
"CV 96,96,97\n"
"MA v,140,a,136,a,56\n"
"TR 162,251,37888\n"
"CV 98,98,99\n"
"v_accvgpr_write_b32 a76, v96\n"
"v_accvgpr_write_b32 a77, v98\n"
"MA v,140,a,132,a,52\n"
"EX 131\n"
"v_mul_f32 v112, s76, v112\n"
"v_mul_f32 v113, s76, v113\n"
"MA v,140,a,128,a,48\n"
"s_waitcnt lgkmcnt(2)\n"
"TR 164,251,41984\n"
"CV 209,130,131\n"
"v_mul_f32 v114, s76, v114\n"
"v_mul_f32 v115, s76, v115\n"
"MC 156,144,48,236\n"
"EX 132\n"
"CV 112,112,113\n"
"CV 114,114,115\n"
"MA v,156,a,148,a,52\n"
"TR 166,251,46080\n"
"v_accvgpr_write_b32 a78, v112\n"
"v_accvgpr_write_b32 a79, v114\n"
"v_mul_f32 v100, s76, v100\n"
"MA v,156,a,152,a,56\n"
"EX 133\n"
"v_mul_f32 v101, s76, v101\n"
"v_mul_f32 v102, s76, v102\n"
"MA v,156,a,156,a,60\n"
"TR 168,252,33792\n"
"CV 212,132,133\n"
"v_mul_f32 v103, s76, v103\n"
"CV 100,100,101\n"
"MC 152,156,44,232\n"
"s_add_u32 m0, s69, 91008\n"
"EX 134\n"
"CV 102,102,103\n"
"v_accvgpr_write_b32 a92, v100\n"
"MA v,152,a,152,a,40\n"
"buffer_load_dwordx4 v254, s[48:51], s27 offen offset:128 nt lds\n"
"v_accvgpr_write_b32 a93, v102\n"
"v_mul_f32 v116, s76, v116\n"
"v_mul_f32 v117, s76, v117\n"
"MA v,152,a,148,a,36\n"
"s_add_u32 s26, s70, s71\n"
"EX 135\n"
"v_mul_f32 v118, s76, v118\n"
"v_mul_f32 v119, s76, v119\n"
"MA v,152,a,144,a,32\n"
"TR 170,252,37888\n"
"CV 213,134,135\n"
"CV 116,116,117\n"
"CV 118,118,119\n"
"MC 148,144,16,228\n"
"s_and_b32 s26, s26, 0x3ffff\n"
"EX 136\n"
"v_accvgpr_write_b32 a94, v116\n"
"v_accvgpr_write_b32 a95, v118\n"
"MA v,148,a,148,a,20\n"
"TR 172,252,41984\n"
"v_mul_f32 v104, s76, v104\n"
"v_mul_f32 v105, s76, v105\n"
"v_mul_f32 v106, s76, v106\n"
"MA v,148,a,152,a,24\n"
"s_and_b32 s81, s70, 0xfffc0000\n"
"EX 137\n"
"v_mul_f32 v107, s76, v107\n"
"CV 104,104,105\n"
"MA v,148,a,156,a,28\n"
"TR 174,252,46080\n"
"CV 216,136,137\n"
"CV 106,106,107\n"
"v_accvgpr_write_b32 a108, v104\n"
"MC 144,156,12,224\n"
"EX 138\n"
"v_accvgpr_write_b32 a109, v106\n"
"v_mul_f32 v120, s76, v120\n"
"MA v,144,a,152,a,8\n"
"TR 176,251,34304\n"
"v_mul_f32 v121, s76, v121\n"
"v_mul_f32 v122, s76, v122\n"
"v_mul_f32 v123, s76, v123\n"
"MA v,144,a,148,a,4\n"
"s_or_b32 s26, s26, s81\n"
"EX 139\n"
"CV 120,120,121\n"
"CV 122,122,123\n"
"MA v,144,a,144,a,0\n"
"s_waitcnt lgkmcnt(5)\n"
"TR 178,251,38400\n"
"CV 217,138,139\n"
"v_accvgpr_write_b32 a110, v120\n"
"v_accvgpr_write_b32 a111, v122\n"
"MZ v,64,v,160,a,160\n"
"EX 140\n"
"v_mul_f32 v108, s76, v108\n"
"v_mul_f32 v109, s76, v109\n"
"MA v,64,v,176,a,164\n"
"TR 180,251,42496\n"
"v_mul_f32 v110, s76, v110\n"
"v_mul_f32 v111, s76, v111\n"
"CV 108,108,109\n"
"MZ v,68,v,180,a,164\n"
"s_add_u32 s70, s70, 8192\n"
"EX 141\n"
"CV 110,110,111\n"
"v_accvgpr_write_b32 a124, v108\n"
"MA v,68,v,164,a,160\n"
"TR 182,251,46592\n"
"CV 220,140,141\n"
"v_accvgpr_write_b32 a125, v110\n"
"v_mul_f32 v124, s76, v124\n"
"MZ v,72,v,168,a,160\n"
"s_add_u32 m0, s69, 8192\n"
"EX 142\n"
"v_mul_f32 v125, s76, v125\n"
"v_mul_f32 v126, s76, v126\n"
"MA v,72,v,184,a,164\n"
"buffer_load_dwordx4 v253, s[44:47], s25 offen lds\n"
"v_mul_f32 v127, s76, v127\n"
"CV 124,124,125\n"
"CV 126,126,127\n"
"MZ v,76,v,188,a,164\n"
"EX 143\n"
"v_accvgpr_write_b32 a126, v124\n"
"v_accvgpr_write_b32 a127, v126\n"
"MA v,76,v,172,a,160\n"
"s_waitcnt lgkmcnt(4)\n"
"TR 184,252,34304\n"
"CV 221,142,143\n"
"MZ v,92,v,172,a,168\n"
"EX 156\n"
"MA v,92,v,188,a,172\n"
"TR 186,252,38400\n"
"MZ v,88,v,184,a,172\n"
"EX 157\n"
"MA v,88,v,168,a,168\n"
"CV 222,156,157\n"
"MZ v,84,v,164,a,168\n"
"EX 158\n"
"MA v,84,v,180,a,172\n"
"TR 188,252,42496\n"
"MZ v,80,v,176,a,172\n"
"EX 159\n"
"MA v,80,v,160,a,168\n"
"s_waitcnt lgkmcnt(3)\n"
"TR 190,252,46592\n"
"CV 223,158,159\n"
"MZ v,96,v,160,a,176\n"
"s_add_u32 m0, s69, 9088\n"
"EX 152\n"
"MA v,96,v,176,a,180\n"
"s_memrealtime vcc\n"
"buffer_load_dwordx4 v253, s[44:47], s25 offen offset:128 lds\n"
"MZ v,100,v,180,a,180\n"
"EX 153\n"
"MA v,100,v,164,a,176\n"
"CV 218,152,153\n"
"MZ v,104,v,168,a,176\n"
"s_waitcnt lgkmcnt(0)\n"
"EX 154\n"
"MA v,104,v,184,a,180\n"
"s_waitcnt vmcnt(16)\n"
"MZ v,108,v,188,a,180\n"
"s_barrier\n"
"EX 155\n"
"MA v,108,v,172,a,176\n"
"RD 128,249,32768\n"
"CV 219,154,155\n"
"MZ v,124,v,172,a,184\n"
"s_sub_u32 s33, s72, vcc_lo\n"
"EX 148\n"
"MA v,124,v,188,a,188\n"
"s_add_u32 s72, vcc_lo, s77\n"
"RD 132,250,32768\n"
"MZ v,120,v,184,a,188\n"
"s_max_i32 s33, s33, -150\n"
"EX 149\n"
"MA v,120,v,168,a,184\n"
"s_add_u32 s72, s72, s33\n"
"RD 136,249,33792\n"
"CV 214,148,149\n"
"MZ v,116,v,164,a,184\n"
"s_cmp_gt_i32 s33, 0\n"
"EX 150\n"
"MA v,116,v,180,a,188\n"
"s_cbranch_scc1 L_PACE0_%=\n"
"L_PACEB0_%=:\n"
"RD 140,250,33792\n"
"MZ v,112,v,176,a,188\n"
"EX 151\n"
"MA v,112,v,160,a,184\n"
"CV 215,150,151\n"
"MA v,240,v,192,a,236\n"
"MA v,240,v,196,a,240\n"
"MA v,240,v,200,a,244\n"
"MA v,240,v,204,a,248\n"
"s_waitcnt lgkmcnt(0)\n"
"s_mov_b64 s[52:53], s[18:19]\n"
"s_add_u32 s52, s52, 0\n"
"s_addc_u32 s53, s53, 0\n"
"s_and_b32 s53, s53, 0xffff\n"
"s_mov_b32 s54, 0\n"
"s_mov_b32 s55, 0x20000\n"
"s_mov_b64 exec, 0\n"
"s_cmp_eq_u32 s64, 0\n"
"s_cbranch_scc1 L_ST9_%=\n"
"s_cmp_eq_u32 s64, 1\n"
"s_cbranch_scc1 L_ST10_%=\n"
"s_cmp_eq_u32 s64, 2\n"
"s_cbranch_scc1 L_ST11_%=\n"
"s_cmp_eq_u32 s64, 3\n"
"s_cbranch_scc1 L_ST12_%=\n"
"L_VB2_%=:\n"
"s_mov_b64 exec, -1\n"
"s_mov_b32 s85, s30\n"
"s_add_u32 s54, s30, 8\n"
"s_mov_b32 s66, 0\n"
"s_cmp_eq_u32 s74, 0\n"
"s_cbranch_scc0 L_SWEEP1_%=\n"
"L_P3_%=:\n"
"MC 128,128,0,224\n"
"EX 144\n"
"MA v,128,a,132,a,4\n"
"RD 144,249,36864\n"
"MA v,128,a,136,a,8\n"
"EX 145\n"
"MA v,128,a,140,a,12\n"
"RD 148,250,36864\n"
"CV 210,144,145\n"
"MC 132,140,28,228\n"
"EX 146\n"
"MA v,132,a,136,a,24\n"
"L_ST6_%=:\n"
"s_cbranch_execz L_VB1_%=\n"
"RD 152,249,37888\n"
"MA v,132,a,132,a,20\n"
"EX 147\n"
"MA v,132,a,128,a,16\n"
"RD 156,250,37888\n"
"CV 211,146,147\n"
"MC 136,128,32,232\n"
"s_add_u32 m0, s69, 98304\n"
"EX 128\n"
"MA v,136,a,132,a,36\n"
"buffer_load_dwordx4 v254, s[48:51], s24 offen nt lds\n"
"MA v,136,a,136,a,40\n"
"EX 129\n"
"MA v,136,a,140,a,44\n"
"TR 160,251,49152\n"
"CV 160,128,129\n"
"MC 140,140,60,236\n"
"EX 130\n"
"MA v,140,a,136,a,56\n"
"TR 162,251,53248\n"
"MA v,140,a,132,a,52\n"
"EX 131\n"
"MA v,140,a,128,a,48\n"
"s_waitcnt lgkmcnt(2)\n"
"TR 164,251,57344\n"
"CV 161,130,131\n"
"MC 156,144,48,236\n"
"EX 132\n"
"MA v,156,a,148,a,52\n"
"TR 166,251,61440\n"
"MA v,156,a,152,a,56\n"
"EX 133\n"
"MA v,156,a,156,a,60\n"
"TR 168,252,49152\n"
"CV 164,132,133\n"
"MC 152,156,44,232\n"
"s_add_u32 m0, s69, 99200\n"
"EX 134\n"
"MA v,152,a,152,a,40\n"
"buffer_load_dwordx4 v254, s[48:51], s24 offen offset:128 nt lds\n"
"MA v,152,a,148,a,36\n"
"s_add_u32 s27, s70, s71\n"
"EX 135\n"
"MA v,152,a,144,a,32\n"
"TR 170,252,53248\n"
"CV 165,134,135\n"
"MC 148,144,16,228\n"
"s_and_b32 s27, s27, 0x3ffff\n"
"EX 136\n"
"MA v,148,a,148,a,20\n"
"TR 172,252,57344\n"
"MA v,148,a,152,a,24\n"
"s_and_b32 s81, s70, 0xfffc0000\n"
"EX 137\n"
"MA v,148,a,156,a,28\n"
"TR 174,252,61440\n"
"CV 168,136,137\n"
"MC 144,156,12,224\n"
"EX 138\n"
"MA v,144,a,152,a,8\n"
"TR 176,251,49664\n"
"MA v,144,a,148,a,4\n"
"s_or_b32 s27, s27, s81\n"
"EX 139\n"
"MA v,144,a,144,a,0\n"
"s_waitcnt lgkmcnt(5)\n"
"TR 178,251,53760\n"
"CV 169,138,139\n"
"MA v,0,v,192,a,160\n"
"EX 140\n"
"MA v,0,v,208,a,164\n"
"TR 180,251,57856\n"
"MA v,4,v,212,a,164\n"
"s_add_u32 s70, s70, 8192\n"
"EX 141\n"
"MA v,4,v,196,a,160\n"
"TR 182,251,61952\n"
"CV 172,140,141\n"
"MA v,8,v,200,a,160\n"
"s_add_u32 m0, s69, 16384\n"
"EX 142\n"
"MA v,8,v,216,a,164\n"
"buffer_load_dwordx4 v253, s[44:47], s26 offen lds\n"
"MA v,12,v,220,a,164\n"
"EX 143\n"
"MA v,12,v,204,a,160\n"
"s_waitcnt lgkmcnt(4)\n"
"TR 184,252,49664\n"
"CV 173,142,143\n"
"MA v,28,v,204,a,168\n"
"EX 156\n"
"MA v,28,v,220,a,172\n"
"TR 186,252,53760\n"
"MA v,24,v,216,a,172\n"
"EX 157\n"
"MA v,24,v,200,a,168\n"
"CV 174,156,157\n"
"MA v,20,v,196,a,168\n"
"EX 158\n"
"MA v,20,v,212,a,172\n"
"TR 188,252,57856\n"
"MA v,16,v,208,a,172\n"
"EX 159\n"
"MA v,16,v,192,a,168\n"
"s_waitcnt lgkmcnt(3)\n"
"TR 190,252,61952\n"
"CV 175,158,159\n"
"MA v,32,v,192,a,176\n"
"s_add_u32 m0, s69, 17280\n"
"EX 152\n"
"MA v,32,v,208,a,180\n"
"buffer_load_dwordx4 v253, s[44:47], s26 offen offset:128 lds\n"
"MA v,36,v,212,a,180\n"
"EX 153\n"
"MA v,36,v,196,a,176\n"
"CV 170,152,153\n"
"MA v,40,v,200,a,176\n"
"EX 154\n"
"MA v,40,v,216,a,180\n"
"MA v,44,v,220,a,180\n"
"EX 155\n"
"MA v,44,v,204,a,176\n"
"s_waitcnt lgkmcnt(0)\n"
"RD 128,249,40960\n"
"CV 171,154,155\n"
"MA v,60,v,204,a,184\n"
"EX 148\n"
"MA v,60,v,220,a,188\n"
"RD 132,250,40960\n"
"MA v,56,v,216,a,188\n"
"EX 149\n"
"MA v,56,v,200,a,184\n"
"RD 136,249,41984\n"
"CV 166,148,149\n"
"MA v,52,v,196,a,184\n"
"EX 150\n"
"MA v,52,v,212,a,188\n"
"RD 140,250,41984\n"
"MA v,48,v,208,a,188\n"
"EX 151\n"
"MA v,48,v,192,a,184\n"
"CV 167,150,151\n"
"MA v,240,v,208,a,236\n"
"MA v,240,v,212,a,240\n"
"global_load_dword a255, v255, s[34:35]\n"
"MA v,240,v,216,a,244\n"
"MA v,240,v,220,a,248\n"
"s_waitcnt lgkmcnt(0)\n"
"MC 128,128,0,224\n"
"EX 144\n"
"MA v,128,a,132,a,4\n"
"RD 144,249,45056\n"
"MA v,128,a,136,a,8\n"
"EX 145\n"
"MA v,128,a,140,a,12\n"
"RD 148,250,45056\n"
"CV 162,144,145\n"
"MC 132,140,28,228\n"
"EX 146\n"
"MA v,132,a,136,a,24\n"
"RD 152,249,46080\n"
"MA v,132,a,132,a,20\n"
"EX 147\n"
"MA v,132,a,128,a,16\n"
"RD 156,250,46080\n"
"CV 163,146,147\n"
"MC 136,128,32,232\n"
"s_add_u32 m0, s69, 106496\n"
"EX 128\n"
"MA v,136,a,132,a,36\n"
"buffer_load_dwordx4 v254, s[48:51], s25 offen nt lds\n"
"MA v,136,a,136,a,40\n"
"EX 129\n"
"MA v,136,a,140,a,44\n"
"TR 160,251,50176\n"
"CV 176,128,129\n"
"MC 140,140,60,236\n"
"EX 130\n"
"MA v,140,a,136,a,56\n"
"TR 162,251,54272\n"
"MA v,140,a,132,a,52\n"
"EX 131\n"
"MA v,140,a,128,a,48\n"
"s_waitcnt lgkmcnt(2)\n"
"TR 164,251,58368\n"
"CV 177,130,131\n"
"MC 156,144,48,236\n"
"EX 132\n"
"MA v,156,a,148,a,52\n"
"TR 166,251,62464\n"
"MA v,156,a,152,a,56\n"
"EX 133\n"
"MA v,156,a,156,a,60\n"
"TR 168,252,50176\n"
"CV 180,132,133\n"
"MC 152,156,44,232\n"
"s_add_u32 m0, s69, 107392\n"
"EX 134\n"
"MA v,152,a,152,a,40\n"
"buffer_load_dwordx4 v254, s[48:51], s25 offen offset:128 nt lds\n"
"MA v,152,a,148,a,36\n"
"s_add_u32 s24, s70, s71\n"
"EX 135\n"
"MA v,152,a,144,a,32\n"
"TR 170,252,54272\n"
"CV 181,134,135\n"
"MC 148,144,16,228\n"
"s_and_b32 s24, s24, 0x3ffff\n"
"EX 136\n"
"MA v,148,a,148,a,20\n"
"TR 172,252,58368\n"
"MA v,148,a,152,a,24\n"
"s_and_b32 s81, s70, 0xfffc0000\n"
"EX 137\n"
"MA v,148,a,156,a,28\n"
"TR 174,252,62464\n"
"CV 184,136,137\n"
"MC 144,156,12,224\n"
"EX 138\n"
"MA v,144,a,152,a,8\n"
"TR 176,251,50688\n"
"MA v,144,a,148,a,4\n"
"s_or_b32 s24, s24, s81\n"
"EX 139\n"
"MA v,144,a,144,a,0\n"
"s_waitcnt lgkmcnt(5)\n"
"TR 178,251,54784\n"
"CV 185,138,139\n"
"MA v,64,v,192,a,160\n"
"EX 140\n"
"MA v,64,v,208,a,164\n"
"TR 180,251,58880\n"
"MA v,68,v,212,a,164\n"
"s_add_u32 s70, s70, 8192\n"
"EX 141\n"
"MA v,68,v,196,a,160\n"
"TR 182,251,62976\n"
"CV 188,140,141\n"
"MA v,72,v,200,a,160\n"
"s_add_u32 m0, s69, 24576\n"
"EX 142\n"
"MA v,72,v,216,a,164\n"
"buffer_load_dwordx4 v253, s[44:47], s27 offen lds\n"
"MA v,76,v,220,a,164\n"
"EX 143\n"
"MA v,76,v,204,a,160\n"
"s_waitcnt lgkmcnt(4)\n"
"TR 184,252,50688\n"
"CV 189,142,143\n"
"MA v,92,v,204,a,168\n"
"EX 156\n"
"MA v,92,v,220,a,172\n"
"TR 186,252,54784\n"
"MA v,88,v,216,a,172\n"
"EX 157\n"
"MA v,88,v,200,a,168\n"
"CV 190,156,157\n"
"MA v,84,v,196,a,168\n"
"EX 158\n"
"MA v,84,v,212,a,172\n"
"TR 188,252,58880\n"
"MA v,80,v,208,a,172\n"
"EX 159\n"
"MA v,80,v,192,a,168\n"
"s_waitcnt lgkmcnt(3)\n"
"TR 190,252,62976\n"
"CV 191,158,159\n"
"MA v,96,v,192,a,176\n"
"s_add_u32 m0, s69, 25472\n"
"EX 152\n"
"MA v,96,v,208,a,180\n"
"s_memrealtime vcc\n"
"buffer_load_dwordx4 v253, s[44:47], s27 offen offset:128 lds\n"
"MA v,100,v,212,a,180\n"
"EX 153\n"
"MA v,100,v,196,a,176\n"
"CV 186,152,153\n"
"MA v,104,v,200,a,176\n"
"s_waitcnt lgkmcnt(0)\n"
"EX 154\n"
"MA v,104,v,216,a,180\n"
"s_waitcnt vmcnt(17)\n"
"MA v,108,v,220,a,180\n"
"s_barrier\n"
"EX 155\n"
"MA v,108,v,204,a,176\n"
"RD 128,249,49152\n"
"CV 187,154,155\n"
"MA v,124,v,204,a,184\n"
"s_sub_u32 s33, s72, vcc_lo\n"
"EX 148\n"
"MA v,124,v,220,a,188\n"
"s_add_u32 s72, vcc_lo, s77\n"
"RD 132,250,49152\n"
"MA v,120,v,216,a,188\n"
"s_max_i32 s33, s33, -150\n"
"EX 149\n"
"MA v,120,v,200,a,184\n"
"s_add_u32 s72, s72, s33\n"
"RD 136,249,50176\n"
"CV 182,148,149\n"
"MA v,116,v,196,a,184\n"
"s_cmp_gt_i32 s33, 0\n"
"EX 150\n"
"MA v,116,v,212,a,188\n"
"s_cbranch_scc1 L_PACE1_%=\n"
"L_PACEB1_%=:\n"
"RD 140,250,50176\n"
"MA v,112,v,208,a,188\n"
"EX 151\n"
"MA v,112,v,192,a,184\n"
"CV 183,150,151\n"
"MA v,240,v,160,a,236\n"
"MA v,240,v,164,a,240\n"
"buffer_store_dwordx4 a[192:195], v255, s[60:63], s66 offen sc1\n"
"MA v,240,v,168,a,244\n"
"s_add_u32 s66, s66, 1024\n"
"MA v,240,v,172,a,248\n"
"s_waitcnt lgkmcnt(0)\n"
"MC 128,128,0,224\n"
"EX 144\n"
"MA v,128,a,132,a,4\n"
"RD 144,249,53248\n"
"v_accvgpr_mov_b32 a192, a204\n"
"MA v,128,a,136,a,8\n"
"EX 145\n"
"MA v,128,a,140,a,12\n"
"RD 148,250,53248\n"
"CV 178,144,145\n"
"MC 132,140,28,228\n"
"EX 146\n"
"MA v,132,a,136,a,24\n"
"RD 152,249,54272\n"
"v_accvgpr_mov_b32 a193, a205\n"
"MA v,132,a,132,a,20\n"
"EX 147\n"
"MA v,132,a,128,a,16\n"
"RD 156,250,54272\n"
"CV 179,146,147\n"
"MC 136,128,32,232\n"
"s_add_u32 m0, s69, 114688\n"
"EX 128\n"
"MA v,136,a,132,a,36\n"
"buffer_load_dwordx4 v254, s[48:51], s26 offen nt lds\n"
"v_accvgpr_mov_b32 a194, a206\n"
"MA v,136,a,136,a,40\n"
"EX 129\n"
"MA v,136,a,140,a,44\n"
"TR 160,251,0\n"
"CV 192,128,129\n"
"MC 140,140,60,236\n"
"EX 130\n"
"MA v,140,a,136,a,56\n"
"TR 162,251,4096\n"
"v_accvgpr_mov_b32 a195, a207\n"
"MA v,140,a,132,a,52\n"
"EX 131\n"
"MA v,140,a,128,a,48\n"
"s_waitcnt lgkmcnt(2)\n"
"TR 164,251,8192\n"
"CV 193,130,131\n"
"MC 156,144,48,236\n"
"EX 132\n"
"MA v,156,a,148,a,52\n"
"TR 166,251,12288\n"
"v_accvgpr_mov_b32 a204, a216\n"
"MA v,156,a,152,a,56\n"
"EX 133\n"
"MA v,156,a,156,a,60\n"
"TR 168,252,0\n"
"CV 196,132,133\n"
"MC 152,156,44,232\n"
"s_add_u32 m0, s69, 115584\n"
"EX 134\n"
"MA v,152,a,152,a,40\n"
"buffer_load_dwordx4 v254, s[48:51], s26 offen offset:128 nt lds\n"
"v_accvgpr_mov_b32 a205, a217\n"
"MA v,152,a,148,a,36\n"
"s_add_u32 s25, s70, s71\n"
"EX 135\n"
"MA v,152,a,144,a,32\n"
"TR 170,252,4096\n"
"CV 197,134,135\n"
"MC 148,144,16,228\n"
"s_and_b32 s25, s25, 0x3ffff\n"
"EX 136\n"
"MA v,148,a,148,a,20\n"
"TR 172,252,8192\n"
"v_accvgpr_mov_b32 a206, a218\n"
"MA v,148,a,152,a,24\n"
"s_and_b32 s81, s70, 0xfffc0000\n"
"EX 137\n"
"MA v,148,a,156,a,28\n"
"TR 174,252,12288\n"
"CV 200,136,137\n"
"MC 144,156,12,224\n"
"EX 138\n"
"MA v,144,a,152,a,8\n"
"TR 176,251,512\n"
"v_accvgpr_mov_b32 a207, a219\n"
"MA v,144,a,148,a,4\n"
"s_or_b32 s25, s25, s81\n"
"EX 139\n"
"MA v,144,a,144,a,0\n"
"s_waitcnt lgkmcnt(5)\n"
"TR 178,251,4608\n"
"CV 201,138,139\n"
"MA v,0,v,160,a,160\n"
"EX 140\n"
"MA v,0,v,176,a,164\n"
"TR 180,251,8704\n"
"MA v,4,v,180,a,164\n"
"s_add_u32 s70, s70, 8192\n"
"EX 141\n"
"MA v,4,v,164,a,160\n"
"TR 182,251,12800\n"
"CV 204,140,141\n"
"MA v,8,v,168,a,160\n"
"s_add_u32 m0, s69, 32768\n"
"EX 142\n"
"MA v,8,v,184,a,164\n"
"buffer_load_dwordx4 v253, s[44:47], s24 offen lds\n"
"MA v,12,v,188,a,164\n"
"EX 143\n"
"MA v,12,v,172,a,160\n"
"s_waitcnt lgkmcnt(4)\n"
"TR 184,252,512\n"
"CV 205,142,143\n"
"MA v,28,v,172,a,168\n"
"EX 156\n"
"MA v,28,v,188,a,172\n"
"TR 186,252,4608\n"
"MA v,24,v,184,a,172\n"
"EX 157\n"
"MA v,24,v,168,a,168\n"
"CV 206,156,157\n"
"MA v,20,v,164,a,168\n"
"EX 158\n"
"MA v,20,v,180,a,172\n"
"TR 188,252,8704\n"
"MA v,16,v,176,a,172\n"
"EX 159\n"
"MA v,16,v,160,a,168\n"
"s_waitcnt lgkmcnt(3)\n"
"TR 190,252,12800\n"
"CV 207,158,159\n"
"MA v,32,v,160,a,176\n"
"s_add_u32 m0, s69, 33664\n"
"EX 152\n"
"MA v,32,v,176,a,180\n"
"buffer_load_dwordx4 v253, s[44:47], s24 offen offset:128 lds\n"
"MA v,36,v,180,a,180\n"
"EX 153\n"
"MA v,36,v,164,a,176\n"
"CV 202,152,153\n"
"MA v,40,v,168,a,176\n"
"EX 154\n"
"MA v,40,v,184,a,180\n"
"MA v,44,v,188,a,180\n"
"EX 155\n"
"MA v,44,v,172,a,176\n"
"s_waitcnt lgkmcnt(0)\n"
"RD 128,249,57344\n"
"CV 203,154,155\n"
"MA v,60,v,172,a,184\n"
"EX 148\n"
"MA v,60,v,188,a,188\n"
"RD 132,250,57344\n"
"MA v,56,v,184,a,188\n"
"EX 149\n"
"MA v,56,v,168,a,184\n"
"RD 136,249,58368\n"
"CV 198,148,149\n"
"MA v,52,v,164,a,184\n"
"EX 150\n"
"MA v,52,v,180,a,188\n"
"RD 140,250,58368\n"
"MA v,48,v,176,a,188\n"
"EX 151\n"
"MA v,48,v,160,a,184\n"
"CV 199,150,151\n"
"MA v,240,v,176,a,236\n"
"MA v,240,v,180,a,240\n"
"MA v,240,v,184,a,244\n"
"MA v,240,v,188,a,248\n"
"s_waitcnt lgkmcnt(0)\n"
"MC 128,128,0,224\n"
"EX 144\n"
"MA v,128,a,132,a,4\n"
"RD 144,249,61440\n"
"MA v,128,a,136,a,8\n"
"EX 145\n"
"MA v,128,a,140,a,12\n"
"RD 148,250,61440\n"
"CV 194,144,145\n"
"MC 132,140,28,228\n"
"EX 146\n"
"MA v,132,a,136,a,24\n"
"RD 152,249,62464\n"
"MA v,132,a,132,a,20\n"
"EX 147\n"
"MA v,132,a,128,a,16\n"
"RD 156,250,62464\n"
"CV 195,146,147\n"
"MC 136,128,32,232\n"
"s_add_u32 m0, s69, 122880\n"
"EX 128\n"
"MA v,136,a,132,a,36\n"
"buffer_load_dwordx4 v254, s[48:51], s27 offen nt lds\n"
"MA v,136,a,136,a,40\n"
"EX 129\n"
"MA v,136,a,140,a,44\n"
"TR 160,251,1024\n"
"CV 208,128,129\n"
"MC 140,140,60,236\n"
"EX 130\n"
"MA v,140,a,136,a,56\n"
"TR 162,251,5120\n"
"MA v,140,a,132,a,52\n"
"EX 131\n"
"MA v,140,a,128,a,48\n"
"s_waitcnt lgkmcnt(2)\n"
"TR 164,251,9216\n"
"CV 209,130,131\n"
"MC 156,144,48,236\n"
"EX 132\n"
"MA v,156,a,148,a,52\n"
"TR 166,251,13312\n"
"MA v,156,a,152,a,56\n"
"EX 133\n"
"MA v,156,a,156,a,60\n"
"TR 168,252,1024\n"
"CV 212,132,133\n"
"MC 152,156,44,232\n"
"s_add_u32 m0, s69, 123776\n"
"EX 134\n"
"MA v,152,a,152,a,40\n"
"buffer_load_dwordx4 v254, s[48:51], s27 offen offset:128 nt lds\n"
"MA v,152,a,148,a,36\n"
"s_add_u32 s26, s70, s71\n"
"EX 135\n"
"MA v,152,a,144,a,32\n"
"TR 170,252,5120\n"
"CV 213,134,135\n"
"MC 148,144,16,228\n"
"s_and_b32 s26, s26, 0x3ffff\n"
"EX 136\n"
"MA v,148,a,148,a,20\n"
"TR 172,252,9216\n"
"MA v,148,a,152,a,24\n"
"s_and_b32 s81, s70, 0xfffc0000\n"
"EX 137\n"
"MA v,148,a,156,a,28\n"
"TR 174,252,13312\n"
"CV 216,136,137\n"
"MC 144,156,12,224\n"
"EX 138\n"
"MA v,144,a,152,a,8\n"
"TR 176,251,1536\n"
"MA v,144,a,148,a,4\n"
"s_or_b32 s26, s26, s81\n"
"EX 139\n"
"MA v,144,a,144,a,0\n"
"s_waitcnt lgkmcnt(5)\n"
"TR 178,251,5632\n"
"CV 217,138,139\n"
"MA v,64,v,160,a,160\n"
"EX 140\n"
"MA v,64,v,176,a,164\n"
"TR 180,251,9728\n"
"MA v,68,v,180,a,164\n"
"s_add_u32 s70, s70, 8192\n"
"EX 141\n"
"MA v,68,v,164,a,160\n"
"TR 182,251,13824\n"
"CV 220,140,141\n"
"MA v,72,v,168,a,160\n"
"s_add_u32 m0, s69, 40960\n"
"EX 142\n"
"MA v,72,v,184,a,164\n"
"buffer_load_dwordx4 v253, s[44:47], s25 offen lds\n"
"MA v,76,v,188,a,164\n"
"EX 143\n"
"MA v,76,v,172,a,160\n"
"s_waitcnt lgkmcnt(4)\n"
"TR 184,252,1536\n"
"CV 221,142,143\n"
"MA v,92,v,172,a,168\n"
"EX 156\n"
"MA v,92,v,188,a,172\n"
"TR 186,252,5632\n"
"MA v,88,v,184,a,172\n"
"L_ST7_%=:\n"
"s_cbranch_execz L_VB1_%=\n"
"EX 157\n"
"MA v,88,v,168,a,168\n"
"CV 222,156,157\n"
"MA v,84,v,164,a,168\n"
"s_add_u32 s73, s73, 1\n"
"EX 158\n"
"MA v,84,v,180,a,172\n"
"TR 188,252,9728\n"
"MA v,80,v,176,a,172\n"
"EX 159\n"
"MA v,80,v,160,a,168\n"
"s_waitcnt lgkmcnt(3)\n"
"TR 190,252,13824\n"
"CV 223,158,159\n"
"MA v,96,v,160,a,176\n"
"s_add_u32 m0, s69, 41856\n"
"EX 152\n"
"MA v,96,v,176,a,180\n"
"s_memrealtime vcc\n"
"buffer_load_dwordx4 v253, s[44:47], s25 offen offset:128 lds\n"
"MA v,100,v,180,a,180\n"
"EX 153\n"
"MA v,100,v,164,a,176\n"
"CV 218,152,153\n"
"MA v,104,v,168,a,176\n"
"s_waitcnt lgkmcnt(0)\n"
"EX 154\n"
"MA v,104,v,184,a,180\n"
"s_waitcnt vmcnt(18)\n"
"MA v,108,v,188,a,180\n"
"s_barrier\n"
"EX 155\n"
"MA v,108,v,172,a,176\n"
"RD 128,249,0\n"
"CV 219,154,155\n"
"MA v,124,v,172,a,184\n"
"s_sub_u32 s33, s72, vcc_lo\n"
"EX 148\n"
"MA v,124,v,188,a,188\n"
"s_add_u32 s72, vcc_lo, s77\n"
"s_and_b32 s79, s73, 3\n"
"s_cselect_b32 s33, s33, 0\n"
"RD 132,250,0\n"
"MA v,120,v,184,a,188\n"
"s_max_i32 s33, s33, -150\n"
"EX 149\n"
"MA v,120,v,168,a,184\n"
"s_add_u32 s72, s72, s33\n"
"RD 136,249,1024\n"
"CV 214,148,149\n"
"MA v,116,v,164,a,184\n"
"s_cmp_gt_i32 s33, 0\n"
"EX 150\n"
"MA v,116,v,180,a,188\n"
"s_cbranch_scc1 L_PACE2_%=\n"
"L_PACEB2_%=:\n"
"RD 140,250,1024\n"
"MA v,112,v,176,a,188\n"
"s_and_b32 s79, s73, 3\n"
"EX 151\n"
"MA v,112,v,160,a,184\n"
"CV 215,150,151\n"
"MA v,240,v,192,a,236\n"
"MA v,240,v,196,a,240\n"
"MA v,240,v,200,a,244\n"
"MA v,240,v,204,a,248\n"
"s_waitcnt lgkmcnt(0)\n"
"s_cbranch_scc0 L_X31_%=\n"
"MC 128,128,0,224\n"
"EX 144\n"
"MA v,128,a,132,a,4\n"
"RD 144,249,4096\n"
"MA v,128,a,136,a,8\n"
"EX 145\n"
"MA v,128,a,140,a,12\n"
"RD 148,250,4096\n"
"CV 210,144,145\n"
"MC 132,140,28,228\n"
"EX 146\n"
"MA v,132,a,136,a,24\n"
"RD 152,249,5120\n"
"MA v,132,a,132,a,20\n"
"EX 147\n"
"MA v,132,a,128,a,16\n"
"RD 156,250,5120\n"
"CV 211,146,147\n"
"MC 136,128,32,232\n"
"s_add_u32 m0, s69, 65536\n"
"EX 128\n"
"MA v,136,a,132,a,36\n"
"buffer_load_dwordx4 v254, s[48:51], s24 offen nt lds\n"
"MA v,136,a,136,a,40\n"
"EX 129\n"
"MA v,136,a,140,a,44\n"
"TR 160,251,16384\n"
"CV 160,128,129\n"
"MC 140,140,60,236\n"
"EX 130\n"
"MA v,140,a,136,a,56\n"
"TR 162,251,20480\n"
"MA v,140,a,132,a,52\n"
"EX 131\n"
"MA v,140,a,128,a,48\n"
"s_waitcnt lgkmcnt(2)\n"
"TR 164,251,24576\n"
"CV 161,130,131\n"
"MC 156,144,48,236\n"
"EX 132\n"
"MA v,156,a,148,a,52\n"
"TR 166,251,28672\n"
"MA v,156,a,152,a,56\n"
"EX 133\n"
"MA v,156,a,156,a,60\n"
"TR 168,252,16384\n"
"CV 164,132,133\n"
"MC 152,156,44,232\n"
"s_add_u32 m0, s69, 66432\n"
"EX 134\n"
"MA v,152,a,152,a,40\n"
"buffer_load_dwordx4 v254, s[48:51], s24 offen offset:128 nt lds\n"
"MA v,152,a,148,a,36\n"
"s_add_u32 s27, s70, s71\n"
"EX 135\n"
"MA v,152,a,144,a,32\n"
"TR 170,252,20480\n"
"CV 165,134,135\n"
"MC 148,144,16,228\n"
"s_and_b32 s27, s27, 0x3ffff\n"
"EX 136\n"
"MA v,148,a,148,a,20\n"
"TR 172,252,24576\n"
"MA v,148,a,152,a,24\n"
"s_and_b32 s81, s70, 0xfffc0000\n"
"EX 137\n"
"MA v,148,a,156,a,28\n"
"TR 174,252,28672\n"
"CV 168,136,137\n"
"MC 144,156,12,224\n"
"EX 138\n"
"MA v,144,a,152,a,8\n"
"TR 176,251,16896\n"
"MA v,144,a,148,a,4\n"
"s_or_b32 s27, s27, s81\n"
"EX 139\n"
"MA v,144,a,144,a,0\n"
"s_waitcnt lgkmcnt(5)\n"
"TR 178,251,20992\n"
"CV 169,138,139\n"
"MA v,0,v,192,a,160\n"
"EX 140\n"
"MA v,0,v,208,a,164\n"
"TR 180,251,25088\n"
"MA v,4,v,212,a,164\n"
"s_add_u32 s70, s70, 8192\n"
"EX 141\n"
"MA v,4,v,196,a,160\n"
"TR 182,251,29184\n"
"CV 172,140,141\n"
"MA v,8,v,200,a,160\n"
"s_add_u32 m0, s69, 49152\n"
"EX 142\n"
"MA v,8,v,216,a,164\n"
"buffer_load_dwordx4 v253, s[44:47], s26 offen lds\n"
"MA v,12,v,220,a,164\n"
"EX 143\n"
"MA v,12,v,204,a,160\n"
"s_waitcnt lgkmcnt(4)\n"
"TR 184,252,16896\n"
"CV 173,142,143\n"
"MA v,28,v,204,a,168\n"
"EX 156\n"
"MA v,28,v,220,a,172\n"
"TR 186,252,20992\n"
"MA v,24,v,216,a,172\n"
"EX 157\n"
"MA v,24,v,200,a,168\n"
"CV 174,156,157\n"
"MA v,20,v,196,a,168\n"
"EX 158\n"
"MA v,20,v,212,a,172\n"
"TR 188,252,25088\n"
"MA v,16,v,208,a,172\n"
"EX 159\n"
"MA v,16,v,192,a,168\n"
"s_waitcnt lgkmcnt(3)\n"
"TR 190,252,29184\n"
"CV 175,158,159\n"
"MA v,32,v,192,a,176\n"
"s_add_u32 m0, s69, 50048\n"
"EX 152\n"
"MA v,32,v,208,a,180\n"
"buffer_load_dwordx4 v253, s[44:47], s26 offen offset:128 lds\n"
"MA v,36,v,212,a,180\n"
"EX 153\n"
"MA v,36,v,196,a,176\n"
"CV 170,152,153\n"
"MA v,40,v,200,a,176\n"
"EX 154\n"
"MA v,40,v,216,a,180\n"
"MA v,44,v,220,a,180\n"
"EX 155\n"
"MA v,44,v,204,a,176\n"
"s_waitcnt lgkmcnt(0)\n"
"RD 128,249,8192\n"
"CV 171,154,155\n"
"MA v,60,v,204,a,184\n"
"EX 148\n"
"MA v,60,v,220,a,188\n"
"RD 132,250,8192\n"
"MA v,56,v,216,a,188\n"
"EX 149\n"
"MA v,56,v,200,a,184\n"
"RD 136,249,9216\n"
"CV 166,148,149\n"
"MA v,52,v,196,a,184\n"
"EX 150\n"
"MA v,52,v,212,a,188\n"
"RD 140,250,9216\n"
"MA v,48,v,208,a,188\n"
"EX 151\n"
"MA v,48,v,192,a,184\n"
"CV 167,150,151\n"
"MA v,240,v,208,a,236\n"
"MA v,240,v,212,a,240\n"
"buffer_store_dwordx4 a[196:199], v255, s[60:63], s66 offen sc1\n"
"MA v,240,v,216,a,244\n"
"s_add_u32 s66, s66, 1024\n"
"MA v,240,v,220,a,248\n"
"s_waitcnt lgkmcnt(0)\n"
"MC 128,128,0,224\n"
"EX 144\n"
"MA v,128,a,132,a,4\n"
"RD 144,249,12288\n"
"v_accvgpr_mov_b32 a196, a208\n"
"MA v,128,a,136,a,8\n"
"EX 145\n"
"MA v,128,a,140,a,12\n"
"RD 148,250,12288\n"
"CV 162,144,145\n"
"MC 132,140,28,228\n"
"EX 146\n"
"MA v,132,a,136,a,24\n"
"RD 152,249,13312\n"
"v_accvgpr_mov_b32 a197, a209\n"
"MA v,132,a,132,a,20\n"
"EX 147\n"
"MA v,132,a,128,a,16\n"
"RD 156,250,13312\n"
"CV 163,146,147\n"
"MC 136,128,32,232\n"
"s_add_u32 m0, s69, 73728\n"
"EX 128\n"
"MA v,136,a,132,a,36\n"
"buffer_load_dwordx4 v254, s[48:51], s25 offen nt lds\n"
"v_accvgpr_mov_b32 a198, a210\n"
"MA v,136,a,136,a,40\n"
"EX 129\n"
"MA v,136,a,140,a,44\n"
"TR 160,251,17408\n"
"CV 176,128,129\n"
"MC 140,140,60,236\n"
"EX 130\n"
"MA v,140,a,136,a,56\n"
"TR 162,251,21504\n"
"v_accvgpr_mov_b32 a199, a211\n"
"MA v,140,a,132,a,52\n"
"EX 131\n"
"MA v,140,a,128,a,48\n"
"s_waitcnt lgkmcnt(2)\n"
"TR 164,251,25600\n"
"CV 177,130,131\n"
"MC 156,144,48,236\n"
"EX 132\n"
"MA v,156,a,148,a,52\n"
"TR 166,251,29696\n"
"v_accvgpr_mov_b32 a208, a220\n"
"MA v,156,a,152,a,56\n"
"EX 133\n"
"MA v,156,a,156,a,60\n"
"TR 168,252,17408\n"
"CV 180,132,133\n"
"MC 152,156,44,232\n"
"s_add_u32 m0, s69, 74624\n"
"EX 134\n"
"MA v,152,a,152,a,40\n"
"buffer_load_dwordx4 v254, s[48:51], s25 offen offset:128 nt lds\n"
"v_accvgpr_mov_b32 a209, a221\n"
"MA v,152,a,148,a,36\n"
"s_add_u32 s24, s70, s71\n"
"EX 135\n"
"MA v,152,a,144,a,32\n"
"TR 170,252,21504\n"
"CV 181,134,135\n"
"MC 148,144,16,228\n"
"s_and_b32 s24, s24, 0x3ffff\n"
"EX 136\n"
"MA v,148,a,148,a,20\n"
"TR 172,252,25600\n"
"v_accvgpr_mov_b32 a210, a222\n"
"MA v,148,a,152,a,24\n"
"s_and_b32 s81, s70, 0xfffc0000\n"
"EX 137\n"
"MA v,148,a,156,a,28\n"
"TR 174,252,29696\n"
"CV 184,136,137\n"
"MC 144,156,12,224\n"
"EX 138\n"
"MA v,144,a,152,a,8\n"
"TR 176,251,17920\n"
"v_accvgpr_mov_b32 a211, a223\n"
"MA v,144,a,148,a,4\n"
"s_or_b32 s24, s24, s81\n"
"EX 139\n"
"MA v,144,a,144,a,0\n"
"s_waitcnt lgkmcnt(5)\n"
"TR 178,251,22016\n"
"CV 185,138,139\n"
"MA v,64,v,192,a,160\n"
"EX 140\n"
"MA v,64,v,208,a,164\n"
"TR 180,251,26112\n"
"MA v,68,v,212,a,164\n"
"s_add_u32 s70, s70, 8192\n"
"EX 141\n"
"MA v,68,v,196,a,160\n"
"TR 182,251,30208\n"
"CV 188,140,141\n"
"MA v,72,v,200,a,160\n"
"s_add_u32 m0, s69, 57344\n"
"EX 142\n"
"MA v,72,v,216,a,164\n"
"buffer_load_dwordx4 v253, s[44:47], s27 offen lds\n"
"MA v,76,v,220,a,164\n"
"EX 143\n"
"MA v,76,v,204,a,160\n"
"s_waitcnt lgkmcnt(4)\n"
"TR 184,252,17920\n"
"CV 189,142,143\n"
"MA v,92,v,204,a,168\n"
"EX 156\n"
"MA v,92,v,220,a,172\n"
"TR 186,252,22016\n"
"MA v,88,v,216,a,172\n"
"EX 157\n"
"MA v,88,v,200,a,168\n"
"CV 190,156,157\n"
"MA v,84,v,196,a,168\n"
"EX 158\n"
"MA v,84,v,212,a,172\n"
"TR 188,252,26112\n"
"MA v,80,v,208,a,172\n"
"EX 159\n"
"MA v,80,v,192,a,168\n"
"s_waitcnt lgkmcnt(3)\n"
"TR 190,252,30208\n"
"CV 191,158,159\n"
"MA v,96,v,192,a,176\n"
"s_add_u32 m0, s69, 58240\n"
"EX 152\n"
"MA v,96,v,208,a,180\n"
"s_memrealtime vcc\n"
"buffer_load_dwordx4 v253, s[44:47], s27 offen offset:128 lds\n"
"MA v,100,v,212,a,180\n"
"EX 153\n"
"MA v,100,v,196,a,176\n"
"CV 186,152,153\n"
"MA v,104,v,200,a,176\n"
"s_waitcnt lgkmcnt(0)\n"
"EX 154\n"
"MA v,104,v,216,a,180\n"
"s_waitcnt vmcnt(18)\n"
"MA v,108,v,220,a,180\n"
"s_barrier\n"
"EX 155\n"
"MA v,108,v,204,a,176\n"
"RD 128,249,16384\n"
"CV 187,154,155\n"
"MA v,124,v,204,a,184\n"
"s_sub_u32 s33, s72, vcc_lo\n"
"EX 148\n"
"MA v,124,v,220,a,188\n"
"s_add_u32 s72, vcc_lo, s77\n"
"RD 132,250,16384\n"
"MA v,120,v,216,a,188\n"
"s_max_i32 s33, s33, -150\n"
"EX 149\n"
"MA v,120,v,200,a,184\n"
"s_add_u32 s72, s72, s33\n"
"RD 136,249,17408\n"
"CV 182,148,149\n"
"MA v,116,v,196,a,184\n"
"s_cmp_gt_i32 s33, 0\n"
"EX 150\n"
"MA v,116,v,212,a,188\n"
"s_cbranch_scc1 L_PACE3_%=\n"
"L_PACEB3_%=:\n"
"RD 140,250,17408\n"
"MA v,112,v,208,a,188\n"
"EX 151\n"
"MA v,112,v,192,a,184\n"
"CV 183,150,151\n"
"MA v,240,v,160,a,236\n"
"MA v,240,v,164,a,240\n"
"MA v,240,v,168,a,244\n"
"MA v,240,v,172,a,248\n"
"s_waitcnt lgkmcnt(0)\n"
"MC 128,128,0,224\n"
"EX 144\n"
"MA v,128,a,132,a,4\n"
"RD 144,249,20480\n"
"MA v,128,a,136,a,8\n"
"EX 145\n"
"MA v,128,a,140,a,12\n"
"RD 148,250,20480\n"
"CV 178,144,145\n"
"MC 132,140,28,228\n"
"EX 146\n"
"MA v,132,a,136,a,24\n"
"RD 152,249,21504\n"
"MA v,132,a,132,a,20\n"
"EX 147\n"
"MA v,132,a,128,a,16\n"
"RD 156,250,21504\n"
"CV 179,146,147\n"
"MC 136,128,32,232\n"
"s_add_u32 m0, s69, 81920\n"
"EX 128\n"
"MA v,136,a,132,a,36\n"
"buffer_load_dwordx4 v254, s[48:51], s26 offen nt lds\n"
"MA v,136,a,136,a,40\n"
"EX 129\n"
"MA v,136,a,140,a,44\n"
"TR 160,251,32768\n"
"CV 192,128,129\n"
"MC 140,140,60,236\n"
"EX 130\n"
"MA v,140,a,136,a,56\n"
"TR 162,251,36864\n"
"MA v,140,a,132,a,52\n"
"EX 131\n"
"MA v,140,a,128,a,48\n"
"s_waitcnt lgkmcnt(2)\n"
"TR 164,251,40960\n"
"CV 193,130,131\n"
"MC 156,144,48,236\n"
"EX 132\n"
"MA v,156,a,148,a,52\n"
"TR 166,251,45056\n"
"MA v,156,a,152,a,56\n"
"EX 133\n"
"MA v,156,a,156,a,60\n"
"TR 168,252,32768\n"
"CV 196,132,133\n"
"MC 152,156,44,232\n"
"s_add_u32 m0, s69, 82816\n"
"EX 134\n"
"MA v,152,a,152,a,40\n"
"buffer_load_dwordx4 v254, s[48:51], s26 offen offset:128 nt lds\n"
"MA v,152,a,148,a,36\n"
"s_add_u32 s25, s70, s71\n"
"EX 135\n"
"MA v,152,a,144,a,32\n"
"TR 170,252,36864\n"
"CV 197,134,135\n"
"MC 148,144,16,228\n"
"s_and_b32 s25, s25, 0x3ffff\n"
"EX 136\n"
"MA v,148,a,148,a,20\n"
"TR 172,252,40960\n"
"MA v,148,a,152,a,24\n"
"s_and_b32 s81, s70, 0xfffc0000\n"
"EX 137\n"
"MA v,148,a,156,a,28\n"
"TR 174,252,45056\n"
"CV 200,136,137\n"
"MC 144,156,12,224\n"
"EX 138\n"
"MA v,144,a,152,a,8\n"
"TR 176,251,33280\n"
"MA v,144,a,148,a,4\n"
"s_or_b32 s25, s25, s81\n"
"EX 139\n"
"MA v,144,a,144,a,0\n"
"s_waitcnt lgkmcnt(5)\n"
"TR 178,251,37376\n"
"CV 201,138,139\n"
"MA v,0,v,160,a,160\n"
"EX 140\n"
"MA v,0,v,176,a,164\n"
"TR 180,251,41472\n"
"MA v,4,v,180,a,164\n"
"s_add_u32 s70, s70, 8192\n"
"EX 141\n"
"MA v,4,v,164,a,160\n"
"TR 182,251,45568\n"
"CV 204,140,141\n"
"MA v,8,v,168,a,160\n"
"s_add_u32 m0, s69, 0\n"
"EX 142\n"
"MA v,8,v,184,a,164\n"
"buffer_load_dwordx4 v253, s[44:47], s24 offen nt lds\n"
"MA v,12,v,188,a,164\n"
"EX 143\n"
"MA v,12,v,172,a,160\n"
"s_waitcnt lgkmcnt(4)\n"
"TR 184,252,33280\n"
"CV 205,142,143\n"
"MA v,28,v,172,a,168\n"
"EX 156\n"
"MA v,28,v,188,a,172\n"
"TR 186,252,37376\n"
"MA v,24,v,184,a,172\n"
"EX 157\n"
"MA v,24,v,168,a,168\n"
"CV 206,156,157\n"
"MA v,20,v,164,a,168\n"
"EX 158\n"
"MA v,20,v,180,a,172\n"
"TR 188,252,41472\n"
"MA v,16,v,176,a,172\n"
"EX 159\n"
"MA v,16,v,160,a,168\n"
"s_waitcnt lgkmcnt(3)\n"
"TR 190,252,45568\n"
"CV 207,158,159\n"
"MA v,32,v,160,a,176\n"
"s_add_u32 m0, s69, 896\n"
"EX 152\n"
"MA v,32,v,176,a,180\n"
"buffer_load_dwordx4 v253, s[44:47], s24 offen offset:128 nt lds\n"
"MA v,36,v,180,a,180\n"
"EX 153\n"
"MA v,36,v,164,a,176\n"
"CV 202,152,153\n"
"MA v,40,v,168,a,176\n"
"EX 154\n"
"MA v,40,v,184,a,180\n"
"MA v,44,v,188,a,180\n"
"EX 155\n"
"MA v,44,v,172,a,176\n"
"s_waitcnt lgkmcnt(0)\n"
"RD 128,249,24576\n"
"CV 203,154,155\n"
"MA v,60,v,172,a,184\n"
"EX 148\n"
"MA v,60,v,188,a,188\n"
"RD 132,250,24576\n"
"MA v,56,v,184,a,188\n"
"EX 149\n"
"MA v,56,v,168,a,184\n"
"RD 136,249,25600\n"
"CV 198,148,149\n"
"MA v,52,v,164,a,184\n"
"EX 150\n"
"MA v,52,v,180,a,188\n"
"RD 140,250,25600\n"
"MA v,48,v,176,a,188\n"
"EX 151\n"
"MA v,48,v,160,a,184\n"
"CV 199,150,151\n"
"MA v,240,v,176,a,236\n"
"MA v,240,v,180,a,240\n"
"buffer_store_dwordx4 a[200:203], v255, s[60:63], s66 offen sc1\n"
"MA v,240,v,184,a,244\n"
"s_add_u32 s66, s66, 1024\n"
"MA v,240,v,188,a,248\n"
"s_waitcnt lgkmcnt(0)\n"
"MC 128,128,0,224\n"
"EX 144\n"
"MA v,128,a,132,a,4\n"
"RD 144,249,28672\n"
"v_accvgpr_mov_b32 a200, a212\n"
"MA v,128,a,136,a,8\n"
"EX 145\n"
"MA v,128,a,140,a,12\n"
"RD 148,250,28672\n"
"CV 194,144,145\n"
"MC 132,140,28,228\n"
"EX 146\n"
"MA v,132,a,136,a,24\n"
"RD 152,249,29696\n"
"v_accvgpr_mov_b32 a201, a213\n"
"MA v,132,a,132,a,20\n"
"EX 147\n"
"MA v,132,a,128,a,16\n"
"RD 156,250,29696\n"
"CV 195,146,147\n"
"MC 136,128,32,232\n"
"s_add_u32 m0, s69, 90112\n"
"EX 128\n"
"MA v,136,a,132,a,36\n"
"buffer_load_dwordx4 v254, s[48:51], s27 offen nt lds\n"
"v_accvgpr_mov_b32 a202, a214\n"
"MA v,136,a,136,a,40\n"
"EX 129\n"
"MA v,136,a,140,a,44\n"
"TR 160,251,33792\n"
"CV 208,128,129\n"
"MC 140,140,60,236\n"
"L_ST8_%=:\n"
"s_cbranch_execz L_VB1_%=\n"
"EX 130\n"
"MA v,140,a,136,a,56\n"
"TR 162,251,37888\n"
"v_accvgpr_mov_b32 a203, a215\n"
"MA v,140,a,132,a,52\n"
"EX 131\n"
"MA v,140,a,128,a,48\n"
"s_waitcnt lgkmcnt(2)\n"
"TR 164,251,41984\n"
"CV 209,130,131\n"
"MC 156,144,48,236\n"
"EX 132\n"
"MA v,156,a,148,a,52\n"
"TR 166,251,46080\n"
"v_accvgpr_mov_b32 a212, a224\n"
"MA v,156,a,152,a,56\n"
"EX 133\n"
"MA v,156,a,156,a,60\n"
"TR 168,252,33792\n"
"CV 212,132,133\n"
"MC 152,156,44,232\n"
"s_add_u32 m0, s69, 91008\n"
"EX 134\n"
"MA v,152,a,152,a,40\n"
"buffer_load_dwordx4 v254, s[48:51], s27 offen offset:128 nt lds\n"
"v_accvgpr_mov_b32 a213, a225\n"
"MA v,152,a,148,a,36\n"
"s_add_u32 s26, s70, s71\n"
"EX 135\n"
"MA v,152,a,144,a,32\n"
"TR 170,252,37888\n"
"CV 213,134,135\n"
"MC 148,144,16,228\n"
"s_and_b32 s26, s26, 0x3ffff\n"
"EX 136\n"
"MA v,148,a,148,a,20\n"
"TR 172,252,41984\n"
"v_accvgpr_mov_b32 a214, a226\n"
"MA v,148,a,152,a,24\n"
"s_and_b32 s81, s70, 0xfffc0000\n"
"EX 137\n"
"MA v,148,a,156,a,28\n"
"TR 174,252,46080\n"
"CV 216,136,137\n"
"MC 144,156,12,224\n"
"EX 138\n"
"MA v,144,a,152,a,8\n"
"TR 176,251,34304\n"
"v_accvgpr_mov_b32 a215, a227\n"
"MA v,144,a,148,a,4\n"
"s_or_b32 s26, s26, s81\n"
"EX 139\n"
"MA v,144,a,144,a,0\n"
"s_waitcnt lgkmcnt(5)\n"
"TR 178,251,38400\n"
"CV 217,138,139\n"
"MA v,64,v,160,a,160\n"
"EX 140\n"
"MA v,64,v,176,a,164\n"
"TR 180,251,42496\n"
"MA v,68,v,180,a,164\n"
"s_add_u32 s70, s70, 8192\n"
"EX 141\n"
"MA v,68,v,164,a,160\n"
"TR 182,251,46592\n"
"CV 220,140,141\n"
"MA v,72,v,168,a,160\n"
"s_add_u32 m0, s69, 8192\n"
"EX 142\n"
"MA v,72,v,184,a,164\n"
"buffer_load_dwordx4 v253, s[44:47], s25 offen lds\n"
"MA v,76,v,188,a,164\n"
"EX 143\n"
"MA v,76,v,172,a,160\n"
"s_waitcnt lgkmcnt(4)\n"
"TR 184,252,34304\n"
"CV 221,142,143\n"
"MA v,92,v,172,a,168\n"
"EX 156\n"
"MA v,92,v,188,a,172\n"
"TR 186,252,38400\n"
"MA v,88,v,184,a,172\n"
"EX 157\n"
"MA v,88,v,168,a,168\n"
"CV 222,156,157\n"
"MA v,84,v,164,a,168\n"
"EX 158\n"
"MA v,84,v,180,a,172\n"
"TR 188,252,42496\n"
"MA v,80,v,176,a,172\n"
"EX 159\n"
"MA v,80,v,160,a,168\n"
"s_waitcnt lgkmcnt(3)\n"
"TR 190,252,46592\n"
"CV 223,158,159\n"
"MA v,96,v,160,a,176\n"
"s_add_u32 m0, s69, 9088\n"
"EX 152\n"
"MA v,96,v,176,a,180\n"
"s_memrealtime vcc\n"
"buffer_load_dwordx4 v253, s[44:47], s25 offen offset:128 lds\n"
"MA v,100,v,180,a,180\n"
"EX 153\n"
"MA v,100,v,164,a,176\n"
"CV 218,152,153\n"
"MA v,104,v,168,a,176\n"
"s_waitcnt lgkmcnt(0)\n"
"EX 154\n"
"MA v,104,v,184,a,180\n"
"s_waitcnt vmcnt(18)\n"
"MA v,108,v,188,a,180\n"
"s_barrier\n"
"EX 155\n"
"MA v,108,v,172,a,176\n"
"RD 128,249,32768\n"
"CV 219,154,155\n"
"MA v,124,v,172,a,184\n"
"s_sub_u32 s33, s72, vcc_lo\n"
"EX 148\n"
"MA v,124,v,188,a,188\n"
"s_add_u32 s72, vcc_lo, s77\n"
"RD 132,250,32768\n"
"MA v,120,v,184,a,188\n"
"s_max_i32 s33, s33, -150\n"
"EX 149\n"
"MA v,120,v,168,a,184\n"
"s_add_u32 s72, s72, s33\n"
"RD 136,249,33792\n"
"CV 214,148,149\n"
"MA v,116,v,164,a,184\n"
"s_cmp_gt_i32 s33, 0\n"
"EX 150\n"
"MA v,116,v,180,a,188\n"
"s_cbranch_scc1 L_PACE4_%=\n"
"L_PACEB4_%=:\n"
"RD 140,250,33792\n"
"MA v,112,v,176,a,188\n"
"EX 151\n"
"MA v,112,v,160,a,184\n"
"CV 215,150,151\n"
"MA v,240,v,192,a,236\n"
"MA v,240,v,196,a,240\n"
"MA v,240,v,200,a,244\n"
"MA v,240,v,204,a,248\n"
"s_waitcnt lgkmcnt(0)\n"
"s_branch L_P3_%=\n"
"L_X31_%=:\n"
"MZ v,128,a,128,a,64\n"
"EX 144\n"
"MA v,128,a,132,a,68\n"
"RD 144,249,4096\n"
"MA v,128,a,136,a,72\n"
"s_lshl2_add_u32 s79, s69, 131072\n"
"EX 145\n"
"MA v,128,a,140,a,76\n"
"RD 148,250,4096\n"
"CV 210,144,145\n"
"MZ v,132,a,140,a,92\n"
"EX 146\n"
"MA v,132,a,136,a,88\n"
"RD 152,249,5120\n"
"MA v,132,a,132,a,84\n"
"EX 147\n"
"MA v,132,a,128,a,80\n"
"RD 156,250,5120\n"
"CV 211,146,147\n"
"MZ v,136,a,128,a,96\n"
"s_add_u32 m0, s69, 65536\n"
"v_lshrrev_b32 v234, 8, v255\n"
"v_bfe_u32 v248, v255, 6, 2\n"
"MA v,136,a,132,a,100\n"
"buffer_load_dwordx4 v254, s[48:51], s24 offen nt lds\n"
"v_xor_b32 v234, v234, v248\n"
"MA v,136,a,136,a,104\n"
"v_and_b32 v248, 0xf0, v255\n"
"v_lshl_add_u32 v248, v248, 1, s79\n"
"MA v,136,a,140,a,108\n"
"TR 160,251,16384\n"
"v_lshl_add_u32 v248, v234, 3, v248\n"
"MA v,240,v,208,a,236\n"
"MA v,240,v,212,a,240\n"
"MA v,240,v,216,a,244\n"
"MA v,240,v,220,a,248\n"
"MZ v,140,a,140,a,124\n"
"MA v,140,a,136,a,120\n"
"TR 162,251,20480\n"
"MA v,140,a,132,a,116\n"
"MA v,140,a,128,a,112\n"
"s_waitcnt lgkmcnt(2)\n"
"TR 164,251,24576\n"
"MZ v,156,a,144,a,112\n"
"MA v,156,a,148,a,116\n"
"TR 166,251,28672\n"
"v_add_f32 v226, v240, v241\n"
"v_add_f32 v230, v242, v243\n"
"MA v,156,a,152,a,120\n"
"AD 226,230\n"
"v_cmp_nge_f32 s[82:83], s84, v226\n"
"MA v,156,a,156,a,124\n"
"TR 168,252,16384\n"
"v_rcp_f32 v244, v240\n"
"MZ v,152,a,156,a,108\n"
"s_add_u32 m0, s69, 66432\n"
"v_rcp_f32 v245, v241\n"
"MA v,152,a,152,a,104\n"
"buffer_load_dwordx4 v254, s[48:51], s24 offen offset:128 nt lds\n"
"v_rcp_f32 v246, v242\n"
"MA v,152,a,148,a,100\n"
"s_add_u32 s27, s70, s71\n"
"v_rcp_f32 v247, v243\n"
"v_max3_f32 v224, v128, v129, v130\n"
"MA v,152,a,144,a,96\n"
"TR 170,252,20480\n"
"v_max3_f32 v228, v132, v133, v134\n"
"v_max3_f32 v232, v136, v137, v138\n"
"MZ v,148,a,144,a,80\n"
"s_and_b32 s27, s27, 0x3ffff\n"
"v_max3_f32 v236, v140, v141, v142\n"
"v_accvgpr_read_b32 v225, a232\n"
"v_accvgpr_read_b32 v229, a233\n"
"MA v,148,a,148,a,84\n"
"TR 172,252,24576\n"
"v_accvgpr_read_b32 v233, a234\n"
"v_accvgpr_read_b32 v237, a235\n"
"MA v,148,a,152,a,88\n"
"s_and_b32 s81, s70, 0xfffc0000\n"
"v_max3_f32 v224, v224, v131, v225\n"
"v_max3_f32 v228, v228, v135, v229\n"
"v_max3_f32 v232, v232, v139, v233\n"
"MA v,148,a,156,a,92\n"
"TR 174,252,28672\n"
"v_max3_f32 v236, v236, v143, v237\n"
"v_mov_b32 v225, v224\n"
"MZ v,144,a,156,a,76\n"
"v_mov_b32 v229, v228\n"
"v_mov_b32 v233, v232\n"
"v_mov_b32 v237, v236\n"
"MA v,144,a,152,a,72\n"
"TR 176,251,16896\n"
"v_permlane32_swap_b32 v225, v224\n"
"v_permlane32_swap_b32 v229, v228\n"
"MA v,144,a,148,a,68\n"
"s_or_b32 s27, s27, s81\n"
"v_permlane32_swap_b32 v233, v232\n"
"v_permlane32_swap_b32 v237, v236\n"
"v_max_f32 v224, v224, v225\n"
"MA v,144,a,144,a,64\n"
"s_waitcnt lgkmcnt(5)\n"
"TR 178,251,20992\n"
"v_max_f32 v228, v228, v229\n"
"v_max_f32 v232, v232, v233\n"
"MA v,0,v,192,a,160\n"
"v_max_f32 v236, v236, v237\n"
"v_mov_b32 v225, v224\n"
"v_mov_b32 v229, v228\n"
"MA v,0,v,208,a,164\n"
"TR 180,251,25088\n"
"v_mov_b32 v233, v232\n"
"v_mov_b32 v237, v236\n"
"MA v,4,v,212,a,164\n"
"s_add_u32 s70, s70, 8192\n"
"v_permlane16_swap_b32 v225, v224\n"
"v_permlane16_swap_b32 v229, v228\n"
"v_permlane16_swap_b32 v233, v232\n"
"MA v,4,v,196,a,160\n"
"TR 182,251,29184\n"
"v_permlane16_swap_b32 v237, v236\n"
"v_max_f32 v224, v224, v225\n"
"MA v,8,v,200,a,160\n"
"s_add_u32 m0, s69, 49152\n"
"v_max_f32 v228, v228, v229\n"
"v_max_f32 v232, v232, v233\n"
"v_max_f32 v236, v236, v237\n"
"MA v,8,v,216,a,164\n"
"buffer_load_dwordx4 v253, s[44:47], s26 offen lds\n"
"v_sub_f32 v227, 0, v224\n"
"v_sub_f32 v231, 0, v228\n"
"MA v,12,v,220,a,164\n"
"v_sub_f32 v235, 0, v232\n"
"v_sub_f32 v239, 0, v236\n"
"v_sub_f32 v226, 0, v224\n"
"MA v,12,v,204,a,160\n"
"s_waitcnt lgkmcnt(4)\n"
"TR 184,252,16896\n"
"v_sub_f32 v230, 0, v228\n"
"v_sub_f32 v234, 0, v232\n"
"MA v,28,v,204,a,168\n"
"v_sub_f32 v238, 0, v236\n"
"v_sub_f32 v225, 0, v224\n"
"v_sub_f32 v229, 0, v228\n"
"MA v,28,v,220,a,172\n"
"TR 186,252,20992\n"
"v_sub_f32 v233, 0, v232\n"
"v_sub_f32 v237, 0, v236\n"
"MA v,24,v,216,a,172\n"
"v_sub_f32 v224, 0, v224\n"
"v_sub_f32 v228, 0, v228\n"
"v_sub_f32 v232, 0, v232\n"
"MA v,24,v,200,a,168\n"
"v_sub_f32 v236, 0, v236\n"
"AD 128,224\n"
"AD 129,224\n"
"MA v,20,v,196,a,168\n"
"AD 130,224\n"
"AD 131,224\n"
"AD 132,228\n"
"MA v,20,v,212,a,172\n"
"TR 188,252,25088\n"
"AD 133,228\n"
"AD 134,228\n"
"MA v,16,v,208,a,172\n"
"AD 135,228\n"
"AD 136,232\n"
"AD 137,232\n"
"MA v,16,v,192,a,168\n"
"s_waitcnt lgkmcnt(3)\n"
"TR 190,252,29184\n"
"AD 138,232\n"
"AD 139,232\n"
"MA v,32,v,192,a,176\n"
"s_add_u32 m0, s69, 50048\n"
"AD 140,236\n"
"AD 141,236\n"
"AD 142,236\n"
"MA v,32,v,208,a,180\n"
"buffer_load_dwordx4 v253, s[44:47], s26 offen offset:128 lds\n"
"AD 143,236\n"
"AD 156,236\n"
"MA v,36,v,212,a,180\n"
"AD 157,236\n"
"AD 158,236\n"
"AD 159,236\n"
"MA v,36,v,196,a,176\n"
"AD 152,232\n"
"AD 153,232\n"
"AD 154,232\n"
"MA v,40,v,200,a,176\n"
"AD 155,232\n"
"AD 148,228\n"
"AD 149,228\n"
"MA v,40,v,216,a,180\n"
"AD 150,228\n"
"AD 151,228\n"
"AD 144,224\n"
"MA v,44,v,220,a,180\n"
"AD 145,224\n"
"AD 146,224\n"
"AD 147,224\n"
"MA v,44,v,204,a,176\n"
"s_waitcnt lgkmcnt(0)\n"
"RD 128,249,8192\n"
"EX 128\n"
"MA v,60,v,204,a,184\n"
"EX 129\n"
"MA v,60,v,220,a,188\n"
"RD 132,250,8192\n"
"EX 130\n"
"MA v,56,v,216,a,188\n"
"EX 131\n"
"MA v,56,v,200,a,184\n"
"RD 136,249,9216\n"
"EX 132\n"
"MA v,52,v,196,a,184\n"
"EX 133\n"
"MA v,52,v,212,a,188\n"
"RD 140,250,9216\n"
"EX 134\n"
"MA v,48,v,208,a,188\n"
"EX 135\n"
"MA v,48,v,192,a,184\n"
"EX 136\n"
"EX 137\n"
"EX 138\n"
"EX 139\n"
"EX 140\n"
"EX 141\n"
"EX 142\n"
"EX 143\n"
"EX 156\n"
"EX 157\n"
"EX 158\n"
"EX 159\n"
"EX 152\n"
"EX 153\n"
"EX 154\n"
"EX 155\n"
"EX 148\n"
"EX 149\n"
"EX 150\n"
"EX 151\n"
"CV 160,128,129\n"
"CV 161,130,131\n"
"CV 164,132,133\n"
"CV 165,134,135\n"
"CV 168,136,137\n"
"CV 169,138,139\n"
"CV 172,140,141\n"
"CV 173,142,143\n"
"CV 174,156,157\n"
"CV 175,158,159\n"
"CV 170,152,153\n"
"CV 171,154,155\n"
"CV 166,148,149\n"
"CV 167,150,151\n"
"s_cmp_eq_u64 s[82:83], 0\n"
"s_cselect_b32 s79, 1, s74\n"
"s_lshl_b32 s79, s79, 0\n"
"s_and_b32 s79, s79, s75\n"
"s_cselect_b32 s58, 32768, 0\n"
"s_xor_b32 s75, s75, s79\n"
"s_lshl2_add_u32 s79, s69, 131072\n"
"s_waitcnt lgkmcnt(3)\n"
"MC 128,128,64,224\n"
"s_waitcnt lgkmcnt(2)\n"
"EX 144\n"
"MA v,128,a,132,a,68\n"
"s_waitcnt lgkmcnt(1)\n"
"RD 144,249,12288\n"
"MA v,128,a,136,a,72\n"
"s_waitcnt lgkmcnt(1)\n"
"EX 145\n"
"MA v,128,a,140,a,76\n"
"RD 148,250,12288\n"
"CV 162,144,145\n"
"MC 132,140,92,228\n"
"EX 146\n"
"v_mul_f32_dpp v48, v244, v48 row_newbcast:0 row_mask:0xf bank_mask:0xf\n"
"MA v,132,a,136,a,88\n"
"RD 152,249,13312\n"
"v_mul_f32_dpp v49, v245, v49 row_newbcast:0 row_mask:0xf bank_mask:0xf\n"
"v_mul_f32_dpp v50, v246, v50 row_newbcast:0 row_mask:0xf bank_mask:0xf\n"
"MA v,132,a,132,a,84\n"
"EX 147\n"
"v_mul_f32_dpp v51, v247, v51 row_newbcast:0 row_mask:0xf bank_mask:0xf\n"
"MA v,132,a,128,a,80\n"
"RD 156,250,13312\n"
"CV 163,146,147\n"
"CV 48,48,49\n"
"MC 136,128,96,232\n"
"s_add_u32 m0, s69, 73728\n"
"EX 128\n"
"CV 49,50,51\n"
"MA v,136,a,132,a,100\n"
"buffer_load_dwordx4 v254, s[48:51], s25 offen nt lds\n"
"v_lshrrev_b32 v50, 4, v255\n"
"v_and_b32 v50, 0x30, v50\n"
"MA v,136,a,136,a,104\n"
"EX 129\n"
"v_xor_b32 v50, v50, v255\n"
"MA v,136,a,140,a,108\n"
"TR 160,251,17408\n"
"CV 176,128,129\n"
"v_lshrrev_b32 v50, 1, v50\n"
"MC 140,140,124,236\n"
"EX 130\n"
"v_add_u32 v50, s79, v50\n"
"MA v,140,a,136,a,120\n"
"TR 162,251,21504\n"
"v_mul_f32_dpp v0, v244, v0 row_newbcast:0 row_mask:0xf bank_mask:0xf\n"
"v_mul_f32_dpp v1, v245, v1 row_newbcast:0 row_mask:0xf bank_mask:0xf\n"
"MA v,140,a,132,a,116\n"
"ds_write_b64 v248, v[48:49] offset:0\n"
"EX 131\n"
"v_mul_f32_dpp v2, v246, v2 row_newbcast:0 row_mask:0xf bank_mask:0xf\n"
"MA v,140,a,128,a,112\n"
"s_waitcnt lgkmcnt(3)\n"
"TR 164,251,25600\n"
"CV 177,130,131\n"
"v_mul_f32_dpp v3, v247, v3 row_newbcast:0 row_mask:0xf bank_mask:0xf\n"
"MC 156,144,112,236\n"
"EX 132\n"
"CV 0,0,1\n"
"MA v,156,a,148,a,116\n"
"TR 166,251,29696\n"
"CV 1,2,3\n"
"v_mul_f32_dpp v4, v244, v4 row_newbcast:1 row_mask:0xf bank_mask:0xf\n"
"MA v,156,a,152,a,120\n"
"EX 133\n"
"v_mul_f32_dpp v5, v245, v5 row_newbcast:1 row_mask:0xf bank_mask:0xf\n"
"MA v,156,a,156,a,124\n"
"TR 168,252,17408\n"
"CV 180,132,133\n"
"v_mul_f32_dpp v6, v246, v6 row_newbcast:1 row_mask:0xf bank_mask:0xf\n"
"MC 152,156,108,232\n"
"s_add_u32 m0, s69, 74624\n"
"EX 134\n"
"v_mul_f32_dpp v7, v247, v7 row_newbcast:1 row_mask:0xf bank_mask:0xf\n"
"MA v,152,a,152,a,104\n"
"buffer_load_dwordx4 v254, s[48:51], s25 offen offset:128 nt lds\n"
"CV 4,4,5\n"
"CV 5,6,7\n"
"MA v,152,a,148,a,100\n"
"s_add_u32 s24, s70, s71\n"
"ds_write_b64 v248, v[0:1] offset:512\n"
"EX 135\n"
"v_mul_f32_dpp v8, v244, v8 row_newbcast:2 row_mask:0xf bank_mask:0xf\n"
"MA v,152,a,144,a,96\n"
"TR 170,252,21504\n"
"CV 181,134,135\n"
"v_mul_f32_dpp v9, v245, v9 row_newbcast:2 row_mask:0xf bank_mask:0xf\n"
"MC 148,144,80,228\n"
"s_and_b32 s24, s24, 0x3ffff\n"
"EX 136\n"
"v_mul_f32_dpp v10, v246, v10 row_newbcast:2 row_mask:0xf bank_mask:0xf\n"
"MA v,148,a,148,a,84\n"
"TR 172,252,25600\n"
"v_mul_f32_dpp v11, v247, v11 row_newbcast:2 row_mask:0xf bank_mask:0xf\n"
"CV 8,8,9\n"
"MA v,148,a,152,a,88\n"
"s_and_b32 s81, s70, 0xfffc0000\n"
"ds_write_b64 v248, v[4:5] offset:1024\n"
"EX 137\n"
"CV 9,10,11\n"
"MA v,148,a,156,a,92\n"
"TR 174,252,29696\n"
"CV 184,136,137\n"
"v_mul_f32_dpp v12, v244, v12 row_newbcast:3 row_mask:0xf bank_mask:0xf\n"
"MC 144,156,76,224\n"
"TR 18,50,0\n"
"EX 138\n"
"v_mul_f32_dpp v13, v245, v13 row_newbcast:3 row_mask:0xf bank_mask:0xf\n"
"MA v,144,a,152,a,72\n"
"TR 176,251,17920\n"
"v_mul_f32_dpp v14, v246, v14 row_newbcast:3 row_mask:0xf bank_mask:0xf\n"
"v_mul_f32_dpp v15, v247, v15 row_newbcast:3 row_mask:0xf bank_mask:0xf\n"
"MA v,144,a,148,a,68\n"
"s_or_b32 s24, s24, s81\n"
"ds_write_b64 v248, v[8:9] offset:1536\n"
"EX 139\n"
"CV 12,12,13\n"
"MA v,144,a,144,a,64\n"
"s_waitcnt lgkmcnt(9)\n"
"TR 178,251,22016\n"
"CV 185,138,139\n"
"CV 13,14,15\n"
"MA v,64,v,192,a,160\n"
"TR 0,50,512\n"
"EX 140\n"
"v_mul_f32_dpp v28, v244, v28 row_newbcast:3 row_mask:0xf bank_mask:0xf\n"
"MA v,64,v,208,a,164\n"
"TR 180,251,26112\n"
"v_mul_f32_dpp v29, v245, v29 row_newbcast:3 row_mask:0xf bank_mask:0xf\n"
"v_mul_f32_dpp v30, v246, v30 row_newbcast:3 row_mask:0xf bank_mask:0xf\n"
"MA v,68,v,212,a,164\n"
"s_add_u32 s70, s70, 8192\n"
"EX 141\n"
"v_mul_f32_dpp v31, v247, v31 row_newbcast:3 row_mask:0xf bank_mask:0xf\n"
"MA v,68,v,196,a,160\n"
"TR 182,251,30208\n"
"CV 188,140,141\n"
"CV 28,28,29\n"
"MA v,72,v,200,a,160\n"
"s_add_u32 m0, s69, 57344\n"
"ds_write_b64 v248, v[12:13] offset:2048\n"
"EX 142\n"
"CV 29,30,31\n"
"MA v,72,v,216,a,164\n"
"buffer_load_dwordx4 v253, s[44:47], s27 offen lds\n"
"v_mul_f32_dpp v24, v244, v24 row_newbcast:2 row_mask:0xf bank_mask:0xf\n"
"v_mul_f32_dpp v25, v245, v25 row_newbcast:2 row_mask:0xf bank_mask:0xf\n"
"MA v,76,v,220,a,164\n"
"TR 4,50,1024\n"
"EX 143\n"
"v_mul_f32_dpp v26, v246, v26 row_newbcast:2 row_mask:0xf bank_mask:0xf\n"
"MA v,76,v,204,a,160\n"
"s_waitcnt lgkmcnt(9)\n"
"TR 184,252,17920\n"
"CV 189,142,143\n"
"v_mul_f32_dpp v27, v247, v27 row_newbcast:2 row_mask:0xf bank_mask:0xf\n"
"MA v,92,v,204,a,168\n"
"ds_write_b64 v248, v[28:29] offset:2560\n"
"EX 156\n"
"CV 24,24,25\n"
"MA v,92,v,220,a,172\n"
"TR 186,252,22016\n"
"CV 25,26,27\n"
"v_mul_f32_dpp v20, v244, v20 row_newbcast:1 row_mask:0xf bank_mask:0xf\n"
"MA v,88,v,216,a,172\n"
"TR 8,50,1536\n"
"EX 157\n"
"v_mul_f32_dpp v21, v245, v21 row_newbcast:1 row_mask:0xf bank_mask:0xf\n"
"MA v,88,v,200,a,168\n"
"CV 190,156,157\n"
"v_mul_f32_dpp v22, v246, v22 row_newbcast:1 row_mask:0xf bank_mask:0xf\n"
"v_mul_f32_dpp v23, v247, v23 row_newbcast:1 row_mask:0xf bank_mask:0xf\n"
"MA v,84,v,196,a,168\n"
"EX 158\n"
"CV 20,20,21\n"
"MA v,84,v,212,a,172\n"
"TR 188,252,26112\n"
"CV 21,22,23\n"
"v_mul_f32_dpp v16, v244, v16 row_newbcast:0 row_mask:0xf bank_mask:0xf\n"
"MA v,80,v,208,a,172\n"
"ds_write_b64 v248, v[24:25] offset:3072\n"
"EX 159\n"
"v_mul_f32_dpp v17, v245, v17 row_newbcast:0 row_mask:0xf bank_mask:0xf\n"
"MA v,80,v,192,a,168\n"
"s_waitcnt lgkmcnt(8)\n"
"TR 190,252,30208\n"
"CV 191,158,159\n"
"v_mul_f32_dpp v18, v246, v18 row_newbcast:0 row_mask:0xf bank_mask:0xf\n"
"MA v,96,v,192,a,176\n"
"s_add_u32 m0, s69, 58240\n"
"TR 12,50,2048\n"
"EX 152\n"
"v_mul_f32_dpp v19, v247, v19 row_newbcast:0 row_mask:0xf bank_mask:0xf\n"
"MA v,96,v,208,a,180\n"
"s_memrealtime vcc\n"
"buffer_load_dwordx4 v253, s[44:47], s27 offen offset:128 lds\n"
"CV 16,16,17\n"
"CV 17,18,19\n"
"MA v,100,v,212,a,180\n"
"ds_write_b64 v248, v[20:21] offset:3584\n"
"EX 153\n"
"v_mul_f32_dpp v32, v244, v32 row_newbcast:0 row_mask:0xf bank_mask:0xf\n"
"MA v,100,v,196,a,176\n"
"TR 14,50,2560\n"
"CV 186,152,153\n"
"v_mul_f32_dpp v33, v245, v33 row_newbcast:0 row_mask:0xf bank_mask:0xf\n"
"v_mul_f32_dpp v34, v246, v34 row_newbcast:0 row_mask:0xf bank_mask:0xf\n"
"MA v,104,v,200,a,176\n"
"s_waitcnt lgkmcnt(0)\n"
"EX 154\n"
"v_mul_f32_dpp v35, v247, v35 row_newbcast:0 row_mask:0xf bank_mask:0xf\n"
"MA v,104,v,216,a,180\n"
"L_ST9_%=:\n"
"s_cbranch_execz L_VB2_%=\n"
"s_waitcnt vmcnt(17)\n"
"ds_write_b64 v248, v[16:17] offset:4096\n"
"CV 32,32,33\n"
"CV 33,34,35\n"
"v_mul_f32_dpp v36, v244, v36 row_newbcast:1 row_mask:0xf bank_mask:0xf\n"
"MA v,108,v,220,a,180\n"
"s_barrier\n"
"TR 10,50,3072\n"
"EX 155\n"
"v_mul_f32_dpp v37, v245, v37 row_newbcast:1 row_mask:0xf bank_mask:0xf\n"
"MA v,108,v,204,a,176\n"
"RD 128,249,16384\n"
"CV 187,154,155\n"
"v_mul_f32_dpp v38, v246, v38 row_newbcast:1 row_mask:0xf bank_mask:0xf\n"
"MA v,124,v,204,a,184\n"
"s_sub_u32 s33, s72, vcc_lo\n"
"EX 148\n"
"v_mul_f32_dpp v39, v247, v39 row_newbcast:1 row_mask:0xf bank_mask:0xf\n"
"MA v,124,v,220,a,188\n"
"s_add_u32 s72, vcc_lo, s77\n"
"RD 132,250,16384\n"
"CV 36,36,37\n"
"CV 37,38,39\n"
"MA v,120,v,216,a,188\n"
"s_max_i32 s33, s33, -150\n"
"ds_write_b64 v248, v[32:33] offset:4608\n"
"EX 149\n"
"v_mul_f32_dpp v40, v244, v40 row_newbcast:2 row_mask:0xf bank_mask:0xf\n"
"MA v,120,v,200,a,184\n"
"s_add_u32 s72, s72, s33\n"
"RD 136,249,17408\n"
"CV 182,148,149\n"
"v_mul_f32_dpp v41, v245, v41 row_newbcast:2 row_mask:0xf bank_mask:0xf\n"
"MA v,116,v,196,a,184\n"
"s_cmp_gt_i32 s33, 0\n"
"TR 6,50,3584\n"
"EX 150\n"
"v_mul_f32_dpp v42, v246, v42 row_newbcast:2 row_mask:0xf bank_mask:0xf\n"
"MA v,116,v,212,a,188\n"
"s_cbranch_scc1 L_PACE5_%=\n"
"L_PACEB5_%=:\n"
"RD 140,250,17408\n"
"v_mul_f32_dpp v43, v247, v43 row_newbcast:2 row_mask:0xf bank_mask:0xf\n"
"CV 40,40,41\n"
"MA v,112,v,208,a,188\n"
"ds_write_b64 v248, v[36:37] offset:5120\n"
"EX 151\n"
"CV 41,42,43\n"
"MA v,112,v,192,a,184\n"
"TR 2,50,4096\n"
"CV 183,150,151\n"
"v_mul_f32_dpp v44, v244, v44 row_newbcast:3 row_mask:0xf bank_mask:0xf\n"
"v_mul_f32_dpp v45, v245, v45 row_newbcast:3 row_mask:0xf bank_mask:0xf\n"
"MZ v,240,v,160,a,236\n"
"ds_write_b64 v248, v[40:41] offset:5632\n"
"v_mul_f32_dpp v46, v246, v46 row_newbcast:3 row_mask:0xf bank_mask:0xf\n"
"MA v,240,v,164,a,240\n"
"TR 16,50,4608\n"
"MA v,240,v,168,a,244\n"
"TR 20,50,5120\n"
"MA v,240,v,172,a,248\n"
"s_waitcnt lgkmcnt(5)\n"
"TR 24,50,5632\n"
"MC 128,128,64,224\n"
"EX 144\n"
"v_mul_f32_dpp v47, v247, v47 row_newbcast:3 row_mask:0xf bank_mask:0xf\n"
"MA v,128,a,132,a,68\n"
"RD 144,249,20480\n"
"CV 44,44,45\n"
"CV 45,46,47\n"
"MA v,128,a,136,a,72\n"
"EX 145\n"
"v_mul_f32_dpp v60, v244, v60 row_newbcast:3 row_mask:0xf bank_mask:0xf\n"
"MA v,128,a,140,a,76\n"
"RD 148,250,20480\n"
"CV 178,144,145\n"
"v_mul_f32_dpp v61, v245, v61 row_newbcast:3 row_mask:0xf bank_mask:0xf\n"
"MC 132,140,92,228\n"
"EX 146\n"
"v_mul_f32_dpp v62, v246, v62 row_newbcast:3 row_mask:0xf bank_mask:0xf\n"
"MA v,132,a,136,a,88\n"
"RD 152,249,21504\n"
"v_mul_f32_dpp v63, v247, v63 row_newbcast:3 row_mask:0xf bank_mask:0xf\n"
"CV 60,60,61\n"
"MA v,132,a,132,a,84\n"
"ds_write_b64 v248, v[44:45] offset:6144\n"
"EX 147\n"
"CV 61,62,63\n"
"MA v,132,a,128,a,80\n"
"RD 156,250,21504\n"
"CV 179,146,147\n"
"v_mul_f32_dpp v56, v244, v56 row_newbcast:2 row_mask:0xf bank_mask:0xf\n"
"MC 136,128,96,232\n"
"s_add_u32 m0, s69, 81920\n"
"EX 128\n"
"v_mul_f32_dpp v57, v245, v57 row_newbcast:2 row_mask:0xf bank_mask:0xf\n"
"MA v,136,a,132,a,100\n"
"buffer_load_dwordx4 v254, s[48:51], s26 offen nt lds\n"
"v_mul_f32_dpp v58, v246, v58 row_newbcast:2 row_mask:0xf bank_mask:0xf\n"
"v_mul_f32_dpp v59, v247, v59 row_newbcast:2 row_mask:0xf bank_mask:0xf\n"
"MA v,136,a,136,a,104\n"
"ds_write_b64 v248, v[60:61] offset:6656\n"
"EX 129\n"
"CV 56,56,57\n"
"MA v,136,a,140,a,108\n"
"TR 160,251,32768\n"
"CV 192,128,129\n"
"CV 57,58,59\n"
"MC 140,140,124,236\n"
"EX 130\n"
"v_mul_f32_dpp v52, v244, v52 row_newbcast:1 row_mask:0xf bank_mask:0xf\n"
"MA v,140,a,136,a,120\n"
"TR 162,251,36864\n"
"v_mul_f32_dpp v53, v245, v53 row_newbcast:1 row_mask:0xf bank_mask:0xf\n"
"v_mul_f32_dpp v54, v246, v54 row_newbcast:1 row_mask:0xf bank_mask:0xf\n"
"MA v,140,a,132,a,116\n"
"EX 131\n"
"v_mul_f32_dpp v55, v247, v55 row_newbcast:1 row_mask:0xf bank_mask:0xf\n"
"MA v,140,a,128,a,112\n"
"s_waitcnt lgkmcnt(3)\n"
"TR 164,251,40960\n"
"CV 193,130,131\n"
"CV 52,52,53\n"
"MC 156,144,112,236\n"
"ds_write_b64 v248, v[56:57] offset:7168\n"
"EX 132\n"
"CV 53,54,55\n"
"MA v,156,a,148,a,116\n"
"TR 166,251,45056\n"
"v_mul_f32_dpp v112, v244, v112 row_newbcast:0 row_mask:0xf bank_mask:0xf\n"
"v_mul_f32_dpp v113, v245, v113 row_newbcast:0 row_mask:0xf bank_mask:0xf\n"
"MA v,156,a,152,a,120\n"
"TR 28,50,6144\n"
"EX 133\n"
"v_mul_f32_dpp v114, v246, v114 row_newbcast:0 row_mask:0xf bank_mask:0xf\n"
"MA v,156,a,156,a,124\n"
"TR 168,252,32768\n"
"CV 196,132,133\n"
"v_mul_f32_dpp v115, v247, v115 row_newbcast:0 row_mask:0xf bank_mask:0xf\n"
"MC 152,156,108,232\n"
"s_add_u32 m0, s69, 82816\n"
"ds_write_b64 v248, v[52:53] offset:7680\n"
"EX 134\n"
"CV 112,112,113\n"
"MA v,152,a,152,a,104\n"
"buffer_load_dwordx4 v254, s[48:51], s26 offen offset:128 nt lds\n"
"CV 113,114,115\n"
"v_mov_b32 v114, v50\n"
"MA v,152,a,148,a,100\n"
"s_add_u32 s25, s70, s71\n"
"TR 30,114,6656\n"
"EX 135\n"
"v_mul_f32_dpp v64, v244, v64 row_newbcast:0 row_mask:0xf bank_mask:0xf\n"
"MA v,152,a,144,a,96\n"
"TR 170,252,36864\n"
"CV 197,134,135\n"
"v_mul_f32_dpp v65, v245, v65 row_newbcast:0 row_mask:0xf bank_mask:0xf\n"
"MC 148,144,80,228\n"
"s_and_b32 s25, s25, 0x3ffff\n"
"EX 136\n"
"v_mul_f32_dpp v66, v246, v66 row_newbcast:0 row_mask:0xf bank_mask:0xf\n"
"MA v,148,a,148,a,84\n"
"TR 172,252,40960\n"
"v_mul_f32_dpp v67, v247, v67 row_newbcast:0 row_mask:0xf bank_mask:0xf\n"
"CV 64,64,65\n"
"MA v,148,a,152,a,88\n"
"s_and_b32 s81, s70, 0xfffc0000\n"
"ds_write_b64 v248, v[112:113] offset:0\n"
"EX 137\n"
"CV 65,66,67\n"
"MA v,148,a,156,a,92\n"
"TR 174,252,45056\n"
"CV 200,136,137\n"
"v_mul_f32_dpp v68, v244, v68 row_newbcast:1 row_mask:0xf bank_mask:0xf\n"
"MC 144,156,76,224\n"
"TR 26,114,7168\n"
"EX 138\n"
"v_mul_f32_dpp v69, v245, v69 row_newbcast:1 row_mask:0xf bank_mask:0xf\n"
"MA v,144,a,152,a,72\n"
"TR 176,251,33280\n"
"v_mul_f32_dpp v70, v246, v70 row_newbcast:1 row_mask:0xf bank_mask:0xf\n"
"v_mul_f32_dpp v71, v247, v71 row_newbcast:1 row_mask:0xf bank_mask:0xf\n"
"MA v,144,a,148,a,68\n"
"s_or_b32 s25, s25, s81\n"
"ds_write_b64 v248, v[64:65] offset:512\n"
"EX 139\n"
"CV 68,68,69\n"
"MA v,144,a,144,a,64\n"
"s_waitcnt lgkmcnt(11)\n"
"TR 178,251,37376\n"
"CV 201,138,139\n"
"CV 69,70,71\n"
"MZ v,0,v,160,a,160\n"
"TR 22,114,7680\n"
"EX 140\n"
"v_mul_f32_dpp v72, v244, v72 row_newbcast:2 row_mask:0xf bank_mask:0xf\n"
"MA v,0,v,176,a,164\n"
"TR 180,251,41472\n"
"v_mul_f32_dpp v73, v245, v73 row_newbcast:2 row_mask:0xf bank_mask:0xf\n"
"v_mul_f32_dpp v74, v246, v74 row_newbcast:2 row_mask:0xf bank_mask:0xf\n"
"MZ v,4,v,180,a,164\n"
"s_add_u32 s70, s70, 8192\n"
"EX 141\n"
"v_mul_f32_dpp v75, v247, v75 row_newbcast:2 row_mask:0xf bank_mask:0xf\n"
"MA v,4,v,164,a,160\n"
"TR 182,251,45568\n"
"CV 204,140,141\n"
"CV 72,72,73\n"
"MZ v,8,v,168,a,160\n"
"s_add_u32 m0, s69, 0\n"
"ds_write_b64 v248, v[68:69] offset:1024\n"
"EX 142\n"
"CV 73,74,75\n"
"MA v,8,v,184,a,164\n"
"buffer_load_dwordx4 v253, s[44:47], s24 offen nt lds\n"
"v_mul_f32_dpp v76, v244, v76 row_newbcast:3 row_mask:0xf bank_mask:0xf\n"
"v_mul_f32_dpp v77, v245, v77 row_newbcast:3 row_mask:0xf bank_mask:0xf\n"
"MZ v,12,v,188,a,164\n"
"TR 50,114,0\n"
"EX 143\n"
"v_mul_f32_dpp v78, v246, v78 row_newbcast:3 row_mask:0xf bank_mask:0xf\n"
"MA v,12,v,172,a,160\n"
"s_waitcnt lgkmcnt(9)\n"
"TR 184,252,33280\n"
"CV 205,142,143\n"
"v_mul_f32_dpp v79, v247, v79 row_newbcast:3 row_mask:0xf bank_mask:0xf\n"
"MZ v,28,v,172,a,168\n"
"ds_write_b64 v248, v[72:73] offset:1536\n"
"EX 156\n"
"CV 76,76,77\n"
"MA v,28,v,188,a,172\n"
"TR 186,252,37376\n"
"CV 77,78,79\n"
"v_mul_f32_dpp v92, v244, v92 row_newbcast:3 row_mask:0xf bank_mask:0xf\n"
"MZ v,24,v,184,a,172\n"
"TR 32,114,512\n"
"EX 157\n"
"v_mul_f32_dpp v93, v245, v93 row_newbcast:3 row_mask:0xf bank_mask:0xf\n"
"MA v,24,v,168,a,168\n"
"buffer_store_dwordx2 a[252:253], v255, s[52:55], s85 offen sc1\n"
"CV 206,156,157\n"
"MZ v,20,v,164,a,168\n"
"EX 158\n"
"v_mul_f32_dpp v94, v246, v94 row_newbcast:3 row_mask:0xf bank_mask:0xf\n"
"MA v,20,v,180,a,172\n"
"TR 188,252,41472\n"
"v_mul_f32_dpp v95, v247, v95 row_newbcast:3 row_mask:0xf bank_mask:0xf\n"
"CV 92,92,93\n"
"MZ v,16,v,176,a,172\n"
"ds_write_b64 v248, v[76:77] offset:2048\n"
"EX 159\n"
"CV 93,94,95\n"
"MA v,16,v,160,a,168\n"
"s_waitcnt lgkmcnt(8)\n"
"TR 190,252,45568\n"
"CV 207,158,159\n"
"v_mul_f32_dpp v88, v244, v88 row_newbcast:2 row_mask:0xf bank_mask:0xf\n"
"MZ v,32,v,160,a,176\n"
"s_add_u32 m0, s69, 896\n"
"TR 36,114,1024\n"
"EX 152\n"
"v_mul_f32_dpp v89, v245, v89 row_newbcast:2 row_mask:0xf bank_mask:0xf\n"
"MA v,32,v,176,a,180\n"
"buffer_load_dwordx4 v253, s[44:47], s24 offen offset:128 nt lds\n"
"v_mul_f32_dpp v90, v246, v90 row_newbcast:2 row_mask:0xf bank_mask:0xf\n"
"v_mul_f32_dpp v91, v247, v91 row_newbcast:2 row_mask:0xf bank_mask:0xf\n"
"MZ v,36,v,180,a,180\n"
"ds_write_b64 v248, v[92:93] offset:2560\n"
"EX 153\n"
"CV 88,88,89\n"
"MA v,36,v,164,a,176\n"
"TR 40,114,1536\n"
"CV 202,152,153\n"
"CV 89,90,91\n"
"v_mul_f32_dpp v84, v244, v84 row_newbcast:1 row_mask:0xf bank_mask:0xf\n"
"MZ v,40,v,168,a,176\n"
"EX 154\n"
"v_mul_f32_dpp v85, v245, v85 row_newbcast:1 row_mask:0xf bank_mask:0xf\n"
"MA v,40,v,184,a,180\n"
"v_mul_f32_dpp v86, v246, v86 row_newbcast:1 row_mask:0xf bank_mask:0xf\n"
"v_mul_f32_dpp v87, v247, v87 row_newbcast:1 row_mask:0xf bank_mask:0xf\n"
"CV 84,84,85\n"
"MZ v,44,v,188,a,180\n"
"EX 155\n"
"CV 85,86,87\n"
"MA v,44,v,172,a,176\n"
"s_waitcnt lgkmcnt(3)\n"
"RD 128,249,24576\n"
"CV 203,154,155\n"
"v_mul_f32_dpp v80, v244, v80 row_newbcast:0 row_mask:0xf bank_mask:0xf\n"
"MZ v,60,v,172,a,184\n"
"ds_write_b64 v248, v[88:89] offset:3072\n"
"EX 148\n"
"v_mul_f32_dpp v81, v245, v81 row_newbcast:0 row_mask:0xf bank_mask:0xf\n"
"MA v,60,v,188,a,188\n"
"RD 132,250,24576\n"
"v_mul_f32_dpp v82, v246, v82 row_newbcast:0 row_mask:0xf bank_mask:0xf\n"
"v_mul_f32_dpp v83, v247, v83 row_newbcast:0 row_mask:0xf bank_mask:0xf\n"
"MZ v,56,v,184,a,188\n"
"ds_write_b64 v248, v[84:85] offset:3584\n"
"EX 149\n"
"CV 80,80,81\n"
"MA v,56,v,168,a,184\n"
"RD 136,249,25600\n"
"CV 198,148,149\n"
"CV 81,82,83\n"
"MZ v,52,v,164,a,184\n"
"TR 44,114,2048\n"
"EX 150\n"
"v_mul_f32_dpp v96, v244, v96 row_newbcast:0 row_mask:0xf bank_mask:0xf\n"
"MA v,52,v,180,a,188\n"
"RD 140,250,25600\n"
"MZ v,48,v,176,a,188\n"
"TR 46,114,2560\n"
"EX 151\n"
"MA v,48,v,160,a,184\n"
"ds_write_b64 v248, v[80:81] offset:4096\n"
"CV 199,150,151\n"
"MA v,240,v,176,a,236\n"
"s_mov_b32 s80, 0\n"
"TR 42,114,3072\n"
"MA v,240,v,180,a,240\n"
"buffer_store_dwordx4 a[0:3], v255, s[56:59], s80 offen sc1\n"
"MA v,240,v,184,a,244\n"
"TR 38,114,3584\n"
"MA v,240,v,188,a,248\n"
"s_waitcnt lgkmcnt(4)\n"
"TR 34,114,4096\n"
"MC 128,128,64,224\n"
"EX 144\n"
"v_mul_f32_dpp v97, v245, v97 row_newbcast:0 row_mask:0xf bank_mask:0xf\n"
"MA v,128,a,132,a,68\n"
"RD 144,249,28672\n"
"v_mul_f32_dpp v98, v246, v98 row_newbcast:0 row_mask:0xf bank_mask:0xf\n"
"v_mul_f32_dpp v99, v247, v99 row_newbcast:0 row_mask:0xf bank_mask:0xf\n"
"MA v,128,a,136,a,72\n"
"EX 145\n"
"CV 96,96,97\n"
"MA v,128,a,140,a,76\n"
"RD 148,250,28672\n"
"CV 194,144,145\n"
"CV 97,98,99\n"
"MC 132,140,92,228\n"
"EX 146\n"
"v_mul_f32_dpp v100, v244, v100 row_newbcast:1 row_mask:0xf bank_mask:0xf\n"
"MA v,132,a,136,a,88\n"
"RD 152,249,29696\n"
"v_mul_f32_dpp v101, v245, v101 row_newbcast:1 row_mask:0xf bank_mask:0xf\n"
"v_mul_f32_dpp v102, v246, v102 row_newbcast:1 row_mask:0xf bank_mask:0xf\n"
"MA v,132,a,132,a,84\n"
"EX 147\n"
"v_mul_f32_dpp v103, v247, v103 row_newbcast:1 row_mask:0xf bank_mask:0xf\n"
"MA v,132,a,128,a,80\n"
"RD 156,250,29696\n"
"CV 195,146,147\n"
"CV 100,100,101\n"
"MC 136,128,96,232\n"
"s_add_u32 m0, s69, 90112\n"
"ds_write_b64 v248, v[96:97] offset:4608\n"
"EX 128\n"
"CV 101,102,103\n"
"MA v,136,a,132,a,100\n"
"buffer_load_dwordx4 v254, s[48:51], s27 offen nt lds\n"
"v_mul_f32_dpp v104, v244, v104 row_newbcast:2 row_mask:0xf bank_mask:0xf\n"
"v_mul_f32_dpp v105, v245, v105 row_newbcast:2 row_mask:0xf bank_mask:0xf\n"
"MA v,136,a,136,a,104\n"
"EX 129\n"
"v_mul_f32_dpp v106, v246, v106 row_newbcast:2 row_mask:0xf bank_mask:0xf\n"
"MA v,136,a,140,a,108\n"
"TR 160,251,33792\n"
"CV 208,128,129\n"
"v_mul_f32_dpp v107, v247, v107 row_newbcast:2 row_mask:0xf bank_mask:0xf\n"
"MC 140,140,124,236\n"
"ds_write_b64 v248, v[100:101] offset:5120\n"
"EX 130\n"
"CV 104,104,105\n"
"MA v,140,a,136,a,120\n"
"TR 162,251,37888\n"
"CV 105,106,107\n"
"v_mul_f32_dpp v108, v244, v108 row_newbcast:3 row_mask:0xf bank_mask:0xf\n"
"MA v,140,a,132,a,116\n"
"EX 131\n"
"v_mul_f32_dpp v109, v245, v109 row_newbcast:3 row_mask:0xf bank_mask:0xf\n"
"MA v,140,a,128,a,112\n"
"s_waitcnt lgkmcnt(4)\n"
"TR 164,251,41984\n"
"CV 209,130,131\n"
"v_mul_f32_dpp v110, v246, v110 row_newbcast:3 row_mask:0xf bank_mask:0xf\n"
"MC 156,144,112,236\n"
"EX 132\n"
"v_mul_f32_dpp v111, v247, v111 row_newbcast:3 row_mask:0xf bank_mask:0xf\n"
"MA v,156,a,148,a,116\n"
"TR 166,251,46080\n"
"CV 108,108,109\n"
"CV 109,110,111\n"
"MA v,156,a,152,a,120\n"
"ds_write_b64 v248, v[104:105] offset:5632\n"
"EX 133\n"
"v_mul_f32_dpp v124, v244, v124 row_newbcast:3 row_mask:0xf bank_mask:0xf\n"
"MA v,156,a,156,a,124\n"
"TR 168,252,33792\n"
"CV 212,132,133\n"
"v_mul_f32_dpp v125, v245, v125 row_newbcast:3 row_mask:0xf bank_mask:0xf\n"
"MC 152,156,108,232\n"
"s_add_u32 m0, s69, 91008\n"
"TR 48,114,4608\n"
"EX 134\n"
"v_mul_f32_dpp v126, v246, v126 row_newbcast:3 row_mask:0xf bank_mask:0xf\n"
"MA v,152,a,152,a,104\n"
"buffer_load_dwordx4 v254, s[48:51], s27 offen offset:128 nt lds\n"
"v_mul_f32_dpp v127, v247, v127 row_newbcast:3 row_mask:0xf bank_mask:0xf\n"
"CV 124,124,125\n"
"MA v,152,a,148,a,100\n"
"s_add_u32 s26, s70, s71\n"
"ds_write_b64 v248, v[108:109] offset:6144\n"
"EX 135\n"
"CV 125,126,127\n"
"MA v,152,a,144,a,96\n"
"TR 170,252,37888\n"
"CV 213,134,135\n"
"v_mul_f32_dpp v120, v244, v120 row_newbcast:2 row_mask:0xf bank_mask:0xf\n"
"MC 148,144,80,228\n"
"s_and_b32 s26, s26, 0x3ffff\n"
"TR 52,114,5120\n"
"EX 136\n"
"v_mul_f32_dpp v121, v245, v121 row_newbcast:2 row_mask:0xf bank_mask:0xf\n"
"MA v,148,a,148,a,84\n"
"TR 172,252,41984\n"
"v_mul_f32_dpp v122, v246, v122 row_newbcast:2 row_mask:0xf bank_mask:0xf\n"
"v_mul_f32_dpp v123, v247, v123 row_newbcast:2 row_mask:0xf bank_mask:0xf\n"
"MA v,148,a,152,a,88\n"
"s_and_b32 s81, s70, 0xfffc0000\n"
"ds_write_b64 v248, v[124:125] offset:6656\n"
"EX 137\n"
"CV 120,120,121\n"
"MA v,148,a,156,a,92\n"
"TR 174,252,46080\n"
"CV 216,136,137\n"
"CV 121,122,123\n"
"MC 144,156,76,224\n"
"TR 56,114,5632\n"
"EX 138\n"
"v_mul_f32_dpp v116, v244, v116 row_newbcast:1 row_mask:0xf bank_mask:0xf\n"
"MA v,144,a,152,a,72\n"
"TR 176,251,34304\n"
"v_mul_f32_dpp v117, v245, v117 row_newbcast:1 row_mask:0xf bank_mask:0xf\n"
"v_mul_f32_dpp v118, v246, v118 row_newbcast:1 row_mask:0xf bank_mask:0xf\n"
"MA v,144,a,148,a,68\n"
"s_or_b32 s26, s26, s81\n"
"EX 139\n"
"v_mul_f32_dpp v119, v247, v119 row_newbcast:1 row_mask:0xf bank_mask:0xf\n"
"MA v,144,a,144,a,64\n"
"s_waitcnt lgkmcnt(11)\n"
"TR 178,251,38400\n"
"CV 217,138,139\n"
"CV 116,116,117\n"
"MZ v,64,v,160,a,160\n"
"ds_write_b64 v248, v[120:121] offset:7168\n"
"EX 140\n"
"CV 117,118,119\n"
"MA v,64,v,176,a,164\n"
"TR 180,251,42496\n"
"MZ v,68,v,180,a,164\n"
"s_add_u32 s70, s70, 8192\n"
"TR 60,114,6144\n"
"EX 141\n"
"MA v,68,v,164,a,160\n"
"TR 182,251,46592\n"
"CV 220,140,141\n"
"MZ v,72,v,168,a,160\n"
"s_add_u32 m0, s69, 8192\n"
"ds_write_b64 v248, v[116:117] offset:7680\n"
"EX 142\n"
"MA v,72,v,184,a,164\n"
"buffer_load_dwordx4 v253, s[44:47], s25 offen lds\n"
"MZ v,76,v,188,a,164\n"
"TR 62,114,6656\n"
"EX 143\n"
"MA v,76,v,172,a,160\n"
"s_waitcnt lgkmcnt(9)\n"
"TR 184,252,34304\n"
"CV 221,142,143\n"
"MZ v,92,v,172,a,168\n"
"TR 58,114,7168\n"
"EX 156\n"
"MA v,92,v,188,a,172\n"
"TR 186,252,38400\n"
"MZ v,88,v,184,a,172\n"
"TR 54,114,7680\n"
"EX 157\n"
"MA v,88,v,168,a,168\n"
"CV 222,156,157\n"
"MZ v,84,v,164,a,168\n"
"EX 158\n"
"MA v,84,v,180,a,172\n"
"TR 188,252,42496\n"
"MZ v,80,v,176,a,172\n"
"EX 159\n"
"MA v,80,v,160,a,168\n"
"s_waitcnt lgkmcnt(7)\n"
"TR 190,252,46592\n"
"CV 223,158,159\n"
"MZ v,96,v,160,a,176\n"
"s_add_u32 m0, s69, 9088\n"
"EX 152\n"
"MA v,96,v,176,a,180\n"
"s_memrealtime vcc\n"
"buffer_load_dwordx4 v253, s[44:47], s25 offen offset:128 lds\n"
"MZ v,100,v,180,a,180\n"
"EX 153\n"
"MA v,100,v,164,a,176\n"
"CV 218,152,153\n"
"MZ v,104,v,168,a,176\n"
"s_waitcnt lgkmcnt(0)\n"
"EX 154\n"
"MA v,104,v,184,a,180\n"
"s_waitcnt vmcnt(18)\n"
"MZ v,108,v,188,a,180\n"
"s_barrier\n"
"EX 155\n"
"MA v,108,v,172,a,176\n"
"RD 128,249,32768\n"
"CV 219,154,155\n"
"MZ v,124,v,172,a,184\n"
"s_sub_u32 s33, s72, vcc_lo\n"
"EX 148\n"
"MA v,124,v,188,a,188\n"
"s_add_u32 s72, vcc_lo, s77\n"
"RD 132,250,32768\n"
"MZ v,120,v,184,a,188\n"
"s_max_i32 s33, s33, -150\n"
"EX 149\n"
"MA v,120,v,168,a,184\n"
"s_add_u32 s72, s72, s33\n"
"RD 136,249,33792\n"
"CV 214,148,149\n"
"MZ v,116,v,164,a,184\n"
"s_cmp_gt_i32 s33, 0\n"
"EX 150\n"
"MA v,116,v,180,a,188\n"
"s_cbranch_scc1 L_PACE6_%=\n"
"L_PACEB6_%=:\n"
"RD 140,250,33792\n"
"MZ v,112,v,176,a,188\n"
"EX 151\n"
"MA v,112,v,160,a,184\n"
"CV 215,150,151\n"
"MA v,240,v,192,a,236\n"
"s_mov_b32 s80, 1024\n"
"MA v,240,v,196,a,240\n"
"buffer_store_dwordx4 a[4:7], v255, s[56:59], s80 offen sc1\n"
"MA v,240,v,200,a,244\n"
"MA v,240,v,204,a,248\n"
"s_waitcnt lgkmcnt(0)\n"
"MC 128,128,64,224\n"
"EX 144\n"
"MA v,128,a,132,a,68\n"
"RD 144,249,36864\n"
"MA v,128,a,136,a,72\n"
"EX 145\n"
"MA v,128,a,140,a,76\n"
"L_ST10_%=:\n"
"s_cbranch_execz L_VB2_%=\n"
"RD 148,250,36864\n"
"CV 210,144,145\n"
"MC 132,140,92,228\n"
"EX 146\n"
"MA v,132,a,136,a,88\n"
"RD 152,249,37888\n"
"MA v,132,a,132,a,84\n"
"EX 147\n"
"MA v,132,a,128,a,80\n"
"RD 156,250,37888\n"
"CV 211,146,147\n"
"MC 136,128,96,232\n"
"s_add_u32 m0, s69, 98304\n"
"EX 128\n"
"MA v,136,a,132,a,100\n"
"buffer_load_dwordx4 v254, s[48:51], s24 offen nt lds\n"
"MA v,136,a,136,a,104\n"
"EX 129\n"
"MA v,136,a,140,a,108\n"
"TR 160,251,49152\n"
"CV 160,128,129\n"
"MC 140,140,124,236\n"
"EX 130\n"
"MA v,140,a,136,a,120\n"
"TR 162,251,53248\n"
"MA v,140,a,132,a,116\n"
"EX 131\n"
"MA v,140,a,128,a,112\n"
"s_waitcnt lgkmcnt(2)\n"
"TR 164,251,57344\n"
"CV 161,130,131\n"
"MC 156,144,112,236\n"
"EX 132\n"
"MA v,156,a,148,a,116\n"
"TR 166,251,61440\n"
"MA v,156,a,152,a,120\n"
"EX 133\n"
"MA v,156,a,156,a,124\n"
"TR 168,252,49152\n"
"CV 164,132,133\n"
"MC 152,156,108,232\n"
"s_add_u32 m0, s69, 99200\n"
"EX 134\n"
"MA v,152,a,152,a,104\n"
"buffer_load_dwordx4 v254, s[48:51], s24 offen offset:128 nt lds\n"
"MA v,152,a,148,a,100\n"
"s_add_u32 s27, s70, s71\n"
"EX 135\n"
"MA v,152,a,144,a,96\n"
"TR 170,252,53248\n"
"CV 165,134,135\n"
"MC 148,144,80,228\n"
"s_and_b32 s27, s27, 0x3ffff\n"
"EX 136\n"
"MA v,148,a,148,a,84\n"
"TR 172,252,57344\n"
"MA v,148,a,152,a,88\n"
"s_and_b32 s81, s70, 0xfffc0000\n"
"EX 137\n"
"MA v,148,a,156,a,92\n"
"TR 174,252,61440\n"
"CV 168,136,137\n"
"MC 144,156,76,224\n"
"EX 138\n"
"MA v,144,a,152,a,72\n"
"TR 176,251,49664\n"
"MA v,144,a,148,a,68\n"
"s_or_b32 s27, s27, s81\n"
"EX 139\n"
"MA v,144,a,144,a,64\n"
"s_waitcnt lgkmcnt(5)\n"
"TR 178,251,53760\n"
"CV 169,138,139\n"
"MA v,0,v,192,a,160\n"
"EX 140\n"
"MA v,0,v,208,a,164\n"
"TR 180,251,57856\n"
"MA v,4,v,212,a,164\n"
"s_add_u32 s70, s70, 8192\n"
"EX 141\n"
"MA v,4,v,196,a,160\n"
"TR 182,251,61952\n"
"CV 172,140,141\n"
"MA v,8,v,200,a,160\n"
"s_add_u32 m0, s69, 16384\n"
"EX 142\n"
"MA v,8,v,216,a,164\n"
"buffer_load_dwordx4 v253, s[44:47], s26 offen lds\n"
"MA v,12,v,220,a,164\n"
"EX 143\n"
"MA v,12,v,204,a,160\n"
"s_waitcnt lgkmcnt(4)\n"
"TR 184,252,49664\n"
"CV 173,142,143\n"
"MA v,28,v,204,a,168\n"
"EX 156\n"
"MA v,28,v,220,a,172\n"
"TR 186,252,53760\n"
"MA v,24,v,216,a,172\n"
"EX 157\n"
"MA v,24,v,200,a,168\n"
"CV 174,156,157\n"
"MA v,20,v,196,a,168\n"
"EX 158\n"
"MA v,20,v,212,a,172\n"
"TR 188,252,57856\n"
"MA v,16,v,208,a,172\n"
"EX 159\n"
"MA v,16,v,192,a,168\n"
"s_waitcnt lgkmcnt(3)\n"
"TR 190,252,61952\n"
"CV 175,158,159\n"
"MA v,32,v,192,a,176\n"
"s_add_u32 m0, s69, 17280\n"
"EX 152\n"
"MA v,32,v,208,a,180\n"
"buffer_load_dwordx4 v253, s[44:47], s26 offen offset:128 lds\n"
"MA v,36,v,212,a,180\n"
"EX 153\n"
"MA v,36,v,196,a,176\n"
"CV 170,152,153\n"
"MA v,40,v,200,a,176\n"
"EX 154\n"
"MA v,40,v,216,a,180\n"
"MA v,44,v,220,a,180\n"
"EX 155\n"
"MA v,44,v,204,a,176\n"
"s_waitcnt lgkmcnt(0)\n"
"RD 128,249,40960\n"
"CV 171,154,155\n"
"MA v,60,v,204,a,184\n"
"EX 148\n"
"MA v,60,v,220,a,188\n"
"RD 132,250,40960\n"
"MA v,56,v,216,a,188\n"
"EX 149\n"
"MA v,56,v,200,a,184\n"
"RD 136,249,41984\n"
"CV 166,148,149\n"
"MA v,52,v,196,a,184\n"
"EX 150\n"
"MA v,52,v,212,a,188\n"
"RD 140,250,41984\n"
"MA v,48,v,208,a,188\n"
"EX 151\n"
"MA v,48,v,192,a,184\n"
"CV 167,150,151\n"
"MA v,240,v,208,a,236\n"
"s_mov_b32 s80, 2048\n"
"MA v,240,v,212,a,240\n"
"buffer_store_dwordx4 a[8:11], v255, s[56:59], s80 offen sc1\n"
"MA v,240,v,216,a,244\n"
"MA v,240,v,220,a,248\n"
"s_waitcnt lgkmcnt(0)\n"
"s_mov_b64 exec, 0\n"
"s_cmp_eq_u32 s64, 0\n"
"s_cbranch_scc1 L_ST13_%=\n"
"s_cmp_eq_u32 s64, 1\n"
"s_cbranch_scc1 L_ST14_%=\n"
"s_cmp_eq_u32 s64, 2\n"
"s_cbranch_scc1 L_ST15_%=\n"
"s_cmp_eq_u32 s64, 3\n"
"s_cbranch_scc1 L_ST16_%=\n"
"L_VB3_%=:\n"
"s_mov_b64 exec, -1\n"
"MC 128,128,64,224\n"
"s_lshr_b32 s79, s20, 16\n"
"EX 144\n"
"MA v,128,a,132,a,68\n"
"RD 144,249,45056\n"
"MA v,128,a,136,a,72\n"
"s_lshl_b32 s79, s79, 11\n"
"EX 145\n"
"MA v,128,a,140,a,76\n"
"RD 148,250,45056\n"
"CV 162,144,145\n"
"MC 132,140,92,228\n"
"s_sub_u32 s79, s79, 15728640\n"
"EX 146\n"
"MA v,132,a,136,a,88\n"
"RD 152,249,46080\n"
"MA v,132,a,132,a,84\n"
"EX 147\n"
"MA v,132,a,128,a,80\n"
"RD 156,250,46080\n"
"CV 163,146,147\n"
"MC 136,128,96,232\n"
"s_add_u32 m0, s69, 106496\n"
"EX 128\n"
"MA v,136,a,132,a,100\n"
"buffer_load_dwordx4 v254, s[48:51], s25 offen nt lds\n"
"MA v,136,a,136,a,104\n"
"EX 129\n"
"MA v,136,a,140,a,108\n"
"TR 160,251,50176\n"
"CV 176,128,129\n"
"MC 140,140,124,236\n"
"EX 130\n"
"MA v,140,a,136,a,120\n"
"TR 162,251,54272\n"
"MA v,140,a,132,a,116\n"
"EX 131\n"
"MA v,140,a,128,a,112\n"
"s_waitcnt lgkmcnt(2)\n"
"TR 164,251,58368\n"
"CV 177,130,131\n"
"MC 156,144,112,236\n"
"EX 132\n"
"MA v,156,a,148,a,116\n"
"TR 166,251,62464\n"
"MA v,156,a,152,a,120\n"
"EX 133\n"
"MA v,156,a,156,a,124\n"
"TR 168,252,50176\n"
"CV 180,132,133\n"
"MC 152,156,108,232\n"
"s_add_u32 m0, s69, 107392\n"
"EX 134\n"
"MA v,152,a,152,a,104\n"
"buffer_load_dwordx4 v254, s[48:51], s25 offen offset:128 nt lds\n"
"MA v,152,a,148,a,100\n"
"s_add_u32 s24, s70, s71\n"
"EX 135\n"
"MA v,152,a,144,a,96\n"
"TR 170,252,54272\n"
"CV 181,134,135\n"
"MC 148,144,80,228\n"
"s_and_b32 s24, s24, 0x3ffff\n"
"EX 136\n"
"MA v,148,a,148,a,84\n"
"TR 172,252,58368\n"
"MA v,148,a,152,a,88\n"
"s_and_b32 s81, s70, 0xfffc0000\n"
"EX 137\n"
"MA v,148,a,156,a,92\n"
"TR 174,252,62464\n"
"CV 184,136,137\n"
"MC 144,156,76,224\n"
"EX 138\n"
"MA v,144,a,152,a,72\n"
"TR 176,251,50688\n"
"MA v,144,a,148,a,68\n"
"s_or_b32 s24, s24, s81\n"
"EX 139\n"
"MA v,144,a,144,a,64\n"
"s_waitcnt lgkmcnt(5)\n"
"TR 178,251,54784\n"
"CV 185,138,139\n"
"MA v,64,v,192,a,160\n"
"EX 140\n"
"MA v,64,v,208,a,164\n"
"TR 180,251,58880\n"
"MA v,68,v,212,a,164\n"
"s_add_u32 s70, s70, 8192\n"
"EX 141\n"
"MA v,68,v,196,a,160\n"
"TR 182,251,62976\n"
"CV 188,140,141\n"
"MA v,72,v,200,a,160\n"
"s_add_u32 m0, s69, 24576\n"
"EX 142\n"
"MA v,72,v,216,a,164\n"
"buffer_load_dwordx4 v253, s[44:47], s27 offen lds\n"
"MA v,76,v,220,a,164\n"
"EX 143\n"
"MA v,76,v,204,a,160\n"
"s_waitcnt lgkmcnt(4)\n"
"TR 184,252,50688\n"
"CV 189,142,143\n"
"MA v,92,v,204,a,168\n"
"EX 156\n"
"MA v,92,v,220,a,172\n"
"TR 186,252,54784\n"
"MA v,88,v,216,a,172\n"
"EX 157\n"
"MA v,88,v,200,a,168\n"
"CV 190,156,157\n"
"MA v,84,v,196,a,168\n"
"EX 158\n"
"MA v,84,v,212,a,172\n"
"TR 188,252,58880\n"
"MA v,80,v,208,a,172\n"
"EX 159\n"
"MA v,80,v,192,a,168\n"
"s_waitcnt lgkmcnt(3)\n"
"TR 190,252,62976\n"
"CV 191,158,159\n"
"MA v,96,v,192,a,176\n"
"s_add_u32 m0, s69, 25472\n"
"EX 152\n"
"MA v,96,v,208,a,180\n"
"s_memrealtime vcc\n"
"buffer_load_dwordx4 v253, s[44:47], s27 offen offset:128 lds\n"
"MA v,100,v,212,a,180\n"
"EX 153\n"
"MA v,100,v,196,a,176\n"
"CV 186,152,153\n"
"MA v,104,v,200,a,176\n"
"s_waitcnt lgkmcnt(0)\n"
"EX 154\n"
"MA v,104,v,216,a,180\n"
"s_waitcnt vmcnt(20)\n"
"MA v,108,v,220,a,180\n"
"s_barrier\n"
"EX 155\n"
"MA v,108,v,204,a,176\n"
"RD 128,249,49152\n"
"CV 187,154,155\n"
"MA v,124,v,204,a,184\n"
"s_sub_u32 s33, s72, vcc_lo\n"
"EX 148\n"
"MA v,124,v,220,a,188\n"
"s_add_u32 s72, vcc_lo, s77\n"
"RD 132,250,49152\n"
"MA v,120,v,216,a,188\n"
"s_max_i32 s33, s33, -150\n"
"EX 149\n"
"MA v,120,v,200,a,184\n"
"s_add_u32 s72, s72, s33\n"
"RD 136,249,50176\n"
"CV 182,148,149\n"
"MA v,116,v,196,a,184\n"
"s_cmp_gt_i32 s33, 0\n"
"EX 150\n"
"MA v,116,v,212,a,188\n"
"s_cbranch_scc1 L_PACE7_%=\n"
"L_PACEB7_%=:\n"
"RD 140,250,50176\n"
"MA v,112,v,208,a,188\n"
"EX 151\n"
"MA v,112,v,192,a,184\n"
"CV 183,150,151\n"
"MA v,240,v,160,a,236\n"
"s_mov_b32 s80, 3072\n"
"MA v,240,v,164,a,240\n"
"buffer_store_dwordx4 a[12:15], v255, s[56:59], s80 offen sc1\n"
"MA v,240,v,168,a,244\n"
"MA v,240,v,172,a,248\n"
"s_waitcnt lgkmcnt(0)\n"
"s_mov_b64 s[40:41], s[18:19]\n"
"s_add_u32 s40, s40, s79\n"
"s_addc_u32 s41, s41, 0\n"
"s_and_b32 s41, s41, 0xffff\n"
"s_mov_b32 s42, 0\n"
"s_mov_b32 s43, 0x20000\n"
"MC 128,128,64,224\n"
"s_and_b32 s79, s20, 0xffff\n"
"EX 144\n"
"MA v,128,a,132,a,68\n"
"RD 144,249,53248\n"
"MA v,128,a,136,a,72\n"
"s_mul_i32 s79, s79, 1152\n"
"EX 145\n"
"MA v,128,a,140,a,76\n"
"RD 148,250,53248\n"
"CV 178,144,145\n"
"MC 132,140,92,228\n"
"s_add_u32 s79, s79, 34603008\n"
"EX 146\n"
"MA v,132,a,136,a,88\n"
"RD 152,249,54272\n"
"MA v,132,a,132,a,84\n"
"EX 147\n"
"MA v,132,a,128,a,80\n"
"RD 156,250,54272\n"
"CV 179,146,147\n"
"MC 136,128,96,232\n"
"s_add_u32 m0, s69, 114688\n"
"EX 128\n"
"MA v,136,a,132,a,100\n"
"buffer_load_dwordx4 v254, s[48:51], s26 offen nt lds\n"
"MA v,136,a,136,a,104\n"
"EX 129\n"
"MA v,136,a,140,a,108\n"
"TR 160,251,0\n"
"CV 192,128,129\n"
"MC 140,140,124,236\n"
"EX 130\n"
"MA v,140,a,136,a,120\n"
"TR 162,251,4096\n"
"MA v,140,a,132,a,116\n"
"EX 131\n"
"MA v,140,a,128,a,112\n"
"s_waitcnt lgkmcnt(2)\n"
"TR 164,251,8192\n"
"CV 193,130,131\n"
"MC 156,144,112,236\n"
"EX 132\n"
"MA v,156,a,148,a,116\n"
"TR 166,251,12288\n"
"MA v,156,a,152,a,120\n"
"EX 133\n"
"MA v,156,a,156,a,124\n"
"TR 168,252,0\n"
"CV 196,132,133\n"
"MC 152,156,108,232\n"
"s_add_u32 m0, s69, 115584\n"
"EX 134\n"
"MA v,152,a,152,a,104\n"
"buffer_load_dwordx4 v254, s[48:51], s26 offen offset:128 nt lds\n"
"MA v,152,a,148,a,100\n"
"s_add_u32 s25, s70, s71\n"
"EX 135\n"
"MA v,152,a,144,a,96\n"
"TR 170,252,4096\n"
"CV 197,134,135\n"
"MC 148,144,80,228\n"
"s_and_b32 s25, s25, 0x3ffff\n"
"EX 136\n"
"MA v,148,a,148,a,84\n"
"TR 172,252,8192\n"
"MA v,148,a,152,a,88\n"
"s_and_b32 s81, s70, 0xfffc0000\n"
"EX 137\n"
"MA v,148,a,156,a,92\n"
"TR 174,252,12288\n"
"CV 200,136,137\n"
"MC 144,156,76,224\n"
"EX 138\n"
"MA v,144,a,152,a,72\n"
"TR 176,251,512\n"
"MA v,144,a,148,a,68\n"
"s_or_b32 s25, s25, s81\n"
"EX 139\n"
"MA v,144,a,144,a,64\n"
"s_waitcnt lgkmcnt(5)\n"
"TR 178,251,4608\n"
"CV 201,138,139\n"
"MA v,0,v,160,a,160\n"
"EX 140\n"
"MA v,0,v,176,a,164\n"
"TR 180,251,8704\n"
"MA v,4,v,180,a,164\n"
"s_add_u32 s70, s70, 8192\n"
"EX 141\n"
"MA v,4,v,164,a,160\n"
"TR 182,251,12800\n"
"CV 204,140,141\n"
"MA v,8,v,168,a,160\n"
"s_add_u32 m0, s69, 32768\n"
"EX 142\n"
"MA v,8,v,184,a,164\n"
"buffer_load_dwordx4 v253, s[44:47], s24 offen lds\n"
"MA v,12,v,188,a,164\n"
"EX 143\n"
"MA v,12,v,172,a,160\n"
"s_waitcnt lgkmcnt(4)\n"
"TR 184,252,512\n"
"CV 205,142,143\n"
"MA v,28,v,172,a,168\n"
"EX 156\n"
"MA v,28,v,188,a,172\n"
"TR 186,252,4608\n"
"MA v,24,v,184,a,172\n"
"EX 157\n"
"MA v,24,v,168,a,168\n"
"CV 206,156,157\n"
"MA v,20,v,164,a,168\n"
"EX 158\n"
"MA v,20,v,180,a,172\n"
"TR 188,252,8704\n"
"MA v,16,v,176,a,172\n"
"EX 159\n"
"MA v,16,v,160,a,168\n"
"s_waitcnt lgkmcnt(3)\n"
"TR 190,252,12800\n"
"CV 207,158,159\n"
"MA v,32,v,160,a,176\n"
"s_add_u32 m0, s69, 33664\n"
"EX 152\n"
"MA v,32,v,176,a,180\n"
"buffer_load_dwordx4 v253, s[44:47], s24 offen offset:128 lds\n"
"MA v,36,v,180,a,180\n"
"EX 153\n"
"MA v,36,v,164,a,176\n"
"CV 202,152,153\n"
"MA v,40,v,168,a,176\n"
"EX 154\n"
"MA v,40,v,184,a,180\n"
"MA v,44,v,188,a,180\n"
"EX 155\n"
"MA v,44,v,172,a,176\n"
"s_waitcnt lgkmcnt(0)\n"
"RD 128,249,57344\n"
"CV 203,154,155\n"
"MA v,60,v,172,a,184\n"
"EX 148\n"
"MA v,60,v,188,a,188\n"
"RD 132,250,57344\n"
"MA v,56,v,184,a,188\n"
"EX 149\n"
"MA v,56,v,168,a,184\n"
"RD 136,249,58368\n"
"CV 198,148,149\n"
"MA v,52,v,164,a,184\n"
"EX 150\n"
"MA v,52,v,180,a,188\n"
"RD 140,250,58368\n"
"MA v,48,v,176,a,188\n"
"EX 151\n"
"MA v,48,v,160,a,184\n"
"CV 199,150,151\n"
"MA v,240,v,176,a,236\n"
"s_mov_b32 s80, 4096\n"
"MA v,240,v,180,a,240\n"
"buffer_store_dwordx4 a[16:19], v255, s[56:59], s80 offen sc1\n"
"MA v,240,v,184,a,244\n"
"MA v,240,v,188,a,248\n"
"s_waitcnt lgkmcnt(0)\n"
"s_mov_b64 s[36:37], s[18:19]\n"
"s_add_u32 s36, s36, s79\n"
"s_addc_u32 s37, s37, 0\n"
"s_and_b32 s37, s37, 0xffff\n"
"s_mov_b32 s38, 0\n"
"s_mov_b32 s39, 0x20000\n"
"MC 128,128,64,224\n"
"s_add_u32 s65, s64, 0\n"
"EX 144\n"
"MA v,128,a,132,a,68\n"
"RD 144,249,61440\n"
"MA v,128,a,136,a,72\n"
"s_and_b32 s65, s65, 3\n"
"EX 145\n"
"MA v,128,a,140,a,76\n"
"RD 148,250,61440\n"
"CV 194,144,145\n"
"MC 132,140,92,228\n"
"s_lshl_b32 s65, s65, 10\n"
"EX 146\n"
"MA v,132,a,136,a,88\n"
"RD 152,249,62464\n"
"MA v,132,a,132,a,84\n"
"s_add_u32 s66, s64, 1\n"
"EX 147\n"
"MA v,132,a,128,a,80\n"
"RD 156,250,62464\n"
"CV 195,146,147\n"
"MC 136,128,96,232\n"
"s_add_u32 m0, s69, 122880\n"
"EX 128\n"
"MA v,136,a,132,a,100\n"
"buffer_load_dwordx4 v254, s[48:51], s27 offen nt lds\n"
"MA v,136,a,136,a,104\n"
"s_and_b32 s66, s66, 3\n"
"EX 129\n"
"MA v,136,a,140,a,108\n"
"TR 160,251,1024\n"
"CV 208,128,129\n"
"MC 140,140,124,236\n"
"s_lshl_b32 s66, s66, 10\n"
"EX 130\n"
"MA v,140,a,136,a,120\n"
"TR 162,251,5120\n"
"MA v,140,a,132,a,116\n"
"s_add_u32 s67, s64, 2\n"
"EX 131\n"
"MA v,140,a,128,a,112\n"
"s_waitcnt lgkmcnt(2)\n"
"TR 164,251,9216\n"
"CV 209,130,131\n"
"MC 156,144,112,236\n"
"s_and_b32 s67, s67, 3\n"
"EX 132\n"
"MA v,156,a,148,a,116\n"
"TR 166,251,13312\n"
"MA v,156,a,152,a,120\n"
"s_lshl_b32 s67, s67, 10\n"
"EX 133\n"
"MA v,156,a,156,a,124\n"
"TR 168,252,1024\n"
"CV 212,132,133\n"
"MC 152,156,108,232\n"
"s_add_u32 m0, s69, 123776\n"
"EX 134\n"
"MA v,152,a,152,a,104\n"
"buffer_load_dwordx4 v254, s[48:51], s27 offen offset:128 nt lds\n"
"MA v,152,a,148,a,100\n"
"s_add_u32 s26, s70, s71\n"
"EX 135\n"
"MA v,152,a,144,a,96\n"
"TR 170,252,5120\n"
"CV 213,134,135\n"
"MC 148,144,80,228\n"
"s_and_b32 s26, s26, 0x3ffff\n"
"EX 136\n"
"MA v,148,a,148,a,84\n"
"TR 172,252,9216\n"
"MA v,148,a,152,a,88\n"
"s_and_b32 s81, s70, 0xfffc0000\n"
"EX 137\n"
"MA v,148,a,156,a,92\n"
"TR 174,252,13312\n"
"CV 216,136,137\n"
"MC 144,156,76,224\n"
"s_add_u32 s78, s64, 3\n"
"EX 138\n"
"MA v,144,a,152,a,72\n"
"TR 176,251,1536\n"
"MA v,144,a,148,a,68\n"
"s_or_b32 s26, s26, s81\n"
"EX 139\n"
"MA v,144,a,144,a,64\n"
"s_waitcnt lgkmcnt(5)\n"
"TR 178,251,5632\n"
"CV 217,138,139\n"
"MA v,64,v,160,a,160\n"
"L_ST11_%=:\n"
"s_cbranch_execz L_VB2_%=\n"
"s_and_b32 s78, s78, 3\n"
"EX 140\n"
"MA v,64,v,176,a,164\n"
"TR 180,251,9728\n"
"MA v,68,v,180,a,164\n"
"s_add_u32 s70, s70, 8192\n"
"EX 141\n"
"MA v,68,v,164,a,160\n"
"TR 182,251,13824\n"
"CV 220,140,141\n"
"MA v,72,v,168,a,160\n"
"s_add_u32 m0, s69, 40960\n"
"EX 142\n"
"MA v,72,v,184,a,164\n"
"buffer_load_dwordx4 v253, s[44:47], s25 offen lds\n"
"MA v,76,v,188,a,164\n"
"s_lshl_b32 s78, s78, 10\n"
"EX 143\n"
"MA v,76,v,172,a,160\n"
"s_waitcnt lgkmcnt(4)\n"
"TR 184,252,1536\n"
"CV 221,142,143\n"
"MA v,92,v,172,a,168\n"
"EX 156\n"
"MA v,92,v,188,a,172\n"
"TR 186,252,5632\n"
"MA v,88,v,184,a,172\n"
"EX 157\n"
"MA v,88,v,168,a,168\n"
"CV 222,156,157\n"
"MA v,84,v,164,a,168\n"
"EX 158\n"
"MA v,84,v,180,a,172\n"
"TR 188,252,9728\n"
"MA v,80,v,176,a,172\n"
"EX 159\n"
"MA v,80,v,160,a,168\n"
"s_waitcnt lgkmcnt(3)\n"
"TR 190,252,13824\n"
"CV 223,158,159\n"
"MA v,96,v,160,a,176\n"
"s_add_u32 m0, s69, 41856\n"
"EX 152\n"
"MA v,96,v,176,a,180\n"
"s_memrealtime vcc\n"
"buffer_load_dwordx4 v253, s[44:47], s25 offen offset:128 lds\n"
"MA v,100,v,180,a,180\n"
"EX 153\n"
"MA v,100,v,164,a,176\n"
"CV 218,152,153\n"
"MA v,104,v,168,a,176\n"
"s_waitcnt lgkmcnt(0)\n"
"EX 154\n"
"MA v,104,v,184,a,180\n"
"s_waitcnt vmcnt(20)\n"
"MA v,108,v,188,a,180\n"
"s_barrier\n"
"EX 155\n"
"MA v,108,v,172,a,176\n"
"RD 128,249,0\n"
"CV 219,154,155\n"
"MA v,124,v,172,a,184\n"
"s_sub_u32 s33, s72, vcc_lo\n"
"EX 148\n"
"MA v,124,v,188,a,188\n"
"s_add_u32 s72, vcc_lo, s77\n"
"RD 132,250,0\n"
"MA v,120,v,184,a,188\n"
"s_max_i32 s33, s33, -150\n"
"EX 149\n"
"MA v,120,v,168,a,184\n"
"s_add_u32 s72, s72, s33\n"
"RD 136,249,1024\n"
"CV 214,148,149\n"
"MA v,116,v,164,a,184\n"
"s_cmp_gt_i32 s33, 0\n"
"EX 150\n"
"MA v,116,v,180,a,188\n"
"s_cbranch_scc1 L_PACE8_%=\n"
"L_PACEB8_%=:\n"
"RD 140,250,1024\n"
"MA v,112,v,176,a,188\n"
"EX 151\n"
"MA v,112,v,160,a,184\n"
"CV 215,150,151\n"
"MA v,240,v,192,a,236\n"
"s_mov_b32 s80, 5120\n"
"MA v,240,v,196,a,240\n"
"buffer_store_dwordx4 a[20:23], v255, s[56:59], s80 offen sc1\n"
"MA v,240,v,200,a,244\n"
"MA v,240,v,204,a,248\n"
"s_waitcnt lgkmcnt(0)\n"
"MC 128,128,64,224\n"
"EX 144\n"
"MA v,128,a,132,a,68\n"
"RD 144,249,4096\n"
"MA v,128,a,136,a,72\n"
"EX 145\n"
"MA v,128,a,140,a,76\n"
"RD 148,250,4096\n"
"CV 210,144,145\n"
"MC 132,140,92,228\n"
"EX 146\n"
"MA v,132,a,136,a,88\n"
"RD 152,249,5120\n"
"MA v,132,a,132,a,84\n"
"EX 147\n"
"MA v,132,a,128,a,80\n"
"RD 156,250,5120\n"
"CV 211,146,147\n"
"MC 136,128,96,232\n"
"s_add_u32 m0, s69, 65536\n"
"EX 128\n"
"MA v,136,a,132,a,100\n"
"buffer_load_dwordx4 v254, s[48:51], s24 offen nt lds\n"
"MA v,136,a,136,a,104\n"
"EX 129\n"
"MA v,136,a,140,a,108\n"
"TR 160,251,16384\n"
"CV 160,128,129\n"
"MC 140,140,124,236\n"
"EX 130\n"
"MA v,140,a,136,a,120\n"
"TR 162,251,20480\n"
"MA v,140,a,132,a,116\n"
"EX 131\n"
"MA v,140,a,128,a,112\n"
"s_waitcnt lgkmcnt(2)\n"
"TR 164,251,24576\n"
"CV 161,130,131\n"
"MC 156,144,112,236\n"
"EX 132\n"
"MA v,156,a,148,a,116\n"
"TR 166,251,28672\n"
"MA v,156,a,152,a,120\n"
"EX 133\n"
"MA v,156,a,156,a,124\n"
"TR 168,252,16384\n"
"CV 164,132,133\n"
"MC 152,156,108,232\n"
"s_add_u32 m0, s69, 66432\n"
"EX 134\n"
"MA v,152,a,152,a,104\n"
"buffer_load_dwordx4 v254, s[48:51], s24 offen offset:128 nt lds\n"
"MA v,152,a,148,a,100\n"
"s_add_u32 s27, s70, s71\n"
"EX 135\n"
"MA v,152,a,144,a,96\n"
"TR 170,252,20480\n"
"CV 165,134,135\n"
"MC 148,144,80,228\n"
"s_and_b32 s27, s27, 0x3ffff\n"
"EX 136\n"
"MA v,148,a,148,a,84\n"
"TR 172,252,24576\n"
"MA v,148,a,152,a,88\n"
"s_and_b32 s81, s70, 0xfffc0000\n"
"EX 137\n"
"MA v,148,a,156,a,92\n"
"TR 174,252,28672\n"
"CV 168,136,137\n"
"MC 144,156,76,224\n"
"EX 138\n"
"MA v,144,a,152,a,72\n"
"TR 176,251,16896\n"
"MA v,144,a,148,a,68\n"
"s_or_b32 s27, s27, s81\n"
"EX 139\n"
"MA v,144,a,144,a,64\n"
"s_waitcnt lgkmcnt(5)\n"
"TR 178,251,20992\n"
"CV 169,138,139\n"
"MA v,0,v,192,a,160\n"
"EX 140\n"
"MA v,0,v,208,a,164\n"
"TR 180,251,25088\n"
"MA v,4,v,212,a,164\n"
"s_add_u32 s70, s70, 8192\n"
"EX 141\n"
"MA v,4,v,196,a,160\n"
"TR 182,251,29184\n"
"CV 172,140,141\n"
"MA v,8,v,200,a,160\n"
"s_add_u32 m0, s69, 49152\n"
"EX 142\n"
"MA v,8,v,216,a,164\n"
"buffer_load_dwordx4 v253, s[44:47], s26 offen lds\n"
"MA v,12,v,220,a,164\n"
"EX 143\n"
"MA v,12,v,204,a,160\n"
"s_waitcnt lgkmcnt(4)\n"
"TR 184,252,16896\n"
"CV 173,142,143\n"
"MA v,28,v,204,a,168\n"
"EX 156\n"
"MA v,28,v,220,a,172\n"
"TR 186,252,20992\n"
"MA v,24,v,216,a,172\n"
"EX 157\n"
"MA v,24,v,200,a,168\n"
"CV 174,156,157\n"
"MA v,20,v,196,a,168\n"
"EX 158\n"
"MA v,20,v,212,a,172\n"
"TR 188,252,25088\n"
"MA v,16,v,208,a,172\n"
"EX 159\n"
"MA v,16,v,192,a,168\n"
"s_waitcnt lgkmcnt(3)\n"
"TR 190,252,29184\n"
"CV 175,158,159\n"
"MA v,32,v,192,a,176\n"
"s_add_u32 m0, s69, 50048\n"
"EX 152\n"
"MA v,32,v,208,a,180\n"
"buffer_load_dwordx4 v253, s[44:47], s26 offen offset:128 lds\n"
"MA v,36,v,212,a,180\n"
"EX 153\n"
"MA v,36,v,196,a,176\n"
"CV 170,152,153\n"
"MA v,40,v,200,a,176\n"
"EX 154\n"
"MA v,40,v,216,a,180\n"
"MA v,44,v,220,a,180\n"
"EX 155\n"
"MA v,44,v,204,a,176\n"
"s_waitcnt lgkmcnt(0)\n"
"RD 128,249,8192\n"
"CV 171,154,155\n"
"MA v,60,v,204,a,184\n"
"EX 148\n"
"MA v,60,v,220,a,188\n"
"RD 132,250,8192\n"
"MA v,56,v,216,a,188\n"
"EX 149\n"
"MA v,56,v,200,a,184\n"
"RD 136,249,9216\n"
"CV 166,148,149\n"
"MA v,52,v,196,a,184\n"
"EX 150\n"
"MA v,52,v,212,a,188\n"
"RD 140,250,9216\n"
"MA v,48,v,208,a,188\n"
"EX 151\n"
"MA v,48,v,192,a,184\n"
"CV 167,150,151\n"
"MA v,240,v,208,a,236\n"
"s_mov_b32 s80, 6144\n"
"MA v,240,v,212,a,240\n"
"buffer_store_dwordx4 a[24:27], v255, s[56:59], s80 offen sc1\n"
"MA v,240,v,216,a,244\n"
"MA v,240,v,220,a,248\n"
"s_waitcnt lgkmcnt(0)\n"
"MC 128,128,64,224\n"
"EX 144\n"
"MA v,128,a,132,a,68\n"
"RD 144,249,12288\n"
"MA v,128,a,136,a,72\n"
"EX 145\n"
"MA v,128,a,140,a,76\n"
"RD 148,250,12288\n"
"CV 162,144,145\n"
"MC 132,140,92,228\n"
"EX 146\n"
"MA v,132,a,136,a,88\n"
"RD 152,249,13312\n"
"MA v,132,a,132,a,84\n"
"EX 147\n"
"MA v,132,a,128,a,80\n"
"RD 156,250,13312\n"
"CV 163,146,147\n"
"MC 136,128,96,232\n"
"s_add_u32 m0, s69, 73728\n"
"EX 128\n"
"MA v,136,a,132,a,100\n"
"buffer_load_dwordx4 v254, s[48:51], s25 offen nt lds\n"
"MA v,136,a,136,a,104\n"
"EX 129\n"
"MA v,136,a,140,a,108\n"
"TR 160,251,17408\n"
"CV 176,128,129\n"
"MC 140,140,124,236\n"
"EX 130\n"
"MA v,140,a,136,a,120\n"
"TR 162,251,21504\n"
"MA v,140,a,132,a,116\n"
"EX 131\n"
"MA v,140,a,128,a,112\n"
"s_waitcnt lgkmcnt(2)\n"
"TR 164,251,25600\n"
"CV 177,130,131\n"
"MC 156,144,112,236\n"
"EX 132\n"
"MA v,156,a,148,a,116\n"
"TR 166,251,29696\n"
"MA v,156,a,152,a,120\n"
"EX 133\n"
"MA v,156,a,156,a,124\n"
"TR 168,252,17408\n"
"CV 180,132,133\n"
"MC 152,156,108,232\n"
"s_add_u32 m0, s69, 74624\n"
"EX 134\n"
"MA v,152,a,152,a,104\n"
"buffer_load_dwordx4 v254, s[48:51], s25 offen offset:128 nt lds\n"
"MA v,152,a,148,a,100\n"
"s_add_u32 s24, s70, s71\n"
"EX 135\n"
"MA v,152,a,144,a,96\n"
"TR 170,252,21504\n"
"CV 181,134,135\n"
"MC 148,144,80,228\n"
"s_and_b32 s24, s24, 0x3ffff\n"
"EX 136\n"
"MA v,148,a,148,a,84\n"
"TR 172,252,25600\n"
"MA v,148,a,152,a,88\n"
"s_and_b32 s81, s70, 0xfffc0000\n"
"EX 137\n"
"MA v,148,a,156,a,92\n"
"TR 174,252,29696\n"
"CV 184,136,137\n"
"MC 144,156,76,224\n"
"EX 138\n"
"MA v,144,a,152,a,72\n"
"TR 176,251,17920\n"
"MA v,144,a,148,a,68\n"
"s_or_b32 s24, s24, s81\n"
"EX 139\n"
"MA v,144,a,144,a,64\n"
"s_waitcnt lgkmcnt(5)\n"
"TR 178,251,22016\n"
"CV 185,138,139\n"
"MA v,64,v,192,a,160\n"
"EX 140\n"
"MA v,64,v,208,a,164\n"
"TR 180,251,26112\n"
"MA v,68,v,212,a,164\n"
"s_add_u32 s70, s70, 8192\n"
"EX 141\n"
"MA v,68,v,196,a,160\n"
"TR 182,251,30208\n"
"CV 188,140,141\n"
"MA v,72,v,200,a,160\n"
"s_add_u32 m0, s69, 57344\n"
"EX 142\n"
"MA v,72,v,216,a,164\n"
"buffer_load_dwordx4 v253, s[44:47], s27 offen lds\n"
"MA v,76,v,220,a,164\n"
"EX 143\n"
"MA v,76,v,204,a,160\n"
"s_waitcnt lgkmcnt(4)\n"
"TR 184,252,17920\n"
"CV 189,142,143\n"
"MA v,92,v,204,a,168\n"
"EX 156\n"
"MA v,92,v,220,a,172\n"
"TR 186,252,22016\n"
"MA v,88,v,216,a,172\n"
"EX 157\n"
"MA v,88,v,200,a,168\n"
"CV 190,156,157\n"
"MA v,84,v,196,a,168\n"
"EX 158\n"
"MA v,84,v,212,a,172\n"
"TR 188,252,26112\n"
"MA v,80,v,208,a,172\n"
"EX 159\n"
"MA v,80,v,192,a,168\n"
"s_waitcnt lgkmcnt(3)\n"
"TR 190,252,30208\n"
"CV 191,158,159\n"
"MA v,96,v,192,a,176\n"
"s_add_u32 m0, s69, 58240\n"
"EX 152\n"
"MA v,96,v,208,a,180\n"
"s_memrealtime vcc\n"
"buffer_load_dwordx4 v253, s[44:47], s27 offen offset:128 lds\n"
"MA v,100,v,212,a,180\n"
"EX 153\n"
"MA v,100,v,196,a,176\n"
"CV 186,152,153\n"
"MA v,104,v,200,a,176\n"
"s_waitcnt lgkmcnt(0)\n"
"EX 154\n"
"MA v,104,v,216,a,180\n"
"s_waitcnt vmcnt(20)\n"
"MA v,108,v,220,a,180\n"
"s_barrier\n"
"EX 155\n"
"MA v,108,v,204,a,176\n"
"RD 128,249,16384\n"
"CV 187,154,155\n"
"MA v,124,v,204,a,184\n"
"s_sub_u32 s33, s72, vcc_lo\n"
"EX 148\n"
"MA v,124,v,220,a,188\n"
"s_add_u32 s72, vcc_lo, s77\n"
"RD 132,250,16384\n"
"MA v,120,v,216,a,188\n"
"s_max_i32 s33, s33, -150\n"
"EX 149\n"
"MA v,120,v,200,a,184\n"
"s_add_u32 s72, s72, s33\n"
"RD 136,249,17408\n"
"CV 182,148,149\n"
"MA v,116,v,196,a,184\n"
"s_cmp_gt_i32 s33, 0\n"
"EX 150\n"
"MA v,116,v,212,a,188\n"
"s_cbranch_scc1 L_PACE9_%=\n"
"L_PACEB9_%=:\n"
"RD 140,250,17408\n"
"MA v,112,v,208,a,188\n"
"EX 151\n"
"MA v,112,v,192,a,184\n"
"CV 183,150,151\n"
"MA v,240,v,160,a,236\n"
"s_mov_b32 s80, 7168\n"
"MA v,240,v,164,a,240\n"
"buffer_store_dwordx4 a[28:31], v255, s[56:59], s80 offen sc1\n"
"MA v,240,v,168,a,244\n"
"MA v,240,v,172,a,248\n"
"s_waitcnt lgkmcnt(0)\n"
"MC 128,128,64,224\n"
"EX 144\n"
"MA v,128,a,132,a,68\n"
"RD 144,249,20480\n"
"MA v,128,a,136,a,72\n"
"EX 145\n"
"MA v,128,a,140,a,76\n"
"RD 148,250,20480\n"
"CV 178,144,145\n"
"MC 132,140,92,228\n"
"EX 146\n"
"MA v,132,a,136,a,88\n"
"RD 152,249,21504\n"
"MA v,132,a,132,a,84\n"
"EX 147\n"
"MA v,132,a,128,a,80\n"
"RD 156,250,21504\n"
"CV 179,146,147\n"
"MC 136,128,96,232\n"
"s_add_u32 m0, s69, 81920\n"
"EX 128\n"
"MA v,136,a,132,a,100\n"
"buffer_load_dwordx4 v254, s[48:51], s26 offen nt lds\n"
"MA v,136,a,136,a,104\n"
"EX 129\n"
"MA v,136,a,140,a,108\n"
"TR 160,251,32768\n"
"CV 192,128,129\n"
"MC 140,140,124,236\n"
"EX 130\n"
"MA v,140,a,136,a,120\n"
"TR 162,251,36864\n"
"MA v,140,a,132,a,116\n"
"EX 131\n"
"MA v,140,a,128,a,112\n"
"s_waitcnt lgkmcnt(2)\n"
"TR 164,251,40960\n"
"CV 193,130,131\n"
"MC 156,144,112,236\n"
"EX 132\n"
"MA v,156,a,148,a,116\n"
"TR 166,251,45056\n"
"MA v,156,a,152,a,120\n"
"EX 133\n"
"MA v,156,a,156,a,124\n"
"TR 168,252,32768\n"
"CV 196,132,133\n"
"MC 152,156,108,232\n"
"s_add_u32 m0, s69, 82816\n"
"EX 134\n"
"MA v,152,a,152,a,104\n"
"buffer_load_dwordx4 v254, s[48:51], s26 offen offset:128 nt lds\n"
"MA v,152,a,148,a,100\n"
"s_add_u32 s25, s70, s71\n"
"EX 135\n"
"MA v,152,a,144,a,96\n"
"TR 170,252,36864\n"
"CV 197,134,135\n"
"MC 148,144,80,228\n"
"s_and_b32 s25, s25, 0x3ffff\n"
"EX 136\n"
"MA v,148,a,148,a,84\n"
"TR 172,252,40960\n"
"MA v,148,a,152,a,88\n"
"s_and_b32 s81, s70, 0xfffc0000\n"
"EX 137\n"
"MA v,148,a,156,a,92\n"
"TR 174,252,45056\n"
"CV 200,136,137\n"
"MC 144,156,76,224\n"
"EX 138\n"
"MA v,144,a,152,a,72\n"
"TR 176,251,33280\n"
"MA v,144,a,148,a,68\n"
"s_or_b32 s25, s25, s81\n"
"EX 139\n"
"MA v,144,a,144,a,64\n"
"s_waitcnt lgkmcnt(5)\n"
"TR 178,251,37376\n"
"CV 201,138,139\n"
"MA v,0,v,160,a,160\n"
"EX 140\n"
"MA v,0,v,176,a,164\n"
"TR 180,251,41472\n"
"MA v,4,v,180,a,164\n"
"s_add_u32 s70, s70, 8192\n"
"EX 141\n"
"MA v,4,v,164,a,160\n"
"TR 182,251,45568\n"
"CV 204,140,141\n"
"MA v,8,v,168,a,160\n"
"s_add_u32 m0, s69, 0\n"
"EX 142\n"
"MA v,8,v,184,a,164\n"
"buffer_load_dwordx4 v253, s[44:47], s24 offen nt lds\n"
"MA v,12,v,188,a,164\n"
"EX 143\n"
"MA v,12,v,172,a,160\n"
"s_waitcnt lgkmcnt(4)\n"
"TR 184,252,33280\n"
"CV 205,142,143\n"
"MA v,28,v,172,a,168\n"
"EX 156\n"
"MA v,28,v,188,a,172\n"
"TR 186,252,37376\n"
"MA v,24,v,184,a,172\n"
"EX 157\n"
"MA v,24,v,168,a,168\n"
"global_load_dword a255, v255, s[34:35]\n"
"CV 206,156,157\n"
"MA v,20,v,164,a,168\n"
"EX 158\n"
"MA v,20,v,180,a,172\n"
"TR 188,252,41472\n"
"MA v,16,v,176,a,172\n"
"EX 159\n"
"MA v,16,v,160,a,168\n"
"s_waitcnt lgkmcnt(3)\n"
"TR 190,252,45568\n"
"CV 207,158,159\n"
"MA v,32,v,160,a,176\n"
"s_add_u32 m0, s69, 896\n"
"EX 152\n"
"MA v,32,v,176,a,180\n"
"buffer_load_dwordx4 v253, s[44:47], s24 offen offset:128 nt lds\n"
"MA v,36,v,180,a,180\n"
"EX 153\n"
"MA v,36,v,164,a,176\n"
"CV 202,152,153\n"
"MA v,40,v,168,a,176\n"
"EX 154\n"
"MA v,40,v,184,a,180\n"
"MA v,44,v,188,a,180\n"
"EX 155\n"
"MA v,44,v,172,a,176\n"
"s_waitcnt lgkmcnt(0)\n"
"RD 128,249,24576\n"
"CV 203,154,155\n"
"MA v,60,v,172,a,184\n"
"EX 148\n"
"MA v,60,v,188,a,188\n"
"RD 132,250,24576\n"
"MA v,56,v,184,a,188\n"
"EX 149\n"
"MA v,56,v,168,a,184\n"
"RD 136,249,25600\n"
"CV 198,148,149\n"
"MA v,52,v,164,a,184\n"
"EX 150\n"
"MA v,52,v,180,a,188\n"
"RD 140,250,25600\n"
"MA v,48,v,176,a,188\n"
"EX 151\n"
"MA v,48,v,160,a,184\n"
"CV 199,150,151\n"
"MA v,240,v,176,a,236\n"
"s_mov_b32 s80, 8192\n"
"MA v,240,v,180,a,240\n"
"buffer_store_dwordx4 a[32:35], v255, s[56:59], s80 offen sc1\n"
"MA v,240,v,184,a,244\n"
"MA v,240,v,188,a,248\n"
"s_waitcnt lgkmcnt(0)\n"
"MC 128,128,64,224\n"
"L_ST12_%=:\n"
"s_cbranch_execz L_VB2_%=\n"
"EX 144\n"
"MA v,128,a,132,a,68\n"
"RD 144,249,28672\n"
"MA v,128,a,136,a,72\n"
"EX 145\n"
"MA v,128,a,140,a,76\n"
"RD 148,250,28672\n"
"CV 194,144,145\n"
"MC 132,140,92,228\n"
"EX 146\n"
"MA v,132,a,136,a,88\n"
"RD 152,249,29696\n"
"MA v,132,a,132,a,84\n"
"EX 147\n"
"MA v,132,a,128,a,80\n"
"RD 156,250,29696\n"
"CV 195,146,147\n"
"MC 136,128,96,232\n"
"s_add_u32 m0, s69, 90112\n"
"EX 128\n"
"MA v,136,a,132,a,100\n"
"buffer_load_dwordx4 v254, s[48:51], s27 offen nt lds\n"
"MA v,136,a,136,a,104\n"
"EX 129\n"
"MA v,136,a,140,a,108\n"
"TR 160,251,33792\n"
"CV 208,128,129\n"
"MC 140,140,124,236\n"
"EX 130\n"
"MA v,140,a,136,a,120\n"
"TR 162,251,37888\n"
"MA v,140,a,132,a,116\n"
"EX 131\n"
"MA v,140,a,128,a,112\n"
"s_waitcnt lgkmcnt(2)\n"
"TR 164,251,41984\n"
"CV 209,130,131\n"
"MC 156,144,112,236\n"
"EX 132\n"
"MA v,156,a,148,a,116\n"
"TR 166,251,46080\n"
"MA v,156,a,152,a,120\n"
"EX 133\n"
"MA v,156,a,156,a,124\n"
"TR 168,252,33792\n"
"CV 212,132,133\n"
"MC 152,156,108,232\n"
"s_add_u32 m0, s69, 91008\n"
"EX 134\n"
"MA v,152,a,152,a,104\n"
"buffer_load_dwordx4 v254, s[48:51], s27 offen offset:128 nt lds\n"
"MA v,152,a,148,a,100\n"
"s_add_u32 s26, s70, s71\n"
"EX 135\n"
"MA v,152,a,144,a,96\n"
"TR 170,252,37888\n"
"CV 213,134,135\n"
"MC 148,144,80,228\n"
"s_and_b32 s26, s26, 0x3ffff\n"
"EX 136\n"
"MA v,148,a,148,a,84\n"
"TR 172,252,41984\n"
"MA v,148,a,152,a,88\n"
"s_and_b32 s81, s70, 0xfffc0000\n"
"EX 137\n"
"MA v,148,a,156,a,92\n"
"TR 174,252,46080\n"
"CV 216,136,137\n"
"MC 144,156,76,224\n"
"EX 138\n"
"MA v,144,a,152,a,72\n"
"TR 176,251,34304\n"
"MA v,144,a,148,a,68\n"
"s_or_b32 s26, s26, s81\n"
"EX 139\n"
"MA v,144,a,144,a,64\n"
"s_waitcnt lgkmcnt(5)\n"
"TR 178,251,38400\n"
"CV 217,138,139\n"
"MA v,64,v,160,a,160\n"
"EX 140\n"
"MA v,64,v,176,a,164\n"
"TR 180,251,42496\n"
"MA v,68,v,180,a,164\n"
"s_add_u32 s70, s70, 8192\n"
"EX 141\n"
"MA v,68,v,164,a,160\n"
"TR 182,251,46592\n"
"CV 220,140,141\n"
"MA v,72,v,168,a,160\n"
"s_add_u32 m0, s69, 8192\n"
"EX 142\n"
"MA v,72,v,184,a,164\n"
"buffer_load_dwordx4 v253, s[44:47], s25 offen lds\n"
"MA v,76,v,188,a,164\n"
"EX 143\n"
"MA v,76,v,172,a,160\n"
"s_waitcnt lgkmcnt(4)\n"
"TR 184,252,34304\n"
"CV 221,142,143\n"
"MA v,92,v,172,a,168\n"
"EX 156\n"
"MA v,92,v,188,a,172\n"
"TR 186,252,38400\n"
"MA v,88,v,184,a,172\n"
"EX 157\n"
"MA v,88,v,168,a,168\n"
"CV 222,156,157\n"
"MA v,84,v,164,a,168\n"
"EX 158\n"
"MA v,84,v,180,a,172\n"
"TR 188,252,42496\n"
"MA v,80,v,176,a,172\n"
"EX 159\n"
"MA v,80,v,160,a,168\n"
"s_waitcnt lgkmcnt(3)\n"
"TR 190,252,46592\n"
"CV 223,158,159\n"
"MA v,96,v,160,a,176\n"
"s_add_u32 m0, s69, 9088\n"
"EX 152\n"
"MA v,96,v,176,a,180\n"
"s_memrealtime vcc\n"
"buffer_load_dwordx4 v253, s[44:47], s25 offen offset:128 lds\n"
"MA v,100,v,180,a,180\n"
"EX 153\n"
"MA v,100,v,164,a,176\n"
"CV 218,152,153\n"
"MA v,104,v,168,a,176\n"
"s_waitcnt lgkmcnt(0)\n"
"EX 154\n"
"MA v,104,v,184,a,180\n"
"s_waitcnt vmcnt(21)\n"
"MA v,108,v,188,a,180\n"
"s_barrier\n"
"EX 155\n"
"MA v,108,v,172,a,176\n"
"RD 128,249,32768\n"
"CV 219,154,155\n"
"MA v,124,v,172,a,184\n"
"s_sub_u32 s33, s72, vcc_lo\n"
"EX 148\n"
"MA v,124,v,188,a,188\n"
"s_add_u32 s72, vcc_lo, s77\n"
"RD 132,250,32768\n"
"MA v,120,v,184,a,188\n"
"s_max_i32 s33, s33, -150\n"
"EX 149\n"
"MA v,120,v,168,a,184\n"
"s_add_u32 s72, s72, s33\n"
"RD 136,249,33792\n"
"CV 214,148,149\n"
"MA v,116,v,164,a,184\n"
"s_cmp_gt_i32 s33, 0\n"
"EX 150\n"
"MA v,116,v,180,a,188\n"
"s_cbranch_scc1 L_PACE10_%=\n"
"L_PACEB10_%=:\n"
"RD 140,250,33792\n"
"MA v,112,v,176,a,188\n"
"EX 151\n"
"MA v,112,v,160,a,184\n"
"CV 215,150,151\n"
"MA v,240,v,192,a,236\n"
"s_mov_b32 s80, 9216\n"
"MA v,240,v,196,a,240\n"
"buffer_store_dwordx4 a[36:39], v255, s[56:59], s80 offen sc1\n"
"MA v,240,v,200,a,244\n"
"MA v,240,v,204,a,248\n"
"s_waitcnt lgkmcnt(0)\n"
"MC 128,128,64,224\n"
"EX 144\n"
"MA v,128,a,132,a,68\n"
"RD 144,249,36864\n"
"MA v,128,a,136,a,72\n"
"EX 145\n"
"MA v,128,a,140,a,76\n"
"RD 148,250,36864\n"
"CV 210,144,145\n"
"MC 132,140,92,228\n"
"EX 146\n"
"MA v,132,a,136,a,88\n"
"RD 152,249,37888\n"
"MA v,132,a,132,a,84\n"
"EX 147\n"
"MA v,132,a,128,a,80\n"
"RD 156,250,37888\n"
"CV 211,146,147\n"
"MC 136,128,96,232\n"
"s_add_u32 m0, s69, 98304\n"
"EX 128\n"
"MA v,136,a,132,a,100\n"
"buffer_load_dwordx4 v254, s[48:51], s24 offen nt lds\n"
"MA v,136,a,136,a,104\n"
"EX 129\n"
"MA v,136,a,140,a,108\n"
"TR 160,251,49152\n"
"CV 160,128,129\n"
"MC 140,140,124,236\n"
"EX 130\n"
"MA v,140,a,136,a,120\n"
"TR 162,251,53248\n"
"MA v,140,a,132,a,116\n"
"EX 131\n"
"MA v,140,a,128,a,112\n"
"s_waitcnt lgkmcnt(2)\n"
"TR 164,251,57344\n"
"CV 161,130,131\n"
"MC 156,144,112,236\n"
"EX 132\n"
"MA v,156,a,148,a,116\n"
"TR 166,251,61440\n"
"MA v,156,a,152,a,120\n"
"EX 133\n"
"MA v,156,a,156,a,124\n"
"TR 168,252,49152\n"
"CV 164,132,133\n"
"MC 152,156,108,232\n"
"s_add_u32 m0, s69, 99200\n"
"EX 134\n"
"MA v,152,a,152,a,104\n"
"buffer_load_dwordx4 v254, s[48:51], s24 offen offset:128 nt lds\n"
"MA v,152,a,148,a,100\n"
"s_add_u32 s27, s70, s71\n"
"EX 135\n"
"MA v,152,a,144,a,96\n"
"TR 170,252,53248\n"
"CV 165,134,135\n"
"MC 148,144,80,228\n"
"s_and_b32 s27, s27, 0x3ffff\n"
"EX 136\n"
"MA v,148,a,148,a,84\n"
"TR 172,252,57344\n"
"MA v,148,a,152,a,88\n"
"s_and_b32 s81, s70, 0xfffc0000\n"
"EX 137\n"
"MA v,148,a,156,a,92\n"
"TR 174,252,61440\n"
"CV 168,136,137\n"
"MC 144,156,76,224\n"
"EX 138\n"
"MA v,144,a,152,a,72\n"
"TR 176,251,49664\n"
"MA v,144,a,148,a,68\n"
"s_or_b32 s27, s27, s81\n"
"EX 139\n"
"MA v,144,a,144,a,64\n"
"s_waitcnt lgkmcnt(5)\n"
"TR 178,251,53760\n"
"CV 169,138,139\n"
"MA v,0,v,192,a,160\n"
"EX 140\n"
"MA v,0,v,208,a,164\n"
"TR 180,251,57856\n"
"MA v,4,v,212,a,164\n"
"s_add_u32 s70, s70, 8192\n"
"EX 141\n"
"MA v,4,v,196,a,160\n"
"TR 182,251,61952\n"
"CV 172,140,141\n"
"MA v,8,v,200,a,160\n"
"s_add_u32 m0, s69, 16384\n"
"EX 142\n"
"MA v,8,v,216,a,164\n"
"buffer_load_dwordx4 v253, s[44:47], s26 offen lds\n"
"MA v,12,v,220,a,164\n"
"EX 143\n"
"MA v,12,v,204,a,160\n"
"s_waitcnt lgkmcnt(4)\n"
"TR 184,252,49664\n"
"CV 173,142,143\n"
"MA v,28,v,204,a,168\n"
"EX 156\n"
"MA v,28,v,220,a,172\n"
"TR 186,252,53760\n"
"MA v,24,v,216,a,172\n"
"EX 157\n"
"MA v,24,v,200,a,168\n"
"CV 174,156,157\n"
"MA v,20,v,196,a,168\n"
"EX 158\n"
"MA v,20,v,212,a,172\n"
"TR 188,252,57856\n"
"MA v,16,v,208,a,172\n"
"EX 159\n"
"MA v,16,v,192,a,168\n"
"s_waitcnt lgkmcnt(3)\n"
"TR 190,252,61952\n"
"CV 175,158,159\n"
"MA v,32,v,192,a,176\n"
"s_add_u32 m0, s69, 17280\n"
"EX 152\n"
"MA v,32,v,208,a,180\n"
"buffer_load_dwordx4 v253, s[44:47], s26 offen offset:128 lds\n"
"MA v,36,v,212,a,180\n"
"EX 153\n"
"MA v,36,v,196,a,176\n"
"CV 170,152,153\n"
"MA v,40,v,200,a,176\n"
"EX 154\n"
"MA v,40,v,216,a,180\n"
"MA v,44,v,220,a,180\n"
"EX 155\n"
"MA v,44,v,204,a,176\n"
"s_waitcnt lgkmcnt(0)\n"
"RD 128,249,40960\n"
"CV 171,154,155\n"
"MA v,60,v,204,a,184\n"
"EX 148\n"
"MA v,60,v,220,a,188\n"
"RD 132,250,40960\n"
"MA v,56,v,216,a,188\n"
"EX 149\n"
"MA v,56,v,200,a,184\n"
"RD 136,249,41984\n"
"CV 166,148,149\n"
"MA v,52,v,196,a,184\n"
"EX 150\n"
"MA v,52,v,212,a,188\n"
"RD 140,250,41984\n"
"MA v,48,v,208,a,188\n"
"EX 151\n"
"MA v,48,v,192,a,184\n"
"CV 167,150,151\n"
"MA v,240,v,208,a,236\n"
"s_mov_b32 s80, 10240\n"
"MA v,240,v,212,a,240\n"
"buffer_store_dwordx4 a[40:43], v255, s[56:59], s80 offen sc1\n"
"MA v,240,v,216,a,244\n"
"MA v,240,v,220,a,248\n"
"s_waitcnt lgkmcnt(0)\n"
"s_mov_b64 exec, 0\n"
"s_cmp_eq_u32 s64, 0\n"
"s_cbranch_scc1 L_ST17_%=\n"
"s_cmp_eq_u32 s64, 1\n"
"s_cbranch_scc1 L_ST18_%=\n"
"s_cmp_eq_u32 s64, 2\n"
"s_cbranch_scc1 L_ST19_%=\n"
"s_cmp_eq_u32 s64, 3\n"
"s_cbranch_scc1 L_ST20_%=\n"
"L_VB4_%=:\n"
"s_mov_b64 exec, -1\n"
"MC 128,128,64,224\n"
"EX 144\n"
"MA v,128,a,132,a,68\n"
"RD 144,249,45056\n"
"MA v,128,a,136,a,72\n"
"EX 145\n"
"MA v,128,a,140,a,76\n"
"RD 148,250,45056\n"
"CV 162,144,145\n"
"MC 132,140,92,228\n"
"EX 146\n"
"MA v,132,a,136,a,88\n"
"RD 152,249,46080\n"
"MA v,132,a,132,a,84\n"
"EX 147\n"
"MA v,132,a,128,a,80\n"
"RD 156,250,46080\n"
"CV 163,146,147\n"
"MC 136,128,96,232\n"
"s_add_u32 m0, s69, 106496\n"
"EX 128\n"
"MA v,136,a,132,a,100\n"
"buffer_load_dwordx4 v254, s[48:51], s25 offen nt lds\n"
"MA v,136,a,136,a,104\n"
"EX 129\n"
"MA v,136,a,140,a,108\n"
"TR 160,251,50176\n"
"CV 176,128,129\n"
"MC 140,140,124,236\n"
"EX 130\n"
"MA v,140,a,136,a,120\n"
"TR 162,251,54272\n"
"MA v,140,a,132,a,116\n"
"EX 131\n"
"MA v,140,a,128,a,112\n"
"s_waitcnt lgkmcnt(2)\n"
"TR 164,251,58368\n"
"CV 177,130,131\n"
"MC 156,144,112,236\n"
"EX 132\n"
"MA v,156,a,148,a,116\n"
"TR 166,251,62464\n"
"MA v,156,a,152,a,120\n"
"EX 133\n"
"MA v,156,a,156,a,124\n"
"TR 168,252,50176\n"
"CV 180,132,133\n"
"MC 152,156,108,232\n"
"s_add_u32 m0, s69, 107392\n"
"EX 134\n"
"MA v,152,a,152,a,104\n"
"buffer_load_dwordx4 v254, s[48:51], s25 offen offset:128 nt lds\n"
"MA v,152,a,148,a,100\n"
"s_add_u32 s24, s70, s71\n"
"EX 135\n"
"MA v,152,a,144,a,96\n"
"TR 170,252,54272\n"
"CV 181,134,135\n"
"MC 148,144,80,228\n"
"s_and_b32 s24, s24, 0x3ffff\n"
"EX 136\n"
"MA v,148,a,148,a,84\n"
"TR 172,252,58368\n"
"MA v,148,a,152,a,88\n"
"s_and_b32 s81, s70, 0xfffc0000\n"
"EX 137\n"
"MA v,148,a,156,a,92\n"
"TR 174,252,62464\n"
"CV 184,136,137\n"
"MC 144,156,76,224\n"
"EX 138\n"
"MA v,144,a,152,a,72\n"
"TR 176,251,50688\n"
"MA v,144,a,148,a,68\n"
"s_or_b32 s24, s24, s81\n"
"EX 139\n"
"MA v,144,a,144,a,64\n"
"s_waitcnt lgkmcnt(5)\n"
"TR 178,251,54784\n"
"CV 185,138,139\n"
"MA v,64,v,192,a,160\n"
"EX 140\n"
"MA v,64,v,208,a,164\n"
"TR 180,251,58880\n"
"MA v,68,v,212,a,164\n"
"s_add_u32 s70, s70, 8192\n"
"EX 141\n"
"MA v,68,v,196,a,160\n"
"TR 182,251,62976\n"
"CV 188,140,141\n"
"MA v,72,v,200,a,160\n"
"s_add_u32 m0, s69, 24576\n"
"EX 142\n"
"MA v,72,v,216,a,164\n"
"buffer_load_dwordx4 v253, s[44:47], s27 offen lds\n"
"MA v,76,v,220,a,164\n"
"EX 143\n"
"MA v,76,v,204,a,160\n"
"s_waitcnt lgkmcnt(4)\n"
"TR 184,252,50688\n"
"CV 189,142,143\n"
"MA v,92,v,204,a,168\n"
"EX 156\n"
"MA v,92,v,220,a,172\n"
"TR 186,252,54784\n"
"MA v,88,v,216,a,172\n"
"EX 157\n"
"MA v,88,v,200,a,168\n"
"CV 190,156,157\n"
"MA v,84,v,196,a,168\n"
"EX 158\n"
"MA v,84,v,212,a,172\n"
"TR 188,252,58880\n"
"MA v,80,v,208,a,172\n"
"EX 159\n"
"MA v,80,v,192,a,168\n"
"s_waitcnt lgkmcnt(3)\n"
"TR 190,252,62976\n"
"CV 191,158,159\n"
"MA v,96,v,192,a,176\n"
"s_add_u32 m0, s69, 25472\n"
"EX 152\n"
"MA v,96,v,208,a,180\n"
"s_memrealtime vcc\n"
"buffer_load_dwordx4 v253, s[44:47], s27 offen offset:128 lds\n"
"MA v,100,v,212,a,180\n"
"EX 153\n"
"MA v,100,v,196,a,176\n"
"CV 186,152,153\n"
"MA v,104,v,200,a,176\n"
"s_waitcnt lgkmcnt(0)\n"
"EX 154\n"
"MA v,104,v,216,a,180\n"
"s_waitcnt vmcnt(21)\n"
"MA v,108,v,220,a,180\n"
"s_barrier\n"
"EX 155\n"
"MA v,108,v,204,a,176\n"
"RD 128,249,49152\n"
"CV 187,154,155\n"
"MA v,124,v,204,a,184\n"
"s_sub_u32 s33, s72, vcc_lo\n"
"EX 148\n"
"MA v,124,v,220,a,188\n"
"s_add_u32 s72, vcc_lo, s77\n"
"RD 132,250,49152\n"
"MA v,120,v,216,a,188\n"
"s_max_i32 s33, s33, -150\n"
"EX 149\n"
"MA v,120,v,200,a,184\n"
"s_add_u32 s72, s72, s33\n"
"RD 136,249,50176\n"
"CV 182,148,149\n"
"MA v,116,v,196,a,184\n"
"s_cmp_gt_i32 s33, 0\n"
"EX 150\n"
"MA v,116,v,212,a,188\n"
"s_cbranch_scc1 L_PACE11_%=\n"
"L_PACEB11_%=:\n"
"RD 140,250,50176\n"
"MA v,112,v,208,a,188\n"
"EX 151\n"
"MA v,112,v,192,a,184\n"
"CV 183,150,151\n"
"MA v,240,v,160,a,236\n"
"s_mov_b32 s80, 11264\n"
"MA v,240,v,164,a,240\n"
"buffer_store_dwordx4 a[44:47], v255, s[56:59], s80 offen sc1\n"
"MA v,240,v,168,a,244\n"
"MA v,240,v,172,a,248\n"
"s_waitcnt lgkmcnt(0)\n"
"MC 128,128,64,224\n"
"EX 144\n"
"MA v,128,a,132,a,68\n"
"RD 144,249,53248\n"
"MA v,128,a,136,a,72\n"
"EX 145\n"
"MA v,128,a,140,a,76\n"
"RD 148,250,53248\n"
"CV 178,144,145\n"
"MC 132,140,92,228\n"
"EX 146\n"
"MA v,132,a,136,a,88\n"
"RD 152,249,54272\n"
"MA v,132,a,132,a,84\n"
"EX 147\n"
"MA v,132,a,128,a,80\n"
"RD 156,250,54272\n"
"CV 179,146,147\n"
"MC 136,128,96,232\n"
"s_add_u32 m0, s69, 114688\n"
"EX 128\n"
"MA v,136,a,132,a,100\n"
"buffer_load_dwordx4 v254, s[48:51], s26 offen nt lds\n"
"MA v,136,a,136,a,104\n"
"EX 129\n"
"MA v,136,a,140,a,108\n"
"TR 160,251,0\n"
"CV 192,128,129\n"
"MC 140,140,124,236\n"
"EX 130\n"
"MA v,140,a,136,a,120\n"
"TR 162,251,4096\n"
"MA v,140,a,132,a,116\n"
"EX 131\n"
"MA v,140,a,128,a,112\n"
"s_waitcnt lgkmcnt(2)\n"
"TR 164,251,8192\n"
"CV 193,130,131\n"
"MC 156,144,112,236\n"
"EX 132\n"
"MA v,156,a,148,a,116\n"
"TR 166,251,12288\n"
"MA v,156,a,152,a,120\n"
"EX 133\n"
"MA v,156,a,156,a,124\n"
"TR 168,252,0\n"
"CV 196,132,133\n"
"MC 152,156,108,232\n"
"s_add_u32 m0, s69, 115584\n"
"EX 134\n"
"MA v,152,a,152,a,104\n"
"buffer_load_dwordx4 v254, s[48:51], s26 offen offset:128 nt lds\n"
"MA v,152,a,148,a,100\n"
"s_add_u32 s25, s70, s71\n"
"EX 135\n"
"MA v,152,a,144,a,96\n"
"TR 170,252,4096\n"
"CV 197,134,135\n"
"MC 148,144,80,228\n"
"s_and_b32 s25, s25, 0x3ffff\n"
"EX 136\n"
"MA v,148,a,148,a,84\n"
"TR 172,252,8192\n"
"MA v,148,a,152,a,88\n"
"s_and_b32 s81, s70, 0xfffc0000\n"
"EX 137\n"
"MA v,148,a,156,a,92\n"
"TR 174,252,12288\n"
"CV 200,136,137\n"
"MC 144,156,76,224\n"
"EX 138\n"
"MA v,144,a,152,a,72\n"
"TR 176,251,512\n"
"MA v,144,a,148,a,68\n"
"s_or_b32 s25, s25, s81\n"
"EX 139\n"
"MA v,144,a,144,a,64\n"
"s_waitcnt lgkmcnt(5)\n"
"TR 178,251,4608\n"
"CV 201,138,139\n"
"MA v,0,v,160,a,160\n"
"EX 140\n"
"MA v,0,v,176,a,164\n"
"TR 180,251,8704\n"
"MA v,4,v,180,a,164\n"
"s_add_u32 s70, s70, 8192\n"
"EX 141\n"
"MA v,4,v,164,a,160\n"
"L_ST13_%=:\n"
"s_cbranch_execz L_VB3_%=\n"
"TR 182,251,12800\n"
"CV 204,140,141\n"
"MA v,8,v,168,a,160\n"
"s_add_u32 m0, s69, 32768\n"
"EX 142\n"
"MA v,8,v,184,a,164\n"
"buffer_load_dwordx4 v253, s[44:47], s24 offen lds\n"
"MA v,12,v,188,a,164\n"
"EX 143\n"
"MA v,12,v,172,a,160\n"
"s_waitcnt lgkmcnt(4)\n"
"TR 184,252,512\n"
"CV 205,142,143\n"
"MA v,28,v,172,a,168\n"
"EX 156\n"
"MA v,28,v,188,a,172\n"
"TR 186,252,4608\n"
"MA v,24,v,184,a,172\n"
"EX 157\n"
"MA v,24,v,168,a,168\n"
"CV 206,156,157\n"
"MA v,20,v,164,a,168\n"
"EX 158\n"
"MA v,20,v,180,a,172\n"
"TR 188,252,8704\n"
"MA v,16,v,176,a,172\n"
"EX 159\n"
"MA v,16,v,160,a,168\n"
"s_waitcnt lgkmcnt(3)\n"
"TR 190,252,12800\n"
"CV 207,158,159\n"
"MA v,32,v,160,a,176\n"
"s_add_u32 m0, s69, 33664\n"
"EX 152\n"
"MA v,32,v,176,a,180\n"
"buffer_load_dwordx4 v253, s[44:47], s24 offen offset:128 lds\n"
"MA v,36,v,180,a,180\n"
"EX 153\n"
"MA v,36,v,164,a,176\n"
"CV 202,152,153\n"
"MA v,40,v,168,a,176\n"
"EX 154\n"
"MA v,40,v,184,a,180\n"
"MA v,44,v,188,a,180\n"
"EX 155\n"
"MA v,44,v,172,a,176\n"
"s_waitcnt lgkmcnt(0)\n"
"RD 128,249,57344\n"
"CV 203,154,155\n"
"MA v,60,v,172,a,184\n"
"EX 148\n"
"MA v,60,v,188,a,188\n"
"RD 132,250,57344\n"
"MA v,56,v,184,a,188\n"
"EX 149\n"
"MA v,56,v,168,a,184\n"
"RD 136,249,58368\n"
"CV 198,148,149\n"
"MA v,52,v,164,a,184\n"
"EX 150\n"
"MA v,52,v,180,a,188\n"
"RD 140,250,58368\n"
"MA v,48,v,176,a,188\n"
"EX 151\n"
"MA v,48,v,160,a,184\n"
"CV 199,150,151\n"
"MA v,240,v,176,a,236\n"
"s_mov_b32 s80, 12288\n"
"MA v,240,v,180,a,240\n"
"buffer_store_dwordx4 a[48:51], v255, s[56:59], s80 offen sc1\n"
"MA v,240,v,184,a,244\n"
"MA v,240,v,188,a,248\n"
"s_waitcnt lgkmcnt(0)\n"
"MC 128,128,64,224\n"
"EX 144\n"
"MA v,128,a,132,a,68\n"
"RD 144,249,61440\n"
"MA v,128,a,136,a,72\n"
"EX 145\n"
"MA v,128,a,140,a,76\n"
"RD 148,250,61440\n"
"CV 194,144,145\n"
"MC 132,140,92,228\n"
"EX 146\n"
"MA v,132,a,136,a,88\n"
"RD 152,249,62464\n"
"MA v,132,a,132,a,84\n"
"EX 147\n"
"MA v,132,a,128,a,80\n"
"RD 156,250,62464\n"
"CV 195,146,147\n"
"MC 136,128,96,232\n"
"s_add_u32 m0, s69, 122880\n"
"EX 128\n"
"MA v,136,a,132,a,100\n"
"buffer_load_dwordx4 v254, s[48:51], s27 offen nt lds\n"
"MA v,136,a,136,a,104\n"
"EX 129\n"
"MA v,136,a,140,a,108\n"
"TR 160,251,1024\n"
"CV 208,128,129\n"
"MC 140,140,124,236\n"
"EX 130\n"
"MA v,140,a,136,a,120\n"
"TR 162,251,5120\n"
"MA v,140,a,132,a,116\n"
"EX 131\n"
"MA v,140,a,128,a,112\n"
"s_waitcnt lgkmcnt(2)\n"
"TR 164,251,9216\n"
"CV 209,130,131\n"
"MC 156,144,112,236\n"
"EX 132\n"
"MA v,156,a,148,a,116\n"
"TR 166,251,13312\n"
"MA v,156,a,152,a,120\n"
"EX 133\n"
"MA v,156,a,156,a,124\n"
"TR 168,252,1024\n"
"CV 212,132,133\n"
"MC 152,156,108,232\n"
"s_add_u32 m0, s69, 123776\n"
"EX 134\n"
"MA v,152,a,152,a,104\n"
"buffer_load_dwordx4 v254, s[48:51], s27 offen offset:128 nt lds\n"
"MA v,152,a,148,a,100\n"
"s_add_u32 s26, s70, s71\n"
"EX 135\n"
"MA v,152,a,144,a,96\n"
"TR 170,252,5120\n"
"CV 213,134,135\n"
"MC 148,144,80,228\n"
"s_and_b32 s26, s26, 0x3ffff\n"
"EX 136\n"
"MA v,148,a,148,a,84\n"
"TR 172,252,9216\n"
"MA v,148,a,152,a,88\n"
"s_and_b32 s81, s70, 0xfffc0000\n"
"EX 137\n"
"MA v,148,a,156,a,92\n"
"TR 174,252,13312\n"
"CV 216,136,137\n"
"MC 144,156,76,224\n"
"EX 138\n"
"MA v,144,a,152,a,72\n"
"TR 176,251,1536\n"
"MA v,144,a,148,a,68\n"
"s_or_b32 s26, s26, s81\n"
"EX 139\n"
"MA v,144,a,144,a,64\n"
"s_waitcnt lgkmcnt(5)\n"
"TR 178,251,5632\n"
"CV 217,138,139\n"
"MA v,64,v,160,a,160\n"
"EX 140\n"
"MA v,64,v,176,a,164\n"
"TR 180,251,9728\n"
"MA v,68,v,180,a,164\n"
"s_add_u32 s70, s70, 8192\n"
"EX 141\n"
"MA v,68,v,164,a,160\n"
"TR 182,251,13824\n"
"CV 220,140,141\n"
"MA v,72,v,168,a,160\n"
"s_add_u32 m0, s69, 40960\n"
"EX 142\n"
"MA v,72,v,184,a,164\n"
"buffer_load_dwordx4 v253, s[44:47], s25 offen lds\n"
"MA v,76,v,188,a,164\n"
"EX 143\n"
"MA v,76,v,172,a,160\n"
"s_waitcnt lgkmcnt(4)\n"
"TR 184,252,1536\n"
"CV 221,142,143\n"
"MA v,92,v,172,a,168\n"
"EX 156\n"
"MA v,92,v,188,a,172\n"
"TR 186,252,5632\n"
"MA v,88,v,184,a,172\n"
"EX 157\n"
"MA v,88,v,168,a,168\n"
"CV 222,156,157\n"
"MA v,84,v,164,a,168\n"
"EX 158\n"
"MA v,84,v,180,a,172\n"
"TR 188,252,9728\n"
"MA v,80,v,176,a,172\n"
"EX 159\n"
"MA v,80,v,160,a,168\n"
"s_waitcnt lgkmcnt(3)\n"
"TR 190,252,13824\n"
"CV 223,158,159\n"
"MA v,96,v,160,a,176\n"
"s_add_u32 m0, s69, 41856\n"
"EX 152\n"
"MA v,96,v,176,a,180\n"
"s_memrealtime vcc\n"
"buffer_load_dwordx4 v253, s[44:47], s25 offen offset:128 lds\n"
"MA v,100,v,180,a,180\n"
"EX 153\n"
"MA v,100,v,164,a,176\n"
"CV 218,152,153\n"
"MA v,104,v,168,a,176\n"
"s_waitcnt lgkmcnt(0)\n"
"EX 154\n"
"MA v,104,v,184,a,180\n"
"s_waitcnt vmcnt(20)\n"
"MA v,108,v,188,a,180\n"
"s_barrier\n"
"EX 155\n"
"MA v,108,v,172,a,176\n"
"RD 128,249,0\n"
"CV 219,154,155\n"
"MA v,124,v,172,a,184\n"
"s_sub_u32 s33, s72, vcc_lo\n"
"EX 148\n"
"MA v,124,v,188,a,188\n"
"s_add_u32 s72, vcc_lo, s77\n"
"RD 132,250,0\n"
"MA v,120,v,184,a,188\n"
"s_max_i32 s33, s33, -150\n"
"EX 149\n"
"MA v,120,v,168,a,184\n"
"s_add_u32 s72, s72, s33\n"
"RD 136,249,1024\n"
"CV 214,148,149\n"
"MA v,116,v,164,a,184\n"
"s_cmp_gt_i32 s33, 0\n"
"EX 150\n"
"MA v,116,v,180,a,188\n"
"s_cbranch_scc1 L_PACE12_%=\n"
"L_PACEB12_%=:\n"
"RD 140,250,1024\n"
"MA v,112,v,176,a,188\n"
"EX 151\n"
"MA v,112,v,160,a,184\n"
"CV 215,150,151\n"
"MA v,240,v,192,a,236\n"
"s_mov_b32 s80, 13312\n"
"MA v,240,v,196,a,240\n"
"buffer_store_dwordx4 a[52:55], v255, s[56:59], s80 offen sc1\n"
"MA v,240,v,200,a,244\n"
"MA v,240,v,204,a,248\n"
"s_waitcnt lgkmcnt(0)\n"
"MC 128,128,64,224\n"
"EX 144\n"
"MA v,128,a,132,a,68\n"
"RD 144,249,4096\n"
"MA v,128,a,136,a,72\n"
"EX 145\n"
"MA v,128,a,140,a,76\n"
"RD 148,250,4096\n"
"CV 210,144,145\n"
"MC 132,140,92,228\n"
"EX 146\n"
"MA v,132,a,136,a,88\n"
"RD 152,249,5120\n"
"MA v,132,a,132,a,84\n"
"EX 147\n"
"MA v,132,a,128,a,80\n"
"RD 156,250,5120\n"
"CV 211,146,147\n"
"MC 136,128,96,232\n"
"s_add_u32 m0, s69, 65536\n"
"EX 128\n"
"MA v,136,a,132,a,100\n"
"buffer_load_dwordx4 v254, s[48:51], s24 offen nt lds\n"
"MA v,136,a,136,a,104\n"
"EX 129\n"
"MA v,136,a,140,a,108\n"
"TR 160,251,16384\n"
"CV 160,128,129\n"
"MC 140,140,124,236\n"
"EX 130\n"
"MA v,140,a,136,a,120\n"
"TR 162,251,20480\n"
"MA v,140,a,132,a,116\n"
"EX 131\n"
"MA v,140,a,128,a,112\n"
"s_waitcnt lgkmcnt(2)\n"
"TR 164,251,24576\n"
"CV 161,130,131\n"
"MC 156,144,112,236\n"
"EX 132\n"
"MA v,156,a,148,a,116\n"
"TR 166,251,28672\n"
"MA v,156,a,152,a,120\n"
"EX 133\n"
"MA v,156,a,156,a,124\n"
"TR 168,252,16384\n"
"CV 164,132,133\n"
"MC 152,156,108,232\n"
"s_add_u32 m0, s69, 66432\n"
"EX 134\n"
"MA v,152,a,152,a,104\n"
"buffer_load_dwordx4 v254, s[48:51], s24 offen offset:128 nt lds\n"
"MA v,152,a,148,a,100\n"
"s_add_u32 s27, s70, s71\n"
"EX 135\n"
"MA v,152,a,144,a,96\n"
"TR 170,252,20480\n"
"CV 165,134,135\n"
"MC 148,144,80,228\n"
"s_and_b32 s27, s27, 0x3ffff\n"
"EX 136\n"
"MA v,148,a,148,a,84\n"
"TR 172,252,24576\n"
"MA v,148,a,152,a,88\n"
"s_and_b32 s81, s70, 0xfffc0000\n"
"EX 137\n"
"MA v,148,a,156,a,92\n"
"TR 174,252,28672\n"
"CV 168,136,137\n"
"MC 144,156,76,224\n"
"EX 138\n"
"MA v,144,a,152,a,72\n"
"TR 176,251,16896\n"
"MA v,144,a,148,a,68\n"
"s_or_b32 s27, s27, s81\n"
"EX 139\n"
"MA v,144,a,144,a,64\n"
"s_waitcnt lgkmcnt(5)\n"
"TR 178,251,20992\n"
"CV 169,138,139\n"
"MA v,0,v,192,a,160\n"
"EX 140\n"
"MA v,0,v,208,a,164\n"
"TR 180,251,25088\n"
"MA v,4,v,212,a,164\n"
"s_add_u32 s70, s70, 8192\n"
"EX 141\n"
"MA v,4,v,196,a,160\n"
"TR 182,251,29184\n"
"CV 172,140,141\n"
"MA v,8,v,200,a,160\n"
"s_add_u32 m0, s69, 49152\n"
"EX 142\n"
"MA v,8,v,216,a,164\n"
"buffer_load_dwordx4 v253, s[44:47], s26 offen lds\n"
"MA v,12,v,220,a,164\n"
"EX 143\n"
"MA v,12,v,204,a,160\n"
"s_waitcnt lgkmcnt(4)\n"
"TR 184,252,16896\n"
"CV 173,142,143\n"
"MA v,28,v,204,a,168\n"
"EX 156\n"
"MA v,28,v,220,a,172\n"
"TR 186,252,20992\n"
"MA v,24,v,216,a,172\n"
"EX 157\n"
"MA v,24,v,200,a,168\n"
"CV 174,156,157\n"
"MA v,20,v,196,a,168\n"
"EX 158\n"
"MA v,20,v,212,a,172\n"
"TR 188,252,25088\n"
"MA v,16,v,208,a,172\n"
"EX 159\n"
"MA v,16,v,192,a,168\n"
"s_waitcnt lgkmcnt(3)\n"
"TR 190,252,29184\n"
"CV 175,158,159\n"
"MA v,32,v,192,a,176\n"
"s_add_u32 m0, s69, 50048\n"
"EX 152\n"
"MA v,32,v,208,a,180\n"
"buffer_load_dwordx4 v253, s[44:47], s26 offen offset:128 lds\n"
"MA v,36,v,212,a,180\n"
"EX 153\n"
"MA v,36,v,196,a,176\n"
"CV 170,152,153\n"
"MA v,40,v,200,a,176\n"
"EX 154\n"
"MA v,40,v,216,a,180\n"
"MA v,44,v,220,a,180\n"
"EX 155\n"
"MA v,44,v,204,a,176\n"
"s_waitcnt lgkmcnt(0)\n"
"RD 128,249,8192\n"
"CV 171,154,155\n"
"MA v,60,v,204,a,184\n"
"EX 148\n"
"MA v,60,v,220,a,188\n"
"RD 132,250,8192\n"
"MA v,56,v,216,a,188\n"
"EX 149\n"
"MA v,56,v,200,a,184\n"
"RD 136,249,9216\n"
"CV 166,148,149\n"
"MA v,52,v,196,a,184\n"
"EX 150\n"
"MA v,52,v,212,a,188\n"
"RD 140,250,9216\n"
"MA v,48,v,208,a,188\n"
"EX 151\n"
"MA v,48,v,192,a,184\n"
"CV 167,150,151\n"
"MA v,240,v,208,a,236\n"
"s_mov_b32 s80, 14336\n"
"MA v,240,v,212,a,240\n"
"buffer_store_dwordx4 a[56:59], v255, s[56:59], s80 offen sc1\n"
"MA v,240,v,216,a,244\n"
"MA v,240,v,220,a,248\n"
"s_waitcnt lgkmcnt(0)\n"
"MC 128,128,64,224\n"
"EX 144\n"
"MA v,128,a,132,a,68\n"
"RD 144,249,12288\n"
"MA v,128,a,136,a,72\n"
"EX 145\n"
"MA v,128,a,140,a,76\n"
"RD 148,250,12288\n"
"CV 162,144,145\n"
"MC 132,140,92,228\n"
"EX 146\n"
"MA v,132,a,136,a,88\n"
"RD 152,249,13312\n"
"MA v,132,a,132,a,84\n"
"EX 147\n"
"MA v,132,a,128,a,80\n"
"RD 156,250,13312\n"
"CV 163,146,147\n"
"MC 136,128,96,232\n"
"s_add_u32 m0, s69, 73728\n"
"EX 128\n"
"MA v,136,a,132,a,100\n"
"buffer_load_dwordx4 v254, s[48:51], s25 offen nt lds\n"
"MA v,136,a,136,a,104\n"
"EX 129\n"
"MA v,136,a,140,a,108\n"
"TR 160,251,17408\n"
"CV 176,128,129\n"
"MC 140,140,124,236\n"
"EX 130\n"
"MA v,140,a,136,a,120\n"
"TR 162,251,21504\n"
"MA v,140,a,132,a,116\n"
"EX 131\n"
"MA v,140,a,128,a,112\n"
"s_waitcnt lgkmcnt(2)\n"
"TR 164,251,25600\n"
"CV 177,130,131\n"
"MC 156,144,112,236\n"
"EX 132\n"
"MA v,156,a,148,a,116\n"
"TR 166,251,29696\n"
"MA v,156,a,152,a,120\n"
"EX 133\n"
"MA v,156,a,156,a,124\n"
"TR 168,252,17408\n"
"CV 180,132,133\n"
"MC 152,156,108,232\n"
"s_add_u32 m0, s69, 74624\n"
"EX 134\n"
"MA v,152,a,152,a,104\n"
"buffer_load_dwordx4 v254, s[48:51], s25 offen offset:128 nt lds\n"
"MA v,152,a,148,a,100\n"
"s_add_u32 s24, s70, s71\n"
"EX 135\n"
"MA v,152,a,144,a,96\n"
"TR 170,252,21504\n"
"CV 181,134,135\n"
"MC 148,144,80,228\n"
"s_and_b32 s24, s24, 0x3ffff\n"
"EX 136\n"
"MA v,148,a,148,a,84\n"
"TR 172,252,25600\n"
"MA v,148,a,152,a,88\n"
"s_and_b32 s81, s70, 0xfffc0000\n"
"EX 137\n"
"MA v,148,a,156,a,92\n"
"TR 174,252,29696\n"
"CV 184,136,137\n"
"MC 144,156,76,224\n"
"EX 138\n"
"MA v,144,a,152,a,72\n"
"TR 176,251,17920\n"
"MA v,144,a,148,a,68\n"
"s_or_b32 s24, s24, s81\n"
"EX 139\n"
"MA v,144,a,144,a,64\n"
"s_waitcnt lgkmcnt(5)\n"
"TR 178,251,22016\n"
"CV 185,138,139\n"
"MA v,64,v,192,a,160\n"
"EX 140\n"
"MA v,64,v,208,a,164\n"
"TR 180,251,26112\n"
"MA v,68,v,212,a,164\n"
"s_add_u32 s70, s70, 8192\n"
"EX 141\n"
"MA v,68,v,196,a,160\n"
"TR 182,251,30208\n"
"CV 188,140,141\n"
"MA v,72,v,200,a,160\n"
"s_add_u32 m0, s69, 57344\n"
"EX 142\n"
"MA v,72,v,216,a,164\n"
"buffer_load_dwordx4 v253, s[44:47], s27 offen lds\n"
"MA v,76,v,220,a,164\n"
"EX 143\n"
"MA v,76,v,204,a,160\n"
"s_waitcnt lgkmcnt(4)\n"
"TR 184,252,17920\n"
"CV 189,142,143\n"
"MA v,92,v,204,a,168\n"
"EX 156\n"
"MA v,92,v,220,a,172\n"
"TR 186,252,22016\n"
"MA v,88,v,216,a,172\n"
"EX 157\n"
"MA v,88,v,200,a,168\n"
"CV 190,156,157\n"
"MA v,84,v,196,a,168\n"
"EX 158\n"
"MA v,84,v,212,a,172\n"
"TR 188,252,26112\n"
"MA v,80,v,208,a,172\n"
"EX 159\n"
"MA v,80,v,192,a,168\n"
"s_waitcnt lgkmcnt(3)\n"
"TR 190,252,30208\n"
"CV 191,158,159\n"
"MA v,96,v,192,a,176\n"
"s_add_u32 m0, s69, 58240\n"
"EX 152\n"
"MA v,96,v,208,a,180\n"
"s_memrealtime vcc\n"
"buffer_load_dwordx4 v253, s[44:47], s27 offen offset:128 lds\n"
"MA v,100,v,212,a,180\n"
"EX 153\n"
"MA v,100,v,196,a,176\n"
"CV 186,152,153\n"
"MA v,104,v,200,a,176\n"
"s_waitcnt lgkmcnt(0)\n"
"EX 154\n"
"MA v,104,v,216,a,180\n"
"s_waitcnt vmcnt(20)\n"
"MA v,108,v,220,a,180\n"
"s_barrier\n"
"EX 155\n"
"MA v,108,v,204,a,176\n"
"RD 128,249,16384\n"
"CV 187,154,155\n"
"MA v,124,v,204,a,184\n"
"s_sub_u32 s33, s72, vcc_lo\n"
"EX 148\n"
"MA v,124,v,220,a,188\n"
"s_add_u32 s72, vcc_lo, s77\n"
"RD 132,250,16384\n"
"MA v,120,v,216,a,188\n"
"s_max_i32 s33, s33, -150\n"
"EX 149\n"
"MA v,120,v,200,a,184\n"
"s_add_u32 s72, s72, s33\n"
"RD 136,249,17408\n"
"CV 182,148,149\n"
"MA v,116,v,196,a,184\n"
"s_cmp_gt_i32 s33, 0\n"
"EX 150\n"
"MA v,116,v,212,a,188\n"
"s_cbranch_scc1 L_PACE13_%=\n"
"L_PACEB13_%=:\n"
"RD 140,250,17408\n"
"MA v,112,v,208,a,188\n"
"EX 151\n"
"MA v,112,v,192,a,184\n"
"CV 183,150,151\n"
"MA v,240,v,160,a,236\n"
"s_mov_b32 s80, 15360\n"
"MA v,240,v,164,a,240\n"
"buffer_store_dwordx4 a[60:63], v255, s[56:59], s80 offen sc1\n"
"MA v,240,v,168,a,244\n"
"MA v,240,v,172,a,248\n"
"s_waitcnt lgkmcnt(0)\n"
"MC 128,128,64,224\n"
"EX 144\n"
"MA v,128,a,132,a,68\n"
"RD 144,249,20480\n"
"MA v,128,a,136,a,72\n"
"EX 145\n"
"MA v,128,a,140,a,76\n"
"RD 148,250,20480\n"
"CV 178,144,145\n"
"MC 132,140,92,228\n"
"EX 146\n"
"MA v,132,a,136,a,88\n"
"L_ST14_%=:\n"
"s_cbranch_execz L_VB3_%=\n"
"RD 152,249,21504\n"
"MA v,132,a,132,a,84\n"
"EX 147\n"
"MA v,132,a,128,a,80\n"
"RD 156,250,21504\n"
"CV 179,146,147\n"
"MC 136,128,96,232\n"
"s_add_u32 m0, s69, 81920\n"
"EX 128\n"
"MA v,136,a,132,a,100\n"
"buffer_load_dwordx4 v254, s[48:51], s26 offen nt lds\n"
"MA v,136,a,136,a,104\n"
"EX 129\n"
"MA v,136,a,140,a,108\n"
"TR 160,251,32768\n"
"CV 192,128,129\n"
"MC 140,140,124,236\n"
"EX 130\n"
"MA v,140,a,136,a,120\n"
"TR 162,251,36864\n"
"MA v,140,a,132,a,116\n"
"EX 131\n"
"MA v,140,a,128,a,112\n"
"s_waitcnt lgkmcnt(2)\n"
"TR 164,251,40960\n"
"CV 193,130,131\n"
"MC 156,144,112,236\n"
"EX 132\n"
"MA v,156,a,148,a,116\n"
"TR 166,251,45056\n"
"MA v,156,a,152,a,120\n"
"EX 133\n"
"MA v,156,a,156,a,124\n"
"TR 168,252,32768\n"
"CV 196,132,133\n"
"MC 152,156,108,232\n"
"s_add_u32 m0, s69, 82816\n"
"EX 134\n"
"MA v,152,a,152,a,104\n"
"buffer_load_dwordx4 v254, s[48:51], s26 offen offset:128 nt lds\n"
"MA v,152,a,148,a,100\n"
"s_add_u32 s25, s70, s71\n"
"EX 135\n"
"MA v,152,a,144,a,96\n"
"TR 170,252,36864\n"
"CV 197,134,135\n"
"MC 148,144,80,228\n"
"s_and_b32 s25, s25, 0x3ffff\n"
"EX 136\n"
"MA v,148,a,148,a,84\n"
"TR 172,252,40960\n"
"MA v,148,a,152,a,88\n"
"s_and_b32 s81, s70, 0xfffc0000\n"
"EX 137\n"
"MA v,148,a,156,a,92\n"
"TR 174,252,45056\n"
"CV 200,136,137\n"
"MC 144,156,76,224\n"
"EX 138\n"
"MA v,144,a,152,a,72\n"
"TR 176,251,33280\n"
"MA v,144,a,148,a,68\n"
"s_or_b32 s25, s25, s81\n"
"EX 139\n"
"MA v,144,a,144,a,64\n"
"s_waitcnt lgkmcnt(5)\n"
"TR 178,251,37376\n"
"CV 201,138,139\n"
"MA v,0,v,160,a,160\n"
"EX 140\n"
"MA v,0,v,176,a,164\n"
"TR 180,251,41472\n"
"MA v,4,v,180,a,164\n"
"s_add_u32 s70, s70, 8192\n"
"EX 141\n"
"MA v,4,v,164,a,160\n"
"TR 182,251,45568\n"
"CV 204,140,141\n"
"MA v,8,v,168,a,160\n"
"s_add_u32 m0, s69, 0\n"
"EX 142\n"
"MA v,8,v,184,a,164\n"
"buffer_load_dwordx4 v253, s[44:47], s24 offen nt lds\n"
"MA v,12,v,188,a,164\n"
"EX 143\n"
"MA v,12,v,172,a,160\n"
"s_waitcnt lgkmcnt(4)\n"
"TR 184,252,33280\n"
"CV 205,142,143\n"
"MA v,28,v,172,a,168\n"
"EX 156\n"
"MA v,28,v,188,a,172\n"
"TR 186,252,37376\n"
"MA v,24,v,184,a,172\n"
"EX 157\n"
"MA v,24,v,168,a,168\n"
"CV 206,156,157\n"
"MA v,20,v,164,a,168\n"
"EX 158\n"
"MA v,20,v,180,a,172\n"
"TR 188,252,41472\n"
"MA v,16,v,176,a,172\n"
"EX 159\n"
"MA v,16,v,160,a,168\n"
"s_waitcnt lgkmcnt(3)\n"
"TR 190,252,45568\n"
"CV 207,158,159\n"
"MA v,32,v,160,a,176\n"
"s_add_u32 m0, s69, 896\n"
"EX 152\n"
"MA v,32,v,176,a,180\n"
"buffer_load_dwordx4 v253, s[44:47], s24 offen offset:128 nt lds\n"
"MA v,36,v,180,a,180\n"
"EX 153\n"
"MA v,36,v,164,a,176\n"
"CV 202,152,153\n"
"MA v,40,v,168,a,176\n"
"EX 154\n"
"MA v,40,v,184,a,180\n"
"MA v,44,v,188,a,180\n"
"EX 155\n"
"MA v,44,v,172,a,176\n"
"s_waitcnt lgkmcnt(0)\n"
"RD 128,249,24576\n"
"CV 203,154,155\n"
"MA v,60,v,172,a,184\n"
"EX 148\n"
"MA v,60,v,188,a,188\n"
"RD 132,250,24576\n"
"MA v,56,v,184,a,188\n"
"EX 149\n"
"MA v,56,v,168,a,184\n"
"RD 136,249,25600\n"
"CV 198,148,149\n"
"MA v,52,v,164,a,184\n"
"EX 150\n"
"MA v,52,v,180,a,188\n"
"RD 140,250,25600\n"
"MA v,48,v,176,a,188\n"
"EX 151\n"
"MA v,48,v,160,a,184\n"
"CV 199,150,151\n"
"MA v,240,v,176,a,236\n"
"MA v,240,v,180,a,240\n"
"MA v,240,v,184,a,244\n"
"MA v,240,v,188,a,248\n"
"s_waitcnt lgkmcnt(0)\n"
"MC 128,128,64,224\n"
"EX 144\n"
"MA v,128,a,132,a,68\n"
"RD 144,249,28672\n"
"MA v,128,a,136,a,72\n"
"EX 145\n"
"MA v,128,a,140,a,76\n"
"RD 148,250,28672\n"
"CV 194,144,145\n"
"MC 132,140,92,228\n"
"EX 146\n"
"MA v,132,a,136,a,88\n"
"RD 152,249,29696\n"
"MA v,132,a,132,a,84\n"
"EX 147\n"
"MA v,132,a,128,a,80\n"
"RD 156,250,29696\n"
"CV 195,146,147\n"
"MC 136,128,96,232\n"
"s_add_u32 m0, s69, 90112\n"
"EX 128\n"
"MA v,136,a,132,a,100\n"
"buffer_load_dwordx4 v254, s[48:51], s27 offen nt lds\n"
"MA v,136,a,136,a,104\n"
"EX 129\n"
"MA v,136,a,140,a,108\n"
"TR 160,251,33792\n"
"CV 208,128,129\n"
"MC 140,140,124,236\n"
"EX 130\n"
"MA v,140,a,136,a,120\n"
"TR 162,251,37888\n"
"MA v,140,a,132,a,116\n"
"EX 131\n"
"MA v,140,a,128,a,112\n"
"s_waitcnt lgkmcnt(2)\n"
"TR 164,251,41984\n"
"CV 209,130,131\n"
"MC 156,144,112,236\n"
"EX 132\n"
"MA v,156,a,148,a,116\n"
"TR 166,251,46080\n"
"MA v,156,a,152,a,120\n"
"EX 133\n"
"MA v,156,a,156,a,124\n"
"TR 168,252,33792\n"
"CV 212,132,133\n"
"MC 152,156,108,232\n"
"s_add_u32 m0, s69, 91008\n"
"EX 134\n"
"MA v,152,a,152,a,104\n"
"buffer_load_dwordx4 v254, s[48:51], s27 offen offset:128 nt lds\n"
"MA v,152,a,148,a,100\n"
"s_add_u32 s26, s70, s71\n"
"EX 135\n"
"MA v,152,a,144,a,96\n"
"TR 170,252,37888\n"
"CV 213,134,135\n"
"MC 148,144,80,228\n"
"s_and_b32 s26, s26, 0x3ffff\n"
"EX 136\n"
"MA v,148,a,148,a,84\n"
"TR 172,252,41984\n"
"MA v,148,a,152,a,88\n"
"s_and_b32 s81, s70, 0xfffc0000\n"
"EX 137\n"
"MA v,148,a,156,a,92\n"
"TR 174,252,46080\n"
"CV 216,136,137\n"
"MC 144,156,76,224\n"
"EX 138\n"
"MA v,144,a,152,a,72\n"
"TR 176,251,34304\n"
"MA v,144,a,148,a,68\n"
"s_or_b32 s26, s26, s81\n"
"EX 139\n"
"MA v,144,a,144,a,64\n"
"s_waitcnt lgkmcnt(5)\n"
"TR 178,251,38400\n"
"CV 217,138,139\n"
"MA v,64,v,160,a,160\n"
"EX 140\n"
"MA v,64,v,176,a,164\n"
"TR 180,251,42496\n"
"MA v,68,v,180,a,164\n"
"s_add_u32 s70, s70, 8192\n"
"EX 141\n"
"MA v,68,v,164,a,160\n"
"TR 182,251,46592\n"
"CV 220,140,141\n"
"MA v,72,v,168,a,160\n"
"s_add_u32 m0, s69, 8192\n"
"EX 142\n"
"MA v,72,v,184,a,164\n"
"buffer_load_dwordx4 v253, s[44:47], s25 offen lds\n"
"MA v,76,v,188,a,164\n"
"EX 143\n"
"MA v,76,v,172,a,160\n"
"s_waitcnt lgkmcnt(4)\n"
"TR 184,252,34304\n"
"CV 221,142,143\n"
"MA v,92,v,172,a,168\n"
"EX 156\n"
"MA v,92,v,188,a,172\n"
"TR 186,252,38400\n"
"MA v,88,v,184,a,172\n"
"EX 157\n"
"MA v,88,v,168,a,168\n"
"CV 222,156,157\n"
"MA v,84,v,164,a,168\n"
"EX 158\n"
"MA v,84,v,180,a,172\n"
"TR 188,252,42496\n"
"MA v,80,v,176,a,172\n"
"EX 159\n"
"MA v,80,v,160,a,168\n"
"s_waitcnt lgkmcnt(3)\n"
"TR 190,252,46592\n"
"CV 223,158,159\n"
"MA v,96,v,160,a,176\n"
"s_add_u32 m0, s69, 9088\n"
"EX 152\n"
"MA v,96,v,176,a,180\n"
"s_memrealtime vcc\n"
"buffer_load_dwordx4 v253, s[44:47], s25 offen offset:128 lds\n"
"MA v,100,v,180,a,180\n"
"EX 153\n"
"MA v,100,v,164,a,176\n"
"CV 218,152,153\n"
"MA v,104,v,168,a,176\n"
"s_waitcnt lgkmcnt(0)\n"
"EX 154\n"
"MA v,104,v,184,a,180\n"
"s_waitcnt vmcnt(19)\n"
"MA v,108,v,188,a,180\n"
"s_barrier\n"
"EX 155\n"
"MA v,108,v,172,a,176\n"
"RD 128,249,32768\n"
"CV 219,154,155\n"
"MA v,124,v,172,a,184\n"
"s_sub_u32 s33, s72, vcc_lo\n"
"EX 148\n"
"MA v,124,v,188,a,188\n"
"s_add_u32 s72, vcc_lo, s77\n"
"RD 132,250,32768\n"
"MA v,120,v,184,a,188\n"
"s_max_i32 s33, s33, -150\n"
"EX 149\n"
"MA v,120,v,168,a,184\n"
"s_add_u32 s72, s72, s33\n"
"RD 136,249,33792\n"
"CV 214,148,149\n"
"MA v,116,v,164,a,184\n"
"s_cmp_gt_i32 s33, 0\n"
"EX 150\n"
"MA v,116,v,180,a,188\n"
"s_cbranch_scc1 L_PACE14_%=\n"
"L_PACEB14_%=:\n"
"RD 140,250,33792\n"
"MA v,112,v,176,a,188\n"
"EX 151\n"
"MA v,112,v,160,a,184\n"
"CV 215,150,151\n"
"MA v,240,v,192,a,236\n"
"MA v,240,v,196,a,240\n"
"MA v,240,v,200,a,244\n"
"MA v,240,v,204,a,248\n"
"s_waitcnt lgkmcnt(0)\n"
"MC 128,128,64,224\n"
"EX 144\n"
"MA v,128,a,132,a,68\n"
"RD 144,249,36864\n"
"MA v,128,a,136,a,72\n"
"EX 145\n"
"MA v,128,a,140,a,76\n"
"RD 148,250,36864\n"
"CV 210,144,145\n"
"MC 132,140,92,228\n"
"EX 146\n"
"MA v,132,a,136,a,88\n"
"RD 152,249,37888\n"
"MA v,132,a,132,a,84\n"
"EX 147\n"
"MA v,132,a,128,a,80\n"
"RD 156,250,37888\n"
"CV 211,146,147\n"
"MC 136,128,96,232\n"
"s_add_u32 m0, s69, 98304\n"
"EX 128\n"
"MA v,136,a,132,a,100\n"
"buffer_load_dwordx4 v254, s[48:51], s24 offen nt lds\n"
"MA v,136,a,136,a,104\n"
"EX 129\n"
"MA v,136,a,140,a,108\n"
"TR 160,251,49152\n"
"CV 160,128,129\n"
"MC 140,140,124,236\n"
"EX 130\n"
"MA v,140,a,136,a,120\n"
"TR 162,251,53248\n"
"MA v,140,a,132,a,116\n"
"EX 131\n"
"MA v,140,a,128,a,112\n"
"s_waitcnt lgkmcnt(2)\n"
"TR 164,251,57344\n"
"CV 161,130,131\n"
"MC 156,144,112,236\n"
"EX 132\n"
"MA v,156,a,148,a,116\n"
"TR 166,251,61440\n"
"MA v,156,a,152,a,120\n"
"EX 133\n"
"MA v,156,a,156,a,124\n"
"TR 168,252,49152\n"
"CV 164,132,133\n"
"MC 152,156,108,232\n"
"s_add_u32 m0, s69, 99200\n"
"EX 134\n"
"MA v,152,a,152,a,104\n"
"buffer_load_dwordx4 v254, s[48:51], s24 offen offset:128 nt lds\n"
"MA v,152,a,148,a,100\n"
"s_add_u32 s27, s70, s71\n"
"EX 135\n"
"MA v,152,a,144,a,96\n"
"TR 170,252,53248\n"
"CV 165,134,135\n"
"MC 148,144,80,228\n"
"s_and_b32 s27, s27, 0x3ffff\n"
"EX 136\n"
"MA v,148,a,148,a,84\n"
"TR 172,252,57344\n"
"MA v,148,a,152,a,88\n"
"s_and_b32 s81, s70, 0xfffc0000\n"
"EX 137\n"
"MA v,148,a,156,a,92\n"
"TR 174,252,61440\n"
"CV 168,136,137\n"
"MC 144,156,76,224\n"
"EX 138\n"
"MA v,144,a,152,a,72\n"
"TR 176,251,49664\n"
"MA v,144,a,148,a,68\n"
"s_or_b32 s27, s27, s81\n"
"EX 139\n"
"MA v,144,a,144,a,64\n"
"s_waitcnt lgkmcnt(5)\n"
"TR 178,251,53760\n"
"CV 169,138,139\n"
"MA v,0,v,192,a,160\n"
"EX 140\n"
"MA v,0,v,208,a,164\n"
"TR 180,251,57856\n"
"MA v,4,v,212,a,164\n"
"s_add_u32 s70, s70, 8192\n"
"EX 141\n"
"MA v,4,v,196,a,160\n"
"TR 182,251,61952\n"
"CV 172,140,141\n"
"MA v,8,v,200,a,160\n"
"s_add_u32 m0, s69, 16384\n"
"EX 142\n"
"MA v,8,v,216,a,164\n"
"buffer_load_dwordx4 v253, s[44:47], s26 offen lds\n"
"MA v,12,v,220,a,164\n"
"EX 143\n"
"MA v,12,v,204,a,160\n"
"s_waitcnt lgkmcnt(4)\n"
"TR 184,252,49664\n"
"CV 173,142,143\n"
"MA v,28,v,204,a,168\n"
"EX 156\n"
"MA v,28,v,220,a,172\n"
"TR 186,252,53760\n"
"MA v,24,v,216,a,172\n"
"EX 157\n"
"MA v,24,v,200,a,168\n"
"CV 174,156,157\n"
"MA v,20,v,196,a,168\n"
"EX 158\n"
"MA v,20,v,212,a,172\n"
"TR 188,252,57856\n"
"MA v,16,v,208,a,172\n"
"EX 159\n"
"MA v,16,v,192,a,168\n"
"s_waitcnt lgkmcnt(3)\n"
"TR 190,252,61952\n"
"CV 175,158,159\n"
"MA v,32,v,192,a,176\n"
"s_add_u32 m0, s69, 17280\n"
"EX 152\n"
"MA v,32,v,208,a,180\n"
"buffer_load_dwordx4 v253, s[44:47], s26 offen offset:128 lds\n"
"MA v,36,v,212,a,180\n"
"EX 153\n"
"MA v,36,v,196,a,176\n"
"CV 170,152,153\n"
"MA v,40,v,200,a,176\n"
"EX 154\n"
"MA v,40,v,216,a,180\n"
"MA v,44,v,220,a,180\n"
"EX 155\n"
"MA v,44,v,204,a,176\n"
"s_waitcnt lgkmcnt(0)\n"
"RD 128,249,40960\n"
"CV 171,154,155\n"
"MA v,60,v,204,a,184\n"
"EX 148\n"
"MA v,60,v,220,a,188\n"
"RD 132,250,40960\n"
"MA v,56,v,216,a,188\n"
"EX 149\n"
"MA v,56,v,200,a,184\n"
"RD 136,249,41984\n"
"CV 166,148,149\n"
"MA v,52,v,196,a,184\n"
"EX 150\n"
"MA v,52,v,212,a,188\n"
"RD 140,250,41984\n"
"MA v,48,v,208,a,188\n"
"EX 151\n"
"MA v,48,v,192,a,184\n"
"CV 167,150,151\n"
"MA v,240,v,208,a,236\n"
"MA v,240,v,212,a,240\n"
"MA v,240,v,216,a,244\n"
"MA v,240,v,220,a,248\n"
"s_waitcnt lgkmcnt(0)\n"
"s_mov_b64 exec, 0\n"
"s_cmp_eq_u32 s64, 0\n"
"s_cbranch_scc1 L_ST21_%=\n"
"s_cmp_eq_u32 s64, 1\n"
"s_cbranch_scc1 L_ST22_%=\n"
"s_cmp_eq_u32 s64, 2\n"
"s_cbranch_scc1 L_ST23_%=\n"
"s_cmp_eq_u32 s64, 3\n"
"s_cbranch_scc1 L_VB5_%=\n"
"L_VB5_%=:\n"
"s_mov_b64 exec, -1\n"
"MC 128,128,64,224\n"
"EX 144\n"
"MA v,128,a,132,a,68\n"
"RD 144,249,45056\n"
"MA v,128,a,136,a,72\n"
"EX 145\n"
"MA v,128,a,140,a,76\n"
"RD 148,250,45056\n"
"CV 162,144,145\n"
"MC 132,140,92,228\n"
"EX 146\n"
"MA v,132,a,136,a,88\n"
"RD 152,249,46080\n"
"MA v,132,a,132,a,84\n"
"EX 147\n"
"MA v,132,a,128,a,80\n"
"RD 156,250,46080\n"
"CV 163,146,147\n"
"MC 136,128,96,232\n"
"s_add_u32 m0, s69, 106496\n"
"EX 128\n"
"MA v,136,a,132,a,100\n"
"buffer_load_dwordx4 v254, s[48:51], s25 offen nt lds\n"
"MA v,136,a,136,a,104\n"
"EX 129\n"
"MA v,136,a,140,a,108\n"
"TR 160,251,50176\n"
"CV 176,128,129\n"
"MC 140,140,124,236\n"
"EX 130\n"
"MA v,140,a,136,a,120\n"
"TR 162,251,54272\n"
"MA v,140,a,132,a,116\n"
"EX 131\n"
"MA v,140,a,128,a,112\n"
"s_waitcnt lgkmcnt(2)\n"
"TR 164,251,58368\n"
"CV 177,130,131\n"
"MC 156,144,112,236\n"
"EX 132\n"
"MA v,156,a,148,a,116\n"
"TR 166,251,62464\n"
"MA v,156,a,152,a,120\n"
"EX 133\n"
"MA v,156,a,156,a,124\n"
"TR 168,252,50176\n"
"CV 180,132,133\n"
"MC 152,156,108,232\n"
"s_add_u32 m0, s69, 107392\n"
"EX 134\n"
"MA v,152,a,152,a,104\n"
"buffer_load_dwordx4 v254, s[48:51], s25 offen offset:128 nt lds\n"
"MA v,152,a,148,a,100\n"
"s_add_u32 s24, s70, s71\n"
"EX 135\n"
"MA v,152,a,144,a,96\n"
"TR 170,252,54272\n"
"CV 181,134,135\n"
"MC 148,144,80,228\n"
"s_and_b32 s24, s24, 0x3ffff\n"
"EX 136\n"
"MA v,148,a,148,a,84\n"
"TR 172,252,58368\n"
"MA v,148,a,152,a,88\n"
"s_and_b32 s81, s70, 0xfffc0000\n"
"EX 137\n"
"MA v,148,a,156,a,92\n"
"TR 174,252,62464\n"
"CV 184,136,137\n"
"MC 144,156,76,224\n"
"EX 138\n"
"MA v,144,a,152,a,72\n"
"TR 176,251,50688\n"
"MA v,144,a,148,a,68\n"
"s_or_b32 s24, s24, s81\n"
"EX 139\n"
"MA v,144,a,144,a,64\n"
"s_waitcnt lgkmcnt(5)\n"
"TR 178,251,54784\n"
"CV 185,138,139\n"
"MA v,64,v,192,a,160\n"
"EX 140\n"
"MA v,64,v,208,a,164\n"
"TR 180,251,58880\n"
"MA v,68,v,212,a,164\n"
"s_add_u32 s70, s70, 8192\n"
"EX 141\n"
"MA v,68,v,196,a,160\n"
"TR 182,251,62976\n"
"CV 188,140,141\n"
"MA v,72,v,200,a,160\n"
"s_add_u32 m0, s69, 24576\n"
"EX 142\n"
"MA v,72,v,216,a,164\n"
"buffer_load_dwordx4 v253, s[44:47], s27 offen lds\n"
"MA v,76,v,220,a,164\n"
"EX 143\n"
"MA v,76,v,204,a,160\n"
"s_waitcnt lgkmcnt(4)\n"
"TR 184,252,50688\n"
"CV 189,142,143\n"
"MA v,92,v,204,a,168\n"
"EX 156\n"
"MA v,92,v,220,a,172\n"
"TR 186,252,54784\n"
"MA v,88,v,216,a,172\n"
"EX 157\n"
"MA v,88,v,200,a,168\n"
"L_ST15_%=:\n"
"s_cbranch_execz L_VB3_%=\n"
"CV 190,156,157\n"
"MA v,84,v,196,a,168\n"
"EX 158\n"
"MA v,84,v,212,a,172\n"
"TR 188,252,58880\n"
"MA v,80,v,208,a,172\n"
"EX 159\n"
"MA v,80,v,192,a,168\n"
"s_waitcnt lgkmcnt(3)\n"
"TR 190,252,62976\n"
"CV 191,158,159\n"
"MA v,96,v,192,a,176\n"
"s_add_u32 m0, s69, 25472\n"
"EX 152\n"
"MA v,96,v,208,a,180\n"
"s_memrealtime vcc\n"
"buffer_load_dwordx4 v253, s[44:47], s27 offen offset:128 lds\n"
"MA v,100,v,212,a,180\n"
"EX 153\n"
"MA v,100,v,196,a,176\n"
"CV 186,152,153\n"
"MA v,104,v,200,a,176\n"
"s_waitcnt lgkmcnt(0)\n"
"EX 154\n"
"MA v,104,v,216,a,180\n"
"s_waitcnt vmcnt(17)\n"
"MA v,108,v,220,a,180\n"
"s_barrier\n"
"EX 155\n"
"MA v,108,v,204,a,176\n"
"RD 128,249,49152\n"
"CV 187,154,155\n"
"MA v,124,v,204,a,184\n"
"s_sub_u32 s33, s72, vcc_lo\n"
"EX 148\n"
"MA v,124,v,220,a,188\n"
"s_add_u32 s72, vcc_lo, s77\n"
"RD 132,250,49152\n"
"MA v,120,v,216,a,188\n"
"s_max_i32 s33, s33, -150\n"
"EX 149\n"
"MA v,120,v,200,a,184\n"
"s_add_u32 s72, s72, s33\n"
"RD 136,249,50176\n"
"CV 182,148,149\n"
"MA v,116,v,196,a,184\n"
"s_cmp_gt_i32 s33, 0\n"
"EX 150\n"
"MA v,116,v,212,a,188\n"
"s_cbranch_scc1 L_PACE15_%=\n"
"L_PACEB15_%=:\n"
"RD 140,250,50176\n"
"MA v,112,v,208,a,188\n"
"EX 151\n"
"MA v,112,v,192,a,184\n"
"CV 183,150,151\n"
"MA v,240,v,160,a,236\n"
"MA v,240,v,164,a,240\n"
"MA v,240,v,168,a,244\n"
"MA v,240,v,172,a,248\n"
"s_waitcnt lgkmcnt(0)\n"
"MC 128,128,64,224\n"
"s_lshr_b32 s79, s20, 16\n"
"EX 144\n"
"MA v,128,a,132,a,68\n"
"RD 144,249,53248\n"
"MA v,128,a,136,a,72\n"
"s_and_b32 s83, s20, 0xffff\n"
"EX 145\n"
"MA v,128,a,140,a,76\n"
"RD 148,250,53248\n"
"CV 178,144,145\n"
"v_and_b32 v247, 32, v255\n"
"MC 132,140,92,228\n"
"EX 146\n"
"MA v,132,a,136,a,88\n"
"RD 152,249,54272\n"
"MA v,132,a,132,a,84\n"
"EX 147\n"
"v_and_b32 v246, 16, v255\n"
"MA v,132,a,128,a,80\n"
"RD 156,250,54272\n"
"CV 179,146,147\n"
"MC 136,128,96,232\n"
"s_add_u32 m0, s69, 114688\n"
"EX 128\n"
"MA v,136,a,132,a,100\n"
"buffer_load_dwordx4 v254, s[48:51], s26 offen nt lds\n"
"MA v,136,a,136,a,104\n"
"EX 129\n"
"v_lshl_or_b32 v246, v247, 3, v246\n"
"MA v,136,a,140,a,108\n"
"TR 160,251,0\n"
"CV 192,128,129\n"
"MC 140,140,124,236\n"
"EX 130\n"
"MA v,140,a,136,a,120\n"
"TR 162,251,4096\n"
"v_xor_b32 v246, s79, v246\n"
"MA v,140,a,132,a,116\n"
"EX 131\n"
"MA v,140,a,128,a,112\n"
"s_waitcnt lgkmcnt(2)\n"
"TR 164,251,8192\n"
"CV 193,130,131\n"
"MC 156,144,112,236\n"
"EX 132\n"
"MA v,156,a,148,a,116\n"
"TR 166,251,12288\n"
"v_and_b32 v248, 0x100, v255\n"
"MA v,156,a,152,a,120\n"
"EX 133\n"
"MA v,156,a,156,a,124\n"
"TR 168,252,0\n"
"CV 196,132,133\n"
"MC 152,156,108,232\n"
"s_add_u32 m0, s69, 115584\n"
"EX 134\n"
"MA v,152,a,152,a,104\n"
"buffer_load_dwordx4 v254, s[48:51], s26 offen offset:128 nt lds\n"
"v_bfe_u32 v247, v255, 1, 7\n"
"MA v,152,a,148,a,100\n"
"s_add_u32 s25, s70, s71\n"
"EX 135\n"
"MA v,152,a,144,a,96\n"
"TR 170,252,4096\n"
"CV 197,134,135\n"
"MC 148,144,80,228\n"
"s_and_b32 s25, s25, 0x3ffff\n"
"EX 136\n"
"v_cmp_gt_u32 vcc, 0x200, v255\n"
"MA v,148,a,148,a,84\n"
"TR 172,252,8192\n"
"MA v,148,a,152,a,88\n"
"s_and_b32 s81, s70, 0xfffc0000\n"
"EX 137\n"
"MA v,148,a,156,a,92\n"
"TR 174,252,12288\n"
"CV 200,136,137\n"
"MC 144,156,76,224\n"
"EX 138\n"
"v_lshl_or_b32 v247, v248, 1, v247\n"
"MA v,144,a,152,a,72\n"
"TR 176,251,512\n"
"MA v,144,a,148,a,68\n"
"s_or_b32 s25, s25, s81\n"
"EX 139\n"
"MA v,144,a,144,a,64\n"
"s_waitcnt lgkmcnt(5)\n"
"TR 178,251,4608\n"
"CV 201,138,139\n"
"v_xor_b32 v247, s83, v247\n"
"MA v,0,v,160,a,160\n"
"EX 140\n"
"MA v,0,v,176,a,164\n"
"TR 180,251,8704\n"
"MA v,4,v,180,a,164\n"
"s_add_u32 s70, s70, 8192\n"
"EX 141\n"
"MA v,4,v,164,a,160\n"
"TR 182,251,12800\n"
"CV 204,140,141\n"
"v_cndmask_b32 v248, v246, v247, vcc\n"
"MA v,8,v,168,a,160\n"
"s_add_u32 m0, s69, 32768\n"
"EX 142\n"
"MA v,8,v,184,a,164\n"
"buffer_load_dwordx4 v253, s[44:47], s24 offen lds\n"
"MA v,12,v,188,a,164\n"
"EX 143\n"
"MA v,12,v,172,a,160\n"
"s_waitcnt lgkmcnt(4)\n"
"TR 184,252,512\n"
"CV 205,142,143\n"
"MA v,28,v,172,a,168\n"
"EX 156\n"
"MA v,28,v,188,a,172\n"
"TR 186,252,4608\n"
"MA v,24,v,184,a,172\n"
"EX 157\n"
"MA v,24,v,168,a,168\n"
"CV 206,156,157\n"
"MA v,20,v,164,a,168\n"
"EX 158\n"
"MA v,20,v,180,a,172\n"
"TR 188,252,8704\n"
"MA v,16,v,176,a,172\n"
"EX 159\n"
"MA v,16,v,160,a,168\n"
"s_waitcnt lgkmcnt(3)\n"
"TR 190,252,12800\n"
"CV 207,158,159\n"
"MA v,32,v,160,a,176\n"
"s_add_u32 m0, s69, 33664\n"
"EX 152\n"
"MA v,32,v,176,a,180\n"
"buffer_load_dwordx4 v253, s[44:47], s24 offen offset:128 lds\n"
"MA v,36,v,180,a,180\n"
"EX 153\n"
"MA v,36,v,164,a,176\n"
"CV 202,152,153\n"
"MA v,40,v,168,a,176\n"
"EX 154\n"
"MA v,40,v,184,a,180\n"
"MA v,44,v,188,a,180\n"
"EX 155\n"
"MA v,44,v,172,a,176\n"
"s_waitcnt lgkmcnt(0)\n"
"RD 128,249,57344\n"
"CV 203,154,155\n"
"MA v,60,v,172,a,184\n"
"EX 148\n"
"MA v,60,v,188,a,188\n"
"RD 132,250,57344\n"
"MA v,56,v,184,a,188\n"
"EX 149\n"
"MA v,56,v,168,a,184\n"
"RD 136,249,58368\n"
"CV 198,148,149\n"
"MA v,52,v,164,a,184\n"
"EX 150\n"
"MA v,52,v,180,a,188\n"
"RD 140,250,58368\n"
"MA v,48,v,176,a,188\n"
"EX 151\n"
"MA v,48,v,160,a,184\n"
"CV 199,150,151\n"
"MA v,240,v,176,a,236\n"
"MA v,240,v,180,a,240\n"
"global_load_dwordx2 v[244:245], v248, s[18:19] sc1\n"
"MA v,240,v,184,a,244\n"
"MA v,240,v,188,a,248\n"
"s_waitcnt lgkmcnt(0)\n"
"s_add_u32 s85, s31, 0\n"
"s_add_u32 s79, s85, 8\n"
"s_cmp_eq_u32 s58, 0\n"
"s_cselect_b32 s54, 0, s79\n"
"MC 128,128,64,224\n"
"EX 144\n"
"MA v,128,a,132,a,68\n"
"RD 144,249,61440\n"
"MA v,128,a,136,a,72\n"
"EX 145\n"
"MA v,128,a,140,a,76\n"
"RD 148,250,61440\n"
"CV 194,144,145\n"
"MC 132,140,92,228\n"
"EX 146\n"
"MA v,132,a,136,a,88\n"
"RD 152,249,62464\n"
"MA v,132,a,132,a,84\n"
"EX 147\n"
"MA v,132,a,128,a,80\n"
"RD 156,250,62464\n"
"CV 195,146,147\n"
"MC 136,128,96,232\n"
"s_add_u32 m0, s69, 122880\n"
"EX 128\n"
"MA v,136,a,132,a,100\n"
"buffer_load_dwordx4 v254, s[48:51], s27 offen nt lds\n"
"MA v,136,a,136,a,104\n"
"EX 129\n"
"MA v,136,a,140,a,108\n"
"TR 160,251,1024\n"
"CV 208,128,129\n"
"MC 140,140,124,236\n"
"EX 130\n"
"MA v,140,a,136,a,120\n"
"TR 162,251,5120\n"
"MA v,140,a,132,a,116\n"
"EX 131\n"
"MA v,140,a,128,a,112\n"
"s_waitcnt lgkmcnt(2)\n"
"TR 164,251,9216\n"
"CV 209,130,131\n"
"MC 156,144,112,236\n"
"EX 132\n"
"MA v,156,a,148,a,116\n"
"TR 166,251,13312\n"
"MA v,156,a,152,a,120\n"
"EX 133\n"
"MA v,156,a,156,a,124\n"
"TR 168,252,1024\n"
"CV 212,132,133\n"
"MC 152,156,108,232\n"
"s_add_u32 m0, s69, 123776\n"
"EX 134\n"
"MA v,152,a,152,a,104\n"
"buffer_load_dwordx4 v254, s[48:51], s27 offen offset:128 nt lds\n"
"MA v,152,a,148,a,100\n"
"s_add_u32 s26, s70, s71\n"
"EX 135\n"
"MA v,152,a,144,a,96\n"
"TR 170,252,5120\n"
"CV 213,134,135\n"
"MC 148,144,80,228\n"
"s_and_b32 s26, s26, 0x3ffff\n"
"EX 136\n"
"MA v,148,a,148,a,84\n"
"TR 172,252,9216\n"
"MA v,148,a,152,a,88\n"
"s_and_b32 s81, s70, 0xfffc0000\n"
"EX 137\n"
"MA v,148,a,156,a,92\n"
"TR 174,252,13312\n"
"CV 216,136,137\n"
"MC 144,156,76,224\n"
"EX 138\n"
"MA v,144,a,152,a,72\n"
"TR 176,251,1536\n"
"MA v,144,a,148,a,68\n"
"s_or_b32 s26, s26, s81\n"
"EX 139\n"
"MA v,144,a,144,a,64\n"
"s_waitcnt lgkmcnt(5)\n"
"TR 178,251,5632\n"
"CV 217,138,139\n"
"MA v,64,v,160,a,160\n"
"EX 140\n"
"MA v,64,v,176,a,164\n"
"TR 180,251,9728\n"
"MA v,68,v,180,a,164\n"
"s_add_u32 s70, s70, 8192\n"
"EX 141\n"
"MA v,68,v,164,a,160\n"
"TR 182,251,13824\n"
"CV 220,140,141\n"
"MA v,72,v,168,a,160\n"
"s_add_u32 m0, s69, 40960\n"
"EX 142\n"
"MA v,72,v,184,a,164\n"
"buffer_load_dwordx4 v253, s[44:47], s25 offen lds\n"
"MA v,76,v,188,a,164\n"
"EX 143\n"
"MA v,76,v,172,a,160\n"
"s_waitcnt lgkmcnt(4)\n"
"TR 184,252,1536\n"
"CV 221,142,143\n"
"MA v,92,v,172,a,168\n"
"EX 156\n"
"MA v,92,v,188,a,172\n"
"TR 186,252,5632\n"
"MA v,88,v,184,a,172\n"
"EX 157\n"
"MA v,88,v,168,a,168\n"
"CV 222,156,157\n"
"MA v,84,v,164,a,168\n"
"EX 158\n"
"MA v,84,v,180,a,172\n"
"TR 188,252,9728\n"
"MA v,80,v,176,a,172\n"
"EX 159\n"
"MA v,80,v,160,a,168\n"
"s_waitcnt lgkmcnt(3)\n"
"TR 190,252,13824\n"
"CV 223,158,159\n"
"MA v,96,v,160,a,176\n"
"s_add_u32 m0, s69, 41856\n"
"EX 152\n"
"MA v,96,v,176,a,180\n"
"s_memrealtime vcc\n"
"buffer_load_dwordx4 v253, s[44:47], s25 offen offset:128 lds\n"
"MA v,100,v,180,a,180\n"
"EX 153\n"
"MA v,100,v,164,a,176\n"
"CV 218,152,153\n"
"MA v,104,v,168,a,176\n"
"s_waitcnt lgkmcnt(0)\n"
"EX 154\n"
"MA v,104,v,184,a,180\n"
"s_waitcnt vmcnt(17)\n"
"MA v,108,v,188,a,180\n"
"s_barrier\n"
"EX 155\n"
"MA v,108,v,172,a,176\n"
"RD 128,249,0\n"
"CV 219,154,155\n"
"MA v,124,v,172,a,184\n"
"s_sub_u32 s33, s72, vcc_lo\n"
"EX 148\n"
"MA v,124,v,188,a,188\n"
"s_add_u32 s72, vcc_lo, s77\n"
"RD 132,250,0\n"
"MA v,120,v,184,a,188\n"
"s_max_i32 s33, s33, -150\n"
"EX 149\n"
"MA v,120,v,168,a,184\n"
"s_add_u32 s72, s72, s33\n"
"RD 136,249,1024\n"
"CV 214,148,149\n"
"MA v,116,v,164,a,184\n"
"s_cmp_gt_i32 s33, 0\n"
"EX 150\n"
"MA v,116,v,180,a,188\n"
"s_cbranch_scc1 L_PACE16_%=\n"
"L_PACEB16_%=:\n"
"RD 140,250,1024\n"
"MA v,112,v,176,a,188\n"
"EX 151\n"
"MA v,112,v,160,a,184\n"
"CV 215,150,151\n"
"MA v,240,v,192,a,236\n"
"MA v,240,v,196,a,240\n"
"buffer_store_dwordx2 a[252:253], v255, s[52:55], s85 offen sc1\n"
"MA v,240,v,200,a,244\n"
"MA v,240,v,204,a,248\n"
"s_waitcnt lgkmcnt(0)\n"
"MC 128,128,64,224\n"
"EX 144\n"
"MA v,128,a,132,a,68\n"
"RD 144,249,4096\n"
"MA v,128,a,136,a,72\n"
"EX 145\n"
"MA v,128,a,140,a,76\n"
"RD 148,250,4096\n"
"CV 210,144,145\n"
"MC 132,140,92,228\n"
"EX 146\n"
"MA v,132,a,136,a,88\n"
"RD 152,249,5120\n"
"MA v,132,a,132,a,84\n"
"EX 147\n"
"MA v,132,a,128,a,80\n"
"RD 156,250,5120\n"
"CV 211,146,147\n"
"MC 136,128,96,232\n"
"s_add_u32 m0, s69, 65536\n"
"EX 128\n"
"MA v,136,a,132,a,100\n"
"buffer_load_dwordx4 v254, s[48:51], s24 offen nt lds\n"
"MA v,136,a,136,a,104\n"
"EX 129\n"
"MA v,136,a,140,a,108\n"
"TR 160,251,16384\n"
"CV 160,128,129\n"
"MC 140,140,124,236\n"
"EX 130\n"
"MA v,140,a,136,a,120\n"
"TR 162,251,20480\n"
"MA v,140,a,132,a,116\n"
"EX 131\n"
"MA v,140,a,128,a,112\n"
"s_waitcnt lgkmcnt(2)\n"
"TR 164,251,24576\n"
"CV 161,130,131\n"
"MC 156,144,112,236\n"
"EX 132\n"
"MA v,156,a,148,a,116\n"
"TR 166,251,28672\n"
"MA v,156,a,152,a,120\n"
"EX 133\n"
"MA v,156,a,156,a,124\n"
"TR 168,252,16384\n"
"CV 164,132,133\n"
"MC 152,156,108,232\n"
"s_add_u32 m0, s69, 66432\n"
"EX 134\n"
"MA v,152,a,152,a,104\n"
"buffer_load_dwordx4 v254, s[48:51], s24 offen offset:128 nt lds\n"
"MA v,152,a,148,a,100\n"
"s_add_u32 s27, s70, s71\n"
"EX 135\n"
"MA v,152,a,144,a,96\n"
"TR 170,252,20480\n"
"CV 165,134,135\n"
"MC 148,144,80,228\n"
"s_and_b32 s27, s27, 0x3ffff\n"
"EX 136\n"
"MA v,148,a,148,a,84\n"
"TR 172,252,24576\n"
"MA v,148,a,152,a,88\n"
"s_and_b32 s81, s70, 0xfffc0000\n"
"EX 137\n"
"MA v,148,a,156,a,92\n"
"TR 174,252,28672\n"
"CV 168,136,137\n"
"MC 144,156,76,224\n"
"EX 138\n"
"MA v,144,a,152,a,72\n"
"TR 176,251,16896\n"
"MA v,144,a,148,a,68\n"
"s_or_b32 s27, s27, s81\n"
"EX 139\n"
"MA v,144,a,144,a,64\n"
"s_waitcnt lgkmcnt(5)\n"
"TR 178,251,20992\n"
"CV 169,138,139\n"
"MA v,0,v,192,a,160\n"
"EX 140\n"
"MA v,0,v,208,a,164\n"
"TR 180,251,25088\n"
"MA v,4,v,212,a,164\n"
"s_add_u32 s70, s70, 8192\n"
"EX 141\n"
"MA v,4,v,196,a,160\n"
"TR 182,251,29184\n"
"CV 172,140,141\n"
"MA v,8,v,200,a,160\n"
"s_add_u32 m0, s69, 49152\n"
"EX 142\n"
"MA v,8,v,216,a,164\n"
"buffer_load_dwordx4 v253, s[44:47], s26 offen lds\n"
"MA v,12,v,220,a,164\n"
"EX 143\n"
"MA v,12,v,204,a,160\n"
"s_waitcnt lgkmcnt(4)\n"
"TR 184,252,16896\n"
"CV 173,142,143\n"
"MA v,28,v,204,a,168\n"
"EX 156\n"
"MA v,28,v,220,a,172\n"
"TR 186,252,20992\n"
"MA v,24,v,216,a,172\n"
"EX 157\n"
"MA v,24,v,200,a,168\n"
"CV 174,156,157\n"
"MA v,20,v,196,a,168\n"
"EX 158\n"
"MA v,20,v,212,a,172\n"
"TR 188,252,25088\n"
"MA v,16,v,208,a,172\n"
"EX 159\n"
"MA v,16,v,192,a,168\n"
"s_waitcnt lgkmcnt(3)\n"
"TR 190,252,29184\n"
"CV 175,158,159\n"
"MA v,32,v,192,a,176\n"
"s_add_u32 m0, s69, 50048\n"
"EX 152\n"
"MA v,32,v,208,a,180\n"
"buffer_load_dwordx4 v253, s[44:47], s26 offen offset:128 lds\n"
"MA v,36,v,212,a,180\n"
"EX 153\n"
"MA v,36,v,196,a,176\n"
"CV 170,152,153\n"
"MA v,40,v,200,a,176\n"
"EX 154\n"
"MA v,40,v,216,a,180\n"
"MA v,44,v,220,a,180\n"
"EX 155\n"
"MA v,44,v,204,a,176\n"
"s_waitcnt lgkmcnt(0)\n"
"RD 128,249,8192\n"
"CV 171,154,155\n"
"MA v,60,v,204,a,184\n"
"EX 148\n"
"MA v,60,v,220,a,188\n"
"RD 132,250,8192\n"
"MA v,56,v,216,a,188\n"
"EX 149\n"
"MA v,56,v,200,a,184\n"
"RD 136,249,9216\n"
"CV 166,148,149\n"
"MA v,52,v,196,a,184\n"
"EX 150\n"
"MA v,52,v,212,a,188\n"
"RD 140,250,9216\n"
"MA v,48,v,208,a,188\n"
"EX 151\n"
"MA v,48,v,192,a,184\n"
"CV 167,150,151\n"
"MA v,240,v,208,a,236\n"
"MA v,240,v,212,a,240\n"
"global_load_dword a255, v255, s[34:35]\n"
"MA v,240,v,216,a,244\n"
"MA v,240,v,220,a,248\n"
"s_waitcnt lgkmcnt(0)\n"
"MC 128,128,64,224\n"
"EX 144\n"
"MA v,128,a,132,a,68\n"
"RD 144,249,12288\n"
"MA v,128,a,136,a,72\n"
"EX 145\n"
"MA v,128,a,140,a,76\n"
"RD 148,250,12288\n"
"CV 162,144,145\n"
"MC 132,140,92,228\n"
"EX 146\n"
"MA v,132,a,136,a,88\n"
"RD 152,249,13312\n"
"MA v,132,a,132,a,84\n"
"EX 147\n"
"MA v,132,a,128,a,80\n"
"RD 156,250,13312\n"
"CV 163,146,147\n"
"MC 136,128,96,232\n"
"s_add_u32 m0, s69, 73728\n"
"EX 128\n"
"MA v,136,a,132,a,100\n"
"buffer_load_dwordx4 v254, s[48:51], s25 offen nt lds\n"
"MA v,136,a,136,a,104\n"
"EX 129\n"
"MA v,136,a,140,a,108\n"
"TR 160,251,17408\n"
"CV 176,128,129\n"
"MC 140,140,124,236\n"
"L_ST16_%=:\n"
"s_cbranch_execz L_VB3_%=\n"
"EX 130\n"
"MA v,140,a,136,a,120\n"
"TR 162,251,21504\n"
"MA v,140,a,132,a,116\n"
"EX 131\n"
"MA v,140,a,128,a,112\n"
"s_waitcnt lgkmcnt(2)\n"
"TR 164,251,25600\n"
"CV 177,130,131\n"
"MC 156,144,112,236\n"
"EX 132\n"
"MA v,156,a,148,a,116\n"
"TR 166,251,29696\n"
"MA v,156,a,152,a,120\n"
"EX 133\n"
"MA v,156,a,156,a,124\n"
"TR 168,252,17408\n"
"CV 180,132,133\n"
"MC 152,156,108,232\n"
"s_add_u32 m0, s69, 74624\n"
"EX 134\n"
"MA v,152,a,152,a,104\n"
"buffer_load_dwordx4 v254, s[48:51], s25 offen offset:128 nt lds\n"
"MA v,152,a,148,a,100\n"
"EX 135\n"
"MA v,152,a,144,a,96\n"
"TR 170,252,21504\n"
"CV 181,134,135\n"
"MC 148,144,80,228\n"
"EX 136\n"
"MA v,148,a,148,a,84\n"
"TR 172,252,25600\n"
"MA v,148,a,152,a,88\n"
"EX 137\n"
"MA v,148,a,156,a,92\n"
"TR 174,252,29696\n"
"CV 184,136,137\n"
"MC 144,156,76,224\n"
"EX 138\n"
"MA v,144,a,152,a,72\n"
"TR 176,251,17920\n"
"MA v,144,a,148,a,68\n"
"EX 139\n"
"MA v,144,a,144,a,64\n"
"s_waitcnt lgkmcnt(5)\n"
"TR 178,251,22016\n"
"CV 185,138,139\n"
"MA v,64,v,192,a,160\n"
"EX 140\n"
"MA v,64,v,208,a,164\n"
"TR 180,251,26112\n"
"MA v,68,v,212,a,164\n"
"EX 141\n"
"MA v,68,v,196,a,160\n"
"TR 182,251,30208\n"
"CV 188,140,141\n"
"MA v,72,v,200,a,160\n"
"s_add_u32 m0, s69, 57344\n"
"EX 142\n"
"MA v,72,v,216,a,164\n"
"buffer_load_dwordx4 v253, s[44:47], s27 offen lds\n"
"MA v,76,v,220,a,164\n"
"EX 143\n"
"MA v,76,v,204,a,160\n"
"s_waitcnt lgkmcnt(4)\n"
"TR 184,252,17920\n"
"CV 189,142,143\n"
"MA v,92,v,204,a,168\n"
"EX 156\n"
"MA v,92,v,220,a,172\n"
"TR 186,252,22016\n"
"MA v,88,v,216,a,172\n"
"EX 157\n"
"MA v,88,v,200,a,168\n"
"CV 190,156,157\n"
"MA v,84,v,196,a,168\n"
"EX 158\n"
"MA v,84,v,212,a,172\n"
"TR 188,252,26112\n"
"MA v,80,v,208,a,172\n"
"EX 159\n"
"MA v,80,v,192,a,168\n"
"s_waitcnt lgkmcnt(3)\n"
"TR 190,252,30208\n"
"CV 191,158,159\n"
"MA v,96,v,192,a,176\n"
"s_add_u32 m0, s69, 58240\n"
"EX 152\n"
"MA v,96,v,208,a,180\n"
"buffer_load_dwordx4 v253, s[44:47], s27 offen offset:128 lds\n"
"MA v,100,v,212,a,180\n"
"s_waitcnt vmcnt(14)\n"
"EX 153\n"
"v_cmp_ne_u64 s[82:83], s[28:29], v[244:245]\n"
"MA v,100,v,196,a,176\n"
"CV 186,152,153\n"
"MA v,104,v,200,a,176\n"
"s_waitcnt lgkmcnt(0)\n"
"EX 154\n"
"MA v,104,v,216,a,180\n"
"s_waitcnt vmcnt(63)\n"
"MA v,108,v,220,a,180\n"
"s_barrier\n"
"EX 155\n"
"MA v,108,v,204,a,176\n"
"RD 128,249,16384\n"
"CV 187,154,155\n"
"MA v,124,v,204,a,184\n"
"EX 148\n"
"MA v,124,v,220,a,188\n"
"RD 132,250,16384\n"
"MA v,120,v,216,a,188\n"
"EX 149\n"
"MA v,120,v,200,a,184\n"
"RD 136,249,17408\n"
"CV 182,148,149\n"
"MA v,116,v,196,a,184\n"
"EX 150\n"
"MA v,116,v,212,a,188\n"
"RD 140,250,17408\n"
"MA v,112,v,208,a,188\n"
"EX 151\n"
"MA v,112,v,192,a,184\n"
"CV 183,150,151\n"
"MA v,240,v,160,a,236\n"
"MA v,240,v,164,a,240\n"
"MA v,240,v,168,a,244\n"
"MA v,240,v,172,a,248\n"
"s_waitcnt lgkmcnt(0)\n"
"s_cmp_eq_u32 s82, 0\n"
"s_cselect_b32 s38, 147456, 0\n"
"MC 128,128,64,224\n"
"s_lshr_b32 s79, s20, 16\n"
"EX 144\n"
"MA v,128,a,132,a,68\n"
"RD 144,249,20480\n"
"MA v,128,a,136,a,72\n"
"s_xor_b32 s79, s79, 256\n"
"EX 145\n"
"MA v,128,a,140,a,76\n"
"RD 148,250,20480\n"
"CV 178,144,145\n"
"MC 132,140,92,228\n"
"s_lshl_b32 s79, s79, 11\n"
"EX 146\n"
"MA v,132,a,136,a,88\n"
"RD 152,249,21504\n"
"MA v,132,a,132,a,84\n"
"s_sub_u32 s79, s79, 15728640\n"
"EX 147\n"
"MA v,132,a,128,a,80\n"
"RD 156,250,21504\n"
"CV 179,146,147\n"
"MC 136,128,96,232\n"
"s_add_u32 m0, s69, 81920\n"
"EX 128\n"
"MA v,136,a,132,a,100\n"
"buffer_load_dwordx4 v254, s[48:51], s26 offen nt lds\n"
"MA v,136,a,136,a,104\n"
"EX 129\n"
"MA v,136,a,140,a,108\n"
"TR 160,251,32768\n"
"CV 192,128,129\n"
"MC 140,140,124,236\n"
"EX 130\n"
"MA v,140,a,136,a,120\n"
"TR 162,251,36864\n"
"MA v,140,a,132,a,116\n"
"EX 131\n"
"MA v,140,a,128,a,112\n"
"s_waitcnt lgkmcnt(2)\n"
"TR 164,251,40960\n"
"CV 193,130,131\n"
"MC 156,144,112,236\n"
"EX 132\n"
"MA v,156,a,148,a,116\n"
"TR 166,251,45056\n"
"MA v,156,a,152,a,120\n"
"EX 133\n"
"MA v,156,a,156,a,124\n"
"TR 168,252,32768\n"
"CV 196,132,133\n"
"MC 152,156,108,232\n"
"s_add_u32 m0, s69, 82816\n"
"EX 134\n"
"MA v,152,a,152,a,104\n"
"buffer_load_dwordx4 v254, s[48:51], s26 offen offset:128 nt lds\n"
"MA v,152,a,148,a,100\n"
"EX 135\n"
"MA v,152,a,144,a,96\n"
"TR 170,252,36864\n"
"CV 197,134,135\n"
"MC 148,144,80,228\n"
"EX 136\n"
"MA v,148,a,148,a,84\n"
"TR 172,252,40960\n"
"MA v,148,a,152,a,88\n"
"EX 137\n"
"MA v,148,a,156,a,92\n"
"TR 174,252,45056\n"
"CV 200,136,137\n"
"MC 144,156,76,224\n"
"EX 138\n"
"MA v,144,a,152,a,72\n"
"TR 176,251,33280\n"
"MA v,144,a,148,a,68\n"
"EX 139\n"
"MA v,144,a,144,a,64\n"
"s_waitcnt lgkmcnt(5)\n"
"TR 178,251,37376\n"
"CV 201,138,139\n"
"MA v,0,v,160,a,160\n"
"EX 140\n"
"MA v,0,v,176,a,164\n"
"TR 180,251,41472\n"
"MA v,4,v,180,a,164\n"
"EX 141\n"
"MA v,4,v,164,a,160\n"
"s_add_u32 m0, s69, 0\n"
"TR 182,251,45568\n"
"CV 204,140,141\n"
"MA v,8,v,168,a,160\n"
"s_mov_b32 s80, 91136\n"
"EX 142\n"
"MA v,8,v,184,a,164\n"
"buffer_load_dwordx4 v255, s[36:39], s80 offen sc1 lds\n"
"MA v,12,v,188,a,164\n"
"EX 143\n"
"MA v,12,v,172,a,160\n"
"s_waitcnt lgkmcnt(4)\n"
"TR 184,252,33280\n"
"CV 205,142,143\n"
"MA v,28,v,172,a,168\n"
"EX 156\n"
"MA v,28,v,188,a,172\n"
"s_add_u32 m0, s69, 132096\n"
"TR 186,252,37376\n"
"MA v,24,v,184,a,172\n"
"s_mov_b32 s80, 19456\n"
"EX 157\n"
"MA v,24,v,168,a,168\n"
"buffer_load_dwordx4 v255, s[36:39], s80 offen sc1 lds\n"
"CV 206,156,157\n"
"MA v,20,v,164,a,168\n"
"EX 158\n"
"MA v,20,v,180,a,172\n"
"TR 188,252,41472\n"
"MA v,16,v,176,a,172\n"
"EX 159\n"
"MA v,16,v,160,a,168\n"
"s_waitcnt lgkmcnt(3)\n"
"s_add_u32 m0, s69, 1024\n"
"TR 190,252,45568\n"
"CV 207,158,159\n"
"MA v,32,v,160,a,176\n"
"s_mov_b32 s80, 18432\n"
"EX 152\n"
"MA v,32,v,176,a,180\n"
"buffer_load_dwordx4 v255, s[36:39], s80 offen sc1 lds\n"
"MA v,36,v,180,a,180\n"
"EX 153\n"
"MA v,36,v,164,a,176\n"
"CV 202,152,153\n"
"MA v,40,v,168,a,176\n"
"EX 154\n"
"MA v,40,v,184,a,180\n"
"MA v,44,v,188,a,180\n"
"EX 155\n"
"MA v,44,v,172,a,176\n"
"s_waitcnt lgkmcnt(0)\n"
"RD 128,249,24576\n"
"CV 203,154,155\n"
"MA v,60,v,172,a,184\n"
"EX 148\n"
"MA v,60,v,188,a,188\n"
"RD 132,250,24576\n"
"MA v,56,v,184,a,188\n"
"EX 149\n"
"MA v,56,v,168,a,184\n"
"RD 136,249,25600\n"
"CV 198,148,149\n"
"MA v,52,v,164,a,184\n"
"EX 150\n"
"MA v,52,v,180,a,188\n"
"RD 140,250,25600\n"
"MA v,48,v,176,a,188\n"
"EX 151\n"
"MA v,48,v,160,a,184\n"
"s_add_u32 m0, s69, 131072\n"
"CV 199,150,151\n"
"MA v,240,v,176,a,236\n"
"s_mov_b32 s80, 27648\n"
"MA v,240,v,180,a,240\n"
"buffer_load_dwordx4 v255, s[36:39], s80 offen sc1 lds\n"
"MA v,240,v,184,a,244\n"
"MA v,240,v,188,a,248\n"
"s_waitcnt lgkmcnt(0)\n"
"s_mov_b64 s[44:45], s[18:19]\n"
"s_add_u32 s44, s44, s79\n"
"s_addc_u32 s45, s45, 0\n"
"s_and_b32 s45, s45, 0xffff\n"
"s_mov_b32 s46, 0\n"
"s_mov_b32 s47, 0x20000\n"
"MC 128,128,64,224\n"
"EX 144\n"
"MA v,128,a,132,a,68\n"
"RD 144,249,28672\n"
"MA v,128,a,136,a,72\n"
"EX 145\n"
"MA v,128,a,140,a,76\n"
"RD 148,250,28672\n"
"CV 194,144,145\n"
"MC 132,140,92,228\n"
"EX 146\n"
"MA v,132,a,136,a,88\n"
"RD 152,249,29696\n"
"MA v,132,a,132,a,84\n"
"EX 147\n"
"MA v,132,a,128,a,80\n"
"RD 156,250,29696\n"
"CV 195,146,147\n"
"MC 136,128,96,232\n"
"s_add_u32 m0, s69, 90112\n"
"EX 128\n"
"MA v,136,a,132,a,100\n"
"buffer_load_dwordx4 v254, s[48:51], s27 offen nt lds\n"
"MA v,136,a,136,a,104\n"
"EX 129\n"
"MA v,136,a,140,a,108\n"
"TR 160,251,33792\n"
"CV 208,128,129\n"
"MC 140,140,124,236\n"
"EX 130\n"
"MA v,140,a,136,a,120\n"
"TR 162,251,37888\n"
"MA v,140,a,132,a,116\n"
"EX 131\n"
"MA v,140,a,128,a,112\n"
"s_waitcnt lgkmcnt(2)\n"
"TR 164,251,41984\n"
"CV 209,130,131\n"
"MC 156,144,112,236\n"
"EX 132\n"
"MA v,156,a,148,a,116\n"
"TR 166,251,46080\n"
"MA v,156,a,152,a,120\n"
"EX 133\n"
"MA v,156,a,156,a,124\n"
"TR 168,252,33792\n"
"CV 212,132,133\n"
"MC 152,156,108,232\n"
"s_add_u32 m0, s69, 91008\n"
"EX 134\n"
"MA v,152,a,152,a,104\n"
"buffer_load_dwordx4 v254, s[48:51], s27 offen offset:128 nt lds\n"
"MA v,152,a,148,a,100\n"
"EX 135\n"
"MA v,152,a,144,a,96\n"
"TR 170,252,37888\n"
"CV 213,134,135\n"
"MC 148,144,80,228\n"
"EX 136\n"
"MA v,148,a,148,a,84\n"
"TR 172,252,41984\n"
"MA v,148,a,152,a,88\n"
"EX 137\n"
"MA v,148,a,156,a,92\n"
"TR 174,252,46080\n"
"CV 216,136,137\n"
"MC 144,156,76,224\n"
"EX 138\n"
"MA v,144,a,152,a,72\n"
"TR 176,251,34304\n"
"MA v,144,a,148,a,68\n"
"EX 139\n"
"MA v,144,a,144,a,64\n"
"s_waitcnt lgkmcnt(5)\n"
"TR 178,251,38400\n"
"CV 217,138,139\n"
"MA v,64,v,160,a,160\n"
"EX 140\n"
"MA v,64,v,176,a,164\n"
"TR 180,251,42496\n"
"MA v,68,v,180,a,164\n"
"EX 141\n"
"MA v,68,v,164,a,160\n"
"s_add_u32 m0, s69, 8192\n"
"TR 182,251,46592\n"
"CV 220,140,141\n"
"MA v,72,v,168,a,160\n"
"s_mov_b32 s80, 28672\n"
"EX 142\n"
"MA v,72,v,184,a,164\n"
"buffer_load_dwordx4 v255, s[36:39], s80 offen sc1 lds\n"
"MA v,76,v,188,a,164\n"
"EX 143\n"
"MA v,76,v,172,a,160\n"
"s_waitcnt lgkmcnt(4)\n"
"TR 184,252,34304\n"
"CV 221,142,143\n"
"MA v,92,v,172,a,168\n"
"EX 156\n"
"MA v,92,v,188,a,172\n"
"s_add_u32 m0, s69, 140288\n"
"TR 186,252,38400\n"
"MA v,88,v,184,a,172\n"
"s_mov_b32 s80, 21504\n"
"EX 157\n"
"MA v,88,v,168,a,168\n"
"buffer_load_dwordx4 v255, s[36:39], s80 offen sc1 lds\n"
"CV 222,156,157\n"
"MA v,84,v,164,a,168\n"
"EX 158\n"
"MA v,84,v,180,a,172\n"
"TR 188,252,42496\n"
"MA v,80,v,176,a,172\n"
"EX 159\n"
"MA v,80,v,160,a,168\n"
"s_waitcnt lgkmcnt(3)\n"
"s_add_u32 m0, s69, 9216\n"
"TR 190,252,46592\n"
"CV 223,158,159\n"
"MA v,96,v,160,a,176\n"
"s_mov_b32 s80, 20480\n"
"EX 152\n"
"MA v,96,v,176,a,180\n"
"buffer_load_dwordx4 v255, s[36:39], s80 offen sc1 lds\n"
"MA v,100,v,180,a,180\n"
"EX 153\n"
"MA v,100,v,164,a,176\n"
"CV 218,152,153\n"
"MA v,104,v,168,a,176\n"
"s_waitcnt lgkmcnt(0)\n"
"EX 154\n"
"MA v,104,v,184,a,180\n"
"s_waitcnt vmcnt(21)\n"
"MA v,108,v,188,a,180\n"
"s_barrier\n"
"EX 155\n"
"MA v,108,v,172,a,176\n"
"RD 128,249,32768\n"
"CV 219,154,155\n"
"MA v,124,v,172,a,184\n"
"EX 148\n"
"MA v,124,v,188,a,188\n"
"RD 132,250,32768\n"
"MA v,120,v,184,a,188\n"
"EX 149\n"
"MA v,120,v,168,a,184\n"
"RD 136,249,33792\n"
"CV 214,148,149\n"
"MA v,116,v,164,a,184\n"
"EX 150\n"
"MA v,116,v,180,a,188\n"
"RD 140,250,33792\n"
"MA v,112,v,176,a,188\n"
"EX 151\n"
"MA v,112,v,160,a,184\n"
"s_add_u32 m0, s69, 139264\n"
"CV 215,150,151\n"
"MA v,240,v,192,a,236\n"
"s_mov_b32 s80, 29696\n"
"MA v,240,v,196,a,240\n"
"buffer_load_dwordx4 v255, s[36:39], s80 offen sc1 lds\n"
"MA v,240,v,200,a,244\n"
"MA v,240,v,204,a,248\n"
"s_waitcnt lgkmcnt(0)\n"
"MC 128,128,64,224\n"
"EX 144\n"
"MA v,128,a,132,a,68\n"
"RD 144,249,36864\n"
"MA v,128,a,136,a,72\n"
"s_add_u32 m0, s69, 16384\n"
"EX 145\n"
"MA v,128,a,140,a,76\n"
"s_mov_b32 s80, 30720\n"
"RD 148,250,36864\n"
"CV 210,144,145\n"
"MC 132,140,92,228\n"
"buffer_load_dwordx4 v255, s[36:39], s80 offen sc1 lds\n"
"EX 146\n"
"MA v,132,a,136,a,88\n"
"RD 152,249,37888\n"
"MA v,132,a,132,a,84\n"
"EX 147\n"
"MA v,132,a,128,a,80\n"
"s_add_u32 m0, s69, 17408\n"
"RD 156,250,37888\n"
"CV 211,146,147\n"
"MC 136,128,96,232\n"
"s_mov_b32 s80, 22528\n"
"EX 128\n"
"MA v,136,a,132,a,100\n"
"buffer_load_dwordx4 v255, s[36:39], s80 offen sc1 lds\n"
"MA v,136,a,136,a,104\n"
"EX 129\n"
"MA v,136,a,140,a,108\n"
"TR 160,251,49152\n"
"CV 160,128,129\n"
"MC 140,140,124,236\n"
"s_add_u32 m0, s69, 98304\n"
"EX 130\n"
"MA v,140,a,136,a,120\n"
"s_mov_b32 s80, 31744\n"
"TR 162,251,53248\n"
"MA v,140,a,132,a,116\n"
"buffer_load_dwordx4 v255, s[36:39], s80 offen sc1 lds\n"
"EX 131\n"
"MA v,140,a,128,a,112\n"
"s_waitcnt lgkmcnt(2)\n"
"TR 164,251,57344\n"
"CV 161,130,131\n"
"MC 156,144,112,236\n"
"EX 132\n"
"MA v,156,a,148,a,116\n"
"TR 166,251,61440\n"
"MA v,156,a,152,a,120\n"
"EX 133\n"
"MA v,156,a,156,a,124\n"
"s_add_u32 m0, s69, 99328\n"
"TR 168,252,49152\n"
"CV 164,132,133\n"
"MC 152,156,108,232\n"
"s_mov_b32 s80, 23552\n"
"EX 134\n"
"MA v,152,a,152,a,104\n"
"buffer_load_dwordx4 v255, s[36:39], s80 offen sc1 lds\n"
"MA v,152,a,148,a,100\n"
"EX 135\n"
"MA v,152,a,144,a,96\n"
"TR 170,252,53248\n"
"CV 165,134,135\n"
"MC 148,144,80,228\n"
"s_add_u32 m0, s69, 147456\n"
"EX 136\n"
"MA v,148,a,148,a,84\n"
"s_mov_b32 s80, 32768\n"
"TR 172,252,57344\n"
"MA v,148,a,152,a,88\n"
"buffer_load_dwordx4 v255, s[36:39], s80 offen sc1 lds\n"
"EX 137\n"
"MA v,148,a,156,a,92\n"
"TR 174,252,61440\n"
"CV 168,136,137\n"
"MC 144,156,76,224\n"
"EX 138\n"
"MA v,144,a,152,a,72\n"
"TR 176,251,49664\n"
"MA v,144,a,148,a,68\n"
"s_add_u32 m0, s69, 148480\n"
"EX 139\n"
"MA v,144,a,144,a,64\n"
"s_waitcnt lgkmcnt(5)\n"
"s_mov_b32 s80, 24576\n"
"TR 178,251,53760\n"
"CV 169,138,139\n"
"MA v,0,v,192,a,160\n"
"buffer_load_dwordx4 v255, s[36:39], s80 offen sc1 lds\n"
"EX 140\n"
"MA v,0,v,208,a,164\n"
"TR 180,251,57856\n"
"MA v,4,v,212,a,164\n"
"EX 141\n"
"MA v,4,v,196,a,160\n"
"s_add_u32 m0, s69, 155648\n"
"TR 182,251,61952\n"
"CV 172,140,141\n"
"MA v,8,v,200,a,160\n"
"s_mov_b32 s80, 33792\n"
"EX 142\n"
"MA v,8,v,216,a,164\n"
"buffer_load_dwordx4 v255, s[36:39], s80 offen sc1 lds\n"
"MA v,12,v,220,a,164\n"
"EX 143\n"
"MA v,12,v,204,a,160\n"
"s_waitcnt lgkmcnt(4)\n"
"TR 184,252,49664\n"
"CV 173,142,143\n"
"MA v,28,v,204,a,168\n"
"EX 156\n"
"MA v,28,v,220,a,172\n"
"TR 186,252,53760\n"
"MA v,24,v,216,a,172\n"
"s_mov_b32 s80, 0\n"
"EX 157\n"
"MA v,24,v,200,a,168\n"
"L_ST17_%=:\n"
"s_cbranch_execz L_VB4_%=\n"
"LG 192,36\n"
"CV 174,156,157\n"
"MA v,20,v,196,a,168\n"
"EX 158\n"
"MA v,20,v,212,a,172\n"
"TR 188,252,57856\n"
"MA v,16,v,208,a,172\n"
"EX 159\n"
"MA v,16,v,192,a,168\n"
"s_waitcnt lgkmcnt(3)\n"
"TR 190,252,61952\n"
"CV 175,158,159\n"
"MA v,32,v,192,a,176\n"
"s_mov_b32 s80, 9216\n"
"EX 152\n"
"MA v,32,v,208,a,180\n"
"LG 196,36\n"
"MA v,36,v,212,a,180\n"
"EX 153\n"
"MA v,36,v,196,a,176\n"
"CV 170,152,153\n"
"MA v,40,v,200,a,176\n"
"s_mov_b32 s80, 1024\n"
"EX 154\n"
"MA v,40,v,216,a,180\n"
"LG 200,36\n"
"MA v,44,v,220,a,180\n"
"EX 155\n"
"MA v,44,v,204,a,176\n"
"s_waitcnt lgkmcnt(0)\n"
"RD 128,249,40960\n"
"CV 171,154,155\n"
"MA v,60,v,204,a,184\n"
"EX 148\n"
"MA v,60,v,220,a,188\n"
"s_mov_b32 s80, 10240\n"
"RD 132,250,40960\n"
"MA v,56,v,216,a,188\n"
"LG 204,36\n"
"EX 149\n"
"MA v,56,v,200,a,184\n"
"RD 136,249,41984\n"
"CV 166,148,149\n"
"MA v,52,v,196,a,184\n"
"EX 150\n"
"MA v,52,v,212,a,188\n"
"RD 140,250,41984\n"
"MA v,48,v,208,a,188\n"
"s_mov_b32 s80, 2048\n"
"EX 151\n"
"MA v,48,v,192,a,184\n"
"LG 208,36\n"
"CV 167,150,151\n"
"MA v,240,v,208,a,236\n"
"MA v,240,v,212,a,240\n"
"MA v,240,v,216,a,244\n"
"MA v,240,v,220,a,248\n"
"s_waitcnt lgkmcnt(0)\n"
"s_mov_b64 s[48:49], s[18:19]\n"
"s_add_u32 s48, s48, 52428800\n"
"s_addc_u32 s49, s49, 0\n"
"s_and_b32 s49, s49, 0xffff\n"
"s_mov_b32 s50, 9437184\n"
"s_mov_b32 s51, 0x20000\n"
"MC 128,128,64,224\n"
"global_load_dwordx2 v[244:245], v248, s[18:19] sc1\n"
"EX 144\n"
"MA v,128,a,132,a,68\n"
"RD 144,249,45056\n"
"MA v,128,a,136,a,72\n"
"s_add_u32 m0, s69, 24576\n"
"EX 145\n"
"MA v,128,a,140,a,76\n"
"s_mov_b32 s80, 25600\n"
"RD 148,250,45056\n"
"CV 162,144,145\n"
"MC 132,140,92,228\n"
"buffer_load_dwordx4 v255, s[36:39], s80 offen sc1 lds\n"
"EX 146\n"
"MA v,132,a,136,a,88\n"
"RD 152,249,46080\n"
"MA v,132,a,132,a,84\n"
"EX 147\n"
"MA v,132,a,128,a,80\n"
"s_add_u32 m0, s69, 25600\n"
"RD 156,250,46080\n"
"CV 163,146,147\n"
"MC 136,128,96,232\n"
"s_mov_b32 s80, 34816\n"
"EX 128\n"
"MA v,136,a,132,a,100\n"
"buffer_load_dwordx4 v255, s[36:39], s80 offen sc1 lds\n"
"MA v,136,a,136,a,104\n"
"EX 129\n"
"MA v,136,a,140,a,108\n"
"TR 160,251,50176\n"
"CV 176,128,129\n"
"MC 140,140,124,236\n"
"EX 130\n"
"MA v,140,a,136,a,120\n"
"TR 162,251,54272\n"
"MA v,140,a,132,a,116\n"
"s_add_u32 m0, s69, 106496\n"
"EX 131\n"
"MA v,140,a,128,a,112\n"
"s_waitcnt lgkmcnt(2)\n"
"s_mov_b32 s80, 26624\n"
"TR 164,251,58368\n"
"CV 177,130,131\n"
"MC 156,144,112,236\n"
"buffer_load_dwordx4 v255, s[36:39], s80 offen sc1 lds\n"
"EX 132\n"
"MA v,156,a,148,a,116\n"
"TR 166,251,62464\n"
"MA v,156,a,152,a,120\n"
"EX 133\n"
"MA v,156,a,156,a,124\n"
"s_add_u32 m0, s69, 107520\n"
"TR 168,252,50176\n"
"CV 180,132,133\n"
"MC 152,156,108,232\n"
"s_mov_b32 s80, 35840\n"
"EX 134\n"
"MA v,152,a,152,a,104\n"
"buffer_load_dwordx4 v255, s[36:39], s80 offen sc1 lds\n"
"MA v,152,a,148,a,100\n"
"EX 135\n"
"MA v,152,a,144,a,96\n"
"TR 170,252,54272\n"
"CV 181,134,135\n"
"MC 148,144,80,228\n"
"EX 136\n"
"MA v,148,a,148,a,84\n"
"TR 172,252,58368\n"
"MA v,148,a,152,a,88\n"
"EX 137\n"
"MA v,148,a,156,a,92\n"
"s_mov_b32 s80, 11264\n"
"TR 174,252,62464\n"
"CV 184,136,137\n"
"MC 144,156,76,224\n"
"LG 212,36\n"
"EX 138\n"
"MA v,144,a,152,a,72\n"
"TR 176,251,50688\n"
"MA v,144,a,148,a,68\n"
"EX 139\n"
"MA v,144,a,144,a,64\n"
"s_waitcnt lgkmcnt(5)\n"
"TR 178,251,54784\n"
"CV 185,138,139\n"
"MA v,64,v,192,a,160\n"
"EX 140\n"
"MA v,64,v,208,a,164\n"
"TR 180,251,58880\n"
"MA v,68,v,212,a,164\n"
"EX 141\n"
"MA v,68,v,196,a,160\n"
"TR 182,251,62976\n"
"CV 188,140,141\n"
"MA v,72,v,200,a,160\n"
"s_mov_b32 s80, 3072\n"
"EX 142\n"
"MA v,72,v,216,a,164\n"
"LG 216,36\n"
"MA v,76,v,220,a,164\n"
"EX 143\n"
"MA v,76,v,204,a,160\n"
"s_waitcnt lgkmcnt(4)\n"
"TR 184,252,50688\n"
"CV 189,142,143\n"
"MA v,92,v,204,a,168\n"
"EX 156\n"
"MA v,92,v,220,a,172\n"
"TR 186,252,54784\n"
"MA v,88,v,216,a,172\n"
"s_mov_b32 s80, 12288\n"
"EX 157\n"
"MA v,88,v,200,a,168\n"
"LG 220,36\n"
"CV 190,156,157\n"
"MA v,84,v,196,a,168\n"
"EX 158\n"
"MA v,84,v,212,a,172\n"
"TR 188,252,58880\n"
"MA v,80,v,208,a,172\n"
"EX 159\n"
"MA v,80,v,192,a,168\n"
"s_waitcnt lgkmcnt(3)\n"
"TR 190,252,62976\n"
"CV 191,158,159\n"
"MA v,96,v,192,a,176\n"
"s_mov_b32 s80, 4096\n"
"EX 152\n"
"MA v,96,v,208,a,180\n"
"LG 224,36\n"
"MA v,100,v,212,a,180\n"
"EX 153\n"
"MA v,100,v,196,a,176\n"
"CV 186,152,153\n"
"MA v,104,v,200,a,176\n"
"s_mov_b32 s80, 13312\n"
"s_waitcnt lgkmcnt(0)\n"
"EX 154\n"
"MA v,104,v,216,a,180\n"
"s_waitcnt vmcnt(33)\n"
"LG 228,36\n"
"MA v,108,v,220,a,180\n"
"s_barrier\n"
"EX 155\n"
"MA v,108,v,204,a,176\n"
"RD 128,249,49152\n"
"CV 187,154,155\n"
"MA v,124,v,204,a,184\n"
"s_waitcnt vmcnt(9)\n"
"EX 148\n"
"v_cmp_ne_u64 s[82:83], s[28:29], v[244:245]\n"
"MA v,124,v,220,a,188\n"
"RD 132,250,49152\n"
"MA v,120,v,216,a,188\n"
"EX 149\n"
"MA v,120,v,200,a,184\n"
"RD 136,249,50176\n"
"CV 182,148,149\n"
"MA v,116,v,196,a,184\n"
"EX 150\n"
"MA v,116,v,212,a,188\n"
"RD 140,250,50176\n"
"MA v,112,v,208,a,188\n"
"EX 151\n"
"MA v,112,v,192,a,184\n"
"CV 183,150,151\n"
"MA v,240,v,160,a,236\n"
"s_mov_b32 s80, 5120\n"
"MA v,240,v,164,a,240\n"
"LG 232,36\n"
"MA v,240,v,168,a,244\n"
"MA v,240,v,172,a,248\n"
"s_waitcnt lgkmcnt(0)\n"
"s_cmp_eq_u32 s83, 0\n"
"s_cselect_b32 s42, 65536, 0\n"
"s_mov_b32 s46, s42\n"
"MC 128,128,64,224\n"
"EX 144\n"
"MA v,128,a,132,a,68\n"
"RD 144,249,53248\n"
"MA v,128,a,136,a,72\n"
"s_add_u32 m0, s69, 32768\n"
"EX 145\n"
"MA v,128,a,140,a,76\n"
"s_add_u32 s80, s65, 8192\n"
"RD 148,250,53248\n"
"CV 178,144,145\n"
"MC 132,140,92,228\n"
"buffer_load_dwordx4 v255, s[44:47], s80 offen sc1 lds\n"
"EX 146\n"
"MA v,132,a,136,a,88\n"
"RD 152,249,54272\n"
"MA v,132,a,132,a,84\n"
"EX 147\n"
"MA v,132,a,128,a,80\n"
"s_add_u32 m0, s69, 33792\n"
"RD 156,250,54272\n"
"CV 179,146,147\n"
"MC 136,128,96,232\n"
"s_add_u32 s80, s65, 12288\n"
"EX 128\n"
"MA v,136,a,132,a,100\n"
"buffer_load_dwordx4 v255, s[44:47], s80 offen sc1 lds\n"
"MA v,136,a,136,a,104\n"
"EX 129\n"
"MA v,136,a,140,a,108\n"
"TR 160,251,0\n"
"CV 192,128,129\n"
"MC 140,140,124,236\n"
"s_add_u32 m0, s69, 114688\n"
"EX 130\n"
"MA v,140,a,136,a,120\n"
"s_add_u32 s80, s66, 8192\n"
"TR 162,251,4096\n"
"MA v,140,a,132,a,116\n"
"buffer_load_dwordx4 v255, s[44:47], s80 offen sc1 lds\n"
"EX 131\n"
"MA v,140,a,128,a,112\n"
"s_waitcnt lgkmcnt(2)\n"
"TR 164,251,8192\n"
"CV 193,130,131\n"
"MC 156,144,112,236\n"
"EX 132\n"
"MA v,156,a,148,a,116\n"
"TR 166,251,12288\n"
"MA v,156,a,152,a,120\n"
"EX 133\n"
"MA v,156,a,156,a,124\n"
"s_add_u32 m0, s69, 115712\n"
"TR 168,252,0\n"
"CV 196,132,133\n"
"MC 152,156,108,232\n"
"s_add_u32 s80, s66, 12288\n"
"EX 134\n"
"MA v,152,a,152,a,104\n"
"buffer_load_dwordx4 v255, s[44:47], s80 offen sc1 lds\n"
"MA v,152,a,148,a,100\n"
"EX 135\n"
"MA v,152,a,144,a,96\n"
"TR 170,252,4096\n"
"CV 197,134,135\n"
"MC 148,144,80,228\n"
"EX 136\n"
"MA v,148,a,148,a,84\n"
"s_add_u32 s80, s65, 0\n"
"TR 172,252,8192\n"
"MA v,148,a,152,a,88\n"
"LG 0,44\n"
"EX 137\n"
"MA v,148,a,156,a,92\n"
"TR 174,252,12288\n"
"CV 200,136,137\n"
"MC 144,156,76,224\n"
"EX 138\n"
"MA v,144,a,152,a,72\n"
"TR 176,251,512\n"
"MA v,144,a,148,a,68\n"
"EX 139\n"
"MA v,144,a,144,a,64\n"
"s_waitcnt lgkmcnt(5)\n"
"s_add_u32 s80, s65, 4096\n"
"TR 178,251,4608\n"
"CV 201,138,139\n"
"MA v,0,v,160,a,160\n"
"LG 4,44\n"
"EX 140\n"
"MA v,0,v,176,a,164\n"
"TR 180,251,8704\n"
"MA v,4,v,180,a,164\n"
"EX 141\n"
"MA v,4,v,164,a,160\n"
"TR 182,251,12800\n"
"CV 204,140,141\n"
"MA v,8,v,168,a,160\n"
"s_add_u32 s80, s66, 0\n"
"EX 142\n"
"MA v,8,v,184,a,164\n"
"LG 8,44\n"
"MA v,12,v,188,a,164\n"
"EX 143\n"
"MA v,12,v,172,a,160\n"
"s_waitcnt lgkmcnt(4)\n"
"TR 184,252,512\n"
"CV 205,142,143\n"
"MA v,28,v,172,a,168\n"
"EX 156\n"
"MA v,28,v,188,a,172\n"
"TR 186,252,4608\n"
"MA v,24,v,184,a,172\n"
"s_add_u32 s80, s66, 4096\n"
"EX 157\n"
"MA v,24,v,168,a,168\n"
"LG 12,44\n"
"CV 206,156,157\n"
"MA v,20,v,164,a,168\n"
"EX 158\n"
"MA v,20,v,180,a,172\n"
"TR 188,252,8704\n"
"MA v,16,v,176,a,172\n"
"EX 159\n"
"MA v,16,v,160,a,168\n"
"s_waitcnt lgkmcnt(3)\n"
"TR 190,252,12800\n"
"CV 207,158,159\n"
"MA v,32,v,160,a,176\n"
"s_add_u32 s80, s67, 0\n"
"EX 152\n"
"MA v,32,v,176,a,180\n"
"LG 16,44\n"
"MA v,36,v,180,a,180\n"
"EX 153\n"
"MA v,36,v,164,a,176\n"
"CV 202,152,153\n"
"MA v,40,v,168,a,176\n"
"s_add_u32 s80, s67, 4096\n"
"EX 154\n"
"MA v,40,v,184,a,180\n"
"LG 20,44\n"
"MA v,44,v,188,a,180\n"
"EX 155\n"
"MA v,44,v,172,a,176\n"
"s_waitcnt lgkmcnt(0)\n"
"RD 128,249,57344\n"
"CV 203,154,155\n"
"MA v,60,v,172,a,184\n"
"EX 148\n"
"MA v,60,v,188,a,188\n"
"s_add_u32 s80, s78, 0\n"
"RD 132,250,57344\n"
"MA v,56,v,184,a,188\n"
"LG 24,44\n"
"EX 149\n"
"MA v,56,v,168,a,184\n"
"RD 136,249,58368\n"
"CV 198,148,149\n"
"MA v,52,v,164,a,184\n"
"EX 150\n"
"MA v,52,v,180,a,188\n"
"RD 140,250,58368\n"
"MA v,48,v,176,a,188\n"
"s_add_u32 s80, s78, 4096\n"
"EX 151\n"
"MA v,48,v,160,a,184\n"
"LG 28,44\n"
"CV 199,150,151\n"
"MA v,240,v,176,a,236\n"
"MA v,240,v,180,a,240\n"
"MA v,240,v,184,a,244\n"
"MA v,240,v,188,a,248\n"
"s_waitcnt lgkmcnt(0)\n"
"MC 128,128,64,224\n"
"EX 144\n"
"MA v,128,a,132,a,68\n"
"RD 144,249,61440\n"
"MA v,128,a,136,a,72\n"
"s_add_u32 m0, s69, 40960\n"
"EX 145\n"
"MA v,128,a,140,a,76\n"
"s_add_u32 s80, s67, 8192\n"
"RD 148,250,61440\n"
"CV 194,144,145\n"
"MC 132,140,92,228\n"
"buffer_load_dwordx4 v255, s[44:47], s80 offen sc1 lds\n"
"EX 146\n"
"MA v,132,a,136,a,88\n"
"RD 152,249,62464\n"
"MA v,132,a,132,a,84\n"
"EX 147\n"
"MA v,132,a,128,a,80\n"
"s_add_u32 m0, s69, 41984\n"
"RD 156,250,62464\n"
"CV 195,146,147\n"
"MC 136,128,96,232\n"
"s_add_u32 s80, s67, 12288\n"
"EX 128\n"
"MA v,136,a,132,a,100\n"
"buffer_load_dwordx4 v255, s[44:47], s80 offen sc1 lds\n"
"MA v,136,a,136,a,104\n"
"EX 129\n"
"MA v,136,a,140,a,108\n"
"TR 160,251,1024\n"
"CV 208,128,129\n"
"MC 140,140,124,236\n"
"s_add_u32 m0, s69, 122880\n"
"EX 130\n"
"MA v,140,a,136,a,120\n"
"s_add_u32 s80, s78, 8192\n"
"TR 162,251,5120\n"
"MA v,140,a,132,a,116\n"
"buffer_load_dwordx4 v255, s[44:47], s80 offen sc1 lds\n"
"EX 131\n"
"MA v,140,a,128,a,112\n"
"s_waitcnt lgkmcnt(2)\n"
"TR 164,251,9216\n"
"CV 209,130,131\n"
"MC 156,144,112,236\n"
"EX 132\n"
"MA v,156,a,148,a,116\n"
"TR 166,251,13312\n"
"MA v,156,a,152,a,120\n"
"EX 133\n"
"MA v,156,a,156,a,124\n"
"s_add_u32 m0, s69, 123904\n"
"TR 168,252,1024\n"
"CV 212,132,133\n"
"MC 152,156,108,232\n"
"s_add_u32 s80, s78, 12288\n"
"EX 134\n"
"MA v,152,a,152,a,104\n"
"buffer_load_dwordx4 v255, s[44:47], s80 offen sc1 lds\n"
"MA v,152,a,148,a,100\n"
"EX 135\n"
"MA v,152,a,144,a,96\n"
"TR 170,252,5120\n"
"CV 213,134,135\n"
"MC 148,144,80,228\n"
"EX 136\n"
"MA v,148,a,148,a,84\n"
"s_add_u32 s80, s65, 0\n"
"TR 172,252,9216\n"
"MA v,148,a,152,a,88\n"
"LG 32,40\n"
"EX 137\n"
"MA v,148,a,156,a,92\n"
"TR 174,252,13312\n"
"CV 216,136,137\n"
"MC 144,156,76,224\n"
"EX 138\n"
"MA v,144,a,152,a,72\n"
"TR 176,251,1536\n"
"MA v,144,a,148,a,68\n"
"EX 139\n"
"MA v,144,a,144,a,64\n"
"s_waitcnt lgkmcnt(5)\n"
"s_add_u32 s80, s65, 4096\n"
"TR 178,251,5632\n"
"CV 217,138,139\n"
"MA v,64,v,160,a,160\n"
"LG 36,40\n"
"EX 140\n"
"MA v,64,v,176,a,164\n"
"TR 180,251,9728\n"
"MA v,68,v,180,a,164\n"
"EX 141\n"
"MA v,68,v,164,a,160\n"
"TR 182,251,13824\n"
"CV 220,140,141\n"
"MA v,72,v,168,a,160\n"
"s_add_u32 s80, s66, 0\n"
"EX 142\n"
"MA v,72,v,184,a,164\n"
"LG 40,40\n"
"MA v,76,v,188,a,164\n"
"EX 143\n"
"MA v,76,v,172,a,160\n"
"s_waitcnt lgkmcnt(4)\n"
"TR 184,252,1536\n"
"CV 221,142,143\n"
"MA v,92,v,172,a,168\n"
"EX 156\n"
"MA v,92,v,188,a,172\n"
"TR 186,252,5632\n"
"MA v,88,v,184,a,172\n"
"s_add_u32 s80, s66, 4096\n"
"EX 157\n"
"MA v,88,v,168,a,168\n"
"LG 44,40\n"
"CV 222,156,157\n"
"MA v,84,v,164,a,168\n"
"EX 158\n"
"MA v,84,v,180,a,172\n"
"TR 188,252,9728\n"
"MA v,80,v,176,a,172\n"
"EX 159\n"
"MA v,80,v,160,a,168\n"
"s_waitcnt lgkmcnt(3)\n"
"TR 190,252,13824\n"
"CV 223,158,159\n"
"MA v,96,v,160,a,176\n"
"s_add_u32 s80, s67, 0\n"
"EX 152\n"
"MA v,96,v,176,a,180\n"
"LG 48,40\n"
"MA v,100,v,180,a,180\n"
"EX 153\n"
"MA v,100,v,164,a,176\n"
"CV 218,152,153\n"
"MA v,104,v,168,a,176\n"
"s_add_u32 s80, s67, 4096\n"
"s_waitcnt lgkmcnt(0)\n"
"EX 154\n"
"MA v,104,v,184,a,180\n"
"s_waitcnt vmcnt(63)\n"
"LG 52,40\n"
"MA v,108,v,188,a,180\n"
"s_barrier\n"
"EX 155\n"
"MA v,108,v,172,a,176\n"
"TR 160,251,16384\n"
"CV 219,154,155\n"
"MA v,124,v,172,a,184\n"
"EX 148\n"
"MA v,124,v,188,a,188\n"
"s_add_u32 s80, s78, 0\n"
"TR 162,251,20480\n"
"MA v,120,v,184,a,188\n"
"LG 56,40\n"
"EX 149\n"
"MA v,120,v,168,a,184\n"
"TR 164,251,24576\n"
"CV 214,148,149\n"
"MA v,116,v,164,a,184\n"
"EX 150\n"
"MA v,116,v,180,a,188\n"
"TR 166,251,28672\n"
"MA v,112,v,176,a,188\n"
"s_add_u32 s80, s78, 4096\n"
"EX 151\n"
"MA v,112,v,160,a,184\n"
"L_ST18_%=:\n"
"s_cbranch_execz L_VB4_%=\n"
"LG 60,40\n"
"CV 215,150,151\n"
"MA v,240,v,192,a,236\n"
"EX 144\n"
"MA v,240,v,196,a,240\n"
"EX 145\n"
"MA v,240,v,200,a,244\n"
"EX 146\n"
"MA v,240,v,204,a,248\n"
"EX 147\n"
"CV 210,144,145\n"
"CV 211,146,147\n"
"s_nop 1\n"
"MA v,240,v,208,a,236\n"
"TR 168,252,16384\n"
"MA v,240,v,212,a,240\n"
"TR 170,252,20480\n"
"MA v,240,v,216,a,244\n"
"TR 172,252,24576\n"
"MA v,240,v,220,a,248\n"
"s_waitcnt lgkmcnt(3)\n"
"s_add_u32 m0, s69, 49152\n"
"TR 174,252,28672\n"
"MA v,0,v,192,a,160\n"
"s_add_u32 s80, s65, 8192\n"
"TR 176,251,16896\n"
"MA v,0,v,208,a,164\n"
"buffer_load_dwordx4 v255, s[40:43], s80 offen sc1 lds\n"
"MA v,4,v,212,a,164\n"
"TR 178,251,20992\n"
"MA v,4,v,196,a,160\n"
"s_add_u32 m0, s69, 50176\n"
"TR 180,251,25088\n"
"MA v,8,v,200,a,160\n"
"s_add_u32 s80, s65, 12288\n"
"TR 182,251,29184\n"
"v_add_f32 v226, v240, v241\n"
"v_add_f32 v230, v242, v243\n"
"MA v,8,v,216,a,164\n"
"buffer_load_dwordx4 v255, s[40:43], s80 offen sc1 lds\n"
"AD 226,230\n"
"v_cmp_nge_f32 s[82:83], s84, v226\n"
"MA v,12,v,220,a,164\n"
"TR 184,252,16896\n"
"v_rcp_f32 v244, v240\n"
"MA v,12,v,204,a,160\n"
"s_waitcnt lgkmcnt(5)\n"
"s_add_u32 m0, s69, 57344\n"
"TR 186,252,20992\n"
"v_rcp_f32 v245, v241\n"
"MA v,28,v,204,a,168\n"
"s_add_u32 s80, s66, 8192\n"
"TR 188,252,25088\n"
"v_rcp_f32 v246, v242\n"
"MA v,28,v,220,a,172\n"
"buffer_load_dwordx4 v255, s[40:43], s80 offen sc1 lds\n"
"v_rcp_f32 v247, v243\n"
"MA v,24,v,216,a,172\n"
"TR 190,252,29184\n"
"MA v,24,v,200,a,168\n"
"s_add_u32 m0, s69, 58368\n"
"TR 128,251,17408\n"
"MA v,20,v,196,a,168\n"
"s_add_u32 s80, s66, 12288\n"
"TR 130,251,21504\n"
"MA v,20,v,212,a,172\n"
"buffer_load_dwordx4 v255, s[40:43], s80 offen sc1 lds\n"
"MA v,16,v,208,a,172\n"
"s_cmp_eq_u64 s[82:83], 0\n"
"s_cselect_b32 s79, 1, s74\n"
"TR 132,251,25600\n"
"MA v,16,v,192,a,168\n"
"s_waitcnt lgkmcnt(7)\n"
"s_add_u32 m0, s69, 65536\n"
"TR 134,251,29696\n"
"MA v,32,v,192,a,176\n"
"s_add_u32 s80, s67, 8192\n"
"TR 136,252,17408\n"
"MA v,32,v,208,a,180\n"
"s_lshl_b32 s79, s79, 1\n"
"buffer_load_dwordx4 v255, s[40:43], s80 offen sc1 lds\n"
"MA v,36,v,212,a,180\n"
"s_and_b32 s79, s79, s75\n"
"s_cselect_b32 s58, 32768, 0\n"
"TR 138,252,21504\n"
"MA v,36,v,196,a,176\n"
"s_add_u32 m0, s69, 66560\n"
"TR 140,252,25600\n"
"MA v,40,v,200,a,176\n"
"s_add_u32 s80, s67, 12288\n"
"TR 142,252,29696\n"
"MA v,40,v,216,a,180\n"
"s_xor_b32 s75, s75, s79\n"
"s_add_u32 s85, s31, 8\n"
"buffer_load_dwordx4 v255, s[40:43], s80 offen sc1 lds\n"
"MA v,44,v,220,a,180\n"
"s_add_u32 s79, s85, 8\n"
"TR 144,251,17920\n"
"MA v,44,v,204,a,176\n"
"s_waitcnt lgkmcnt(9)\n"
"s_add_u32 m0, s69, 73728\n"
"TR 146,251,22016\n"
"MA v,60,v,204,a,184\n"
"s_add_u32 s80, s78, 8192\n"
"TR 148,251,26112\n"
"MA v,60,v,220,a,188\n"
"s_cmp_eq_u32 s58, 0\n"
"s_cselect_b32 s54, 0, s79\n"
"buffer_load_dwordx4 v255, s[40:43], s80 offen sc1 lds\n"
"MA v,56,v,216,a,188\n"
"s_lshl2_add_u32 s79, s64, 163776\n"
"TR 150,251,30208\n"
"v_mov_b32 v148, s75\n"
"MA v,56,v,200,a,184\n"
"s_add_u32 m0, s69, 74752\n"
"TR 152,252,17920\n"
"v_mov_b32 v149, s79\n"
"MA v,52,v,196,a,184\n"
"s_add_u32 s80, s78, 12288\n"
"TR 154,252,22016\n"
"v_mov_b32 v150, 163776\n"
"MA v,52,v,212,a,188\n"
"buffer_load_dwordx4 v255, s[40:43], s80 offen sc1 lds\n"
"MA v,48,v,208,a,188\n"
"TR 156,252,26112\n"
"MA v,48,v,192,a,184\n"
"TR 158,252,30208\n"
"ds_write_b32 v149, v148\n"
"s_add_u32 s79, s69, 81920\n"
"s_waitcnt lgkmcnt(14)\n"
"MA v,64,v,192,a,128\n"
"s_waitcnt lgkmcnt(13)\n"
"s_mov_b32 s80, 14336\n"
"v_lshrrev_b32 v151, 8, v255\n"
"v_bfe_u32 v252, v255, 6, 2\n"
"v_xor_b32 v151, v151, v252\n"
"MA v,64,v,208,a,132\n"
"LG 236,36\n"
"v_and_b32 v252, 0xf0, v255\n"
"v_lshl_add_u32 v252, v252, 1, s79\n"
"MA v,68,v,212,a,132\n"
"s_waitcnt lgkmcnt(0)\n"
"v_lshl_add_u32 v252, v151, 3, v252\n"
"v_lshrrev_b32 v253, 4, v255\n"
"v_and_b32 v253, 0x30, v253\n"
"MA v,68,v,196,a,128\n"
"v_xor_b32 v253, v253, v255\n"
"v_lshrrev_b32 v253, 1, v253\n"
"v_add_u32 v253, s79, v253\n"
"MA v,72,v,200,a,128\n"
"s_barrier\n"
"v_mul_f32_dpp v0, v244, v0 row_newbcast:0 row_mask:0xf bank_mask:0xf\n"
"v_mul_f32_dpp v1, v245, v1 row_newbcast:0 row_mask:0xf bank_mask:0xf\n"
"v_mul_f32_dpp v2, v246, v2 row_newbcast:0 row_mask:0xf bank_mask:0xf\n"
"MA v,72,v,216,a,132\n"
"ds_read_b128 v[152:155], v150\n"
"v_mul_f32_dpp v3, v247, v3 row_newbcast:0 row_mask:0xf bank_mask:0xf\n"
"CV 0,0,1\n"
"MA v,76,v,220,a,132\n"
"s_mov_b32 s80, 6144\n"
"CV 1,2,3\n"
"v_mul_f32_dpp v16, v244, v16 row_newbcast:0 row_mask:0xf bank_mask:0xf\n"
"v_mul_f32_dpp v17, v245, v17 row_newbcast:0 row_mask:0xf bank_mask:0xf\n"
"MA v,76,v,204,a,128\n"
"LG 240,36\n"
"v_mul_f32_dpp v18, v246, v18 row_newbcast:0 row_mask:0xf bank_mask:0xf\n"
"v_mul_f32_dpp v19, v247, v19 row_newbcast:0 row_mask:0xf bank_mask:0xf\n"
"MA v,92,v,204,a,136\n"
"CV 16,16,17\n"
"CV 17,18,19\n"
"v_mul_f32_dpp v4, v244, v4 row_newbcast:1 row_mask:0xf bank_mask:0xf\n"
"MA v,92,v,220,a,140\n"
"v_mul_f32_dpp v5, v245, v5 row_newbcast:1 row_mask:0xf bank_mask:0xf\n"
"v_mul_f32_dpp v6, v246, v6 row_newbcast:1 row_mask:0xf bank_mask:0xf\n"
"v_mul_f32_dpp v7, v247, v7 row_newbcast:1 row_mask:0xf bank_mask:0xf\n"
"MA v,88,v,216,a,140\n"
"s_waitcnt lgkmcnt(0)\n"
"s_mov_b32 s80, 15360\n"
"ds_write_b64 v252, v[0:1] offset:0\n"
"v_or3_b32 v152, v152, v153, v154\n"
"v_or_b32 v152, v152, v155\n"
"CV 4,4,5\n"
"MA v,88,v,200,a,136\n"
"LG 244,36\n"
"v_readfirstlane_b32 s73, v152\n"
"CV 5,6,7\n"
"MA v,84,v,196,a,136\n"
"ds_write_b64 v252, v[16:17] offset:512\n"
"v_mul_f32_dpp v20, v244, v20 row_newbcast:1 row_mask:0xf bank_mask:0xf\n"
"v_mul_f32_dpp v21, v245, v21 row_newbcast:1 row_mask:0xf bank_mask:0xf\n"
"v_mul_f32_dpp v22, v246, v22 row_newbcast:1 row_mask:0xf bank_mask:0xf\n"
"MA v,84,v,212,a,140\n"
"v_mul_f32_dpp v23, v247, v23 row_newbcast:1 row_mask:0xf bank_mask:0xf\n"
"CV 20,20,21\n"
"CV 21,22,23\n"
"s_min_u32 s79, s38, s42\n"
"s_or_b32 s79, s73, s79\n"
"s_cbranch_scc0 L_LATE_%=\n"
"L_PA_OK_%=:\n"
"MA v,80,v,208,a,140\n"
"v_mul_f32_dpp v8, v244, v8 row_newbcast:2 row_mask:0xf bank_mask:0xf\n"
"v_mul_f32_dpp v9, v245, v9 row_newbcast:2 row_mask:0xf bank_mask:0xf\n"
"v_mul_f32_dpp v10, v246, v10 row_newbcast:2 row_mask:0xf bank_mask:0xf\n"
"MA v,80,v,192,a,136\n"
"s_mov_b32 s80, 7168\n"
"ds_write_b64 v252, v[4:5] offset:1024\n"
"v_mul_f32_dpp v11, v247, v11 row_newbcast:2 row_mask:0xf bank_mask:0xf\n"
"CV 8,8,9\n"
"CV 9,10,11\n"
"MA v,96,v,192,a,144\n"
"LG 248,36\n"
"v_mul_f32_dpp v24, v244, v24 row_newbcast:2 row_mask:0xf bank_mask:0xf\n"
"v_mul_f32_dpp v25, v245, v25 row_newbcast:2 row_mask:0xf bank_mask:0xf\n"
"MA v,96,v,208,a,148\n"
"ds_write_b64 v252, v[20:21] offset:1536\n"
"v_mul_f32_dpp v26, v246, v26 row_newbcast:2 row_mask:0xf bank_mask:0xf\n"
"v_mul_f32_dpp v27, v247, v27 row_newbcast:2 row_mask:0xf bank_mask:0xf\n"
"CV 24,24,25\n"
"MA v,100,v,212,a,148\n"
"ds_read_b64_tr_b16 v[160:161], v253 offset:0\n"
"CV 25,26,27\n"
"v_mul_f32_dpp v12, v244, v12 row_newbcast:3 row_mask:0xf bank_mask:0xf\n"
"v_mul_f32_dpp v13, v245, v13 row_newbcast:3 row_mask:0xf bank_mask:0xf\n"
"MA v,100,v,196,a,144\n"
"s_mov_b32 s80, 16384\n"
"ds_write_b64 v252, v[8:9] offset:8192\n"
"v_mul_f32_dpp v14, v246, v14 row_newbcast:3 row_mask:0xf bank_mask:0xf\n"
"v_mul_f32_dpp v15, v247, v15 row_newbcast:3 row_mask:0xf bank_mask:0xf\n"
"CV 12,12,13\n"
"MA v,104,v,200,a,144\n"
"LG 172,36\n"
"CV 13,14,15\n"
"v_mul_f32_dpp v28, v244, v28 row_newbcast:3 row_mask:0xf bank_mask:0xf\n"
"MA v,104,v,216,a,148\n"
"ds_read_b64_tr_b16 v[162:163], v253 offset:512\n"
"v_mul_f32_dpp v29, v245, v29 row_newbcast:3 row_mask:0xf bank_mask:0xf\n"
"v_mul_f32_dpp v30, v246, v30 row_newbcast:3 row_mask:0xf bank_mask:0xf\n"
"v_mul_f32_dpp v31, v247, v31 row_newbcast:3 row_mask:0xf bank_mask:0xf\n"
"MA v,108,v,220,a,148\n"
"ds_write_b64 v252, v[24:25] offset:8704\n"
"CV 28,28,29\n"
"CV 29,30,31\n"
"v_mul_f32_dpp v32, v244, v32 row_newbcast:0 row_mask:0xf bank_mask:0xf\n"
"MA v,108,v,204,a,144\n"
"s_mov_b32 s80, 8192\n"
"ds_read_b64_tr_b16 v[164:165], v253 offset:1024\n"
"v_mul_f32_dpp v33, v245, v33 row_newbcast:0 row_mask:0xf bank_mask:0xf\n"
"v_mul_f32_dpp v34, v246, v34 row_newbcast:0 row_mask:0xf bank_mask:0xf\n"
"v_mul_f32_dpp v35, v247, v35 row_newbcast:0 row_mask:0xf bank_mask:0xf\n"
"MA v,124,v,204,a,152\n"
"LG 176,36\n"
"CV 32,32,33\n"
"CV 33,34,35\n"
"MA v,124,v,220,a,156\n"
"ds_write_b64 v252, v[12:13] offset:9216\n"
"v_mul_f32_dpp v48, v244, v48 row_newbcast:0 row_mask:0xf bank_mask:0xf\n"
"v_mul_f32_dpp v49, v245, v49 row_newbcast:0 row_mask:0xf bank_mask:0xf\n"
"v_mul_f32_dpp v50, v246, v50 row_newbcast:0 row_mask:0xf bank_mask:0xf\n"
"MA v,120,v,216,a,156\n"
"ds_write_b64 v252, v[28:29] offset:9728\n"
"v_mul_f32_dpp v51, v247, v51 row_newbcast:0 row_mask:0xf bank_mask:0xf\n"
"CV 48,48,49\n"
"CV 49,50,51\n"
"MA v,120,v,200,a,152\n"
"s_mov_b32 s80, 17408\n"
"ds_read_b64_tr_b16 v[166:167], v253 offset:1536\n"
"v_mul_f32_dpp v36, v244, v36 row_newbcast:1 row_mask:0xf bank_mask:0xf\n"
"v_mul_f32_dpp v37, v245, v37 row_newbcast:1 row_mask:0xf bank_mask:0xf\n"
"v_mul_f32_dpp v38, v246, v38 row_newbcast:1 row_mask:0xf bank_mask:0xf\n"
"MA v,116,v,196,a,152\n"
"LG 180,36\n"
"v_mul_f32_dpp v39, v247, v39 row_newbcast:1 row_mask:0xf bank_mask:0xf\n"
"CV 36,36,37\n"
"MA v,116,v,212,a,156\n"
"ds_write_b64 v252, v[32:33] offset:0\n"
"CV 37,38,39\n"
"v_mul_f32_dpp v52, v244, v52 row_newbcast:1 row_mask:0xf bank_mask:0xf\n"
"v_mul_f32_dpp v53, v245, v53 row_newbcast:1 row_mask:0xf bank_mask:0xf\n"
"MA v,112,v,208,a,156\n"
"ds_write_b64 v252, v[48:49] offset:512\n"
"v_mul_f32_dpp v54, v246, v54 row_newbcast:1 row_mask:0xf bank_mask:0xf\n"
"v_mul_f32_dpp v55, v247, v55 row_newbcast:1 row_mask:0xf bank_mask:0xf\n"
"CV 52,52,53\n"
"MA v,112,v,192,a,152\n"
"ds_read_b64_tr_b16 v[168:169], v253 offset:8192\n"
"CV 53,54,55\n"
"v_mul_f32_dpp v40, v244, v40 row_newbcast:2 row_mask:0xf bank_mask:0xf\n"
"v_mul_f32_dpp v41, v245, v41 row_newbcast:2 row_mask:0xf bank_mask:0xf\n"
"s_cmp_lg_u32 s73, 0\n"
"s_cbranch_scc1 L_RETRY_%=\n"
"s_waitcnt vmcnt(34)\n"
"MZ v,128,a,192,a,0\n"
"s_waitcnt vmcnt(33)\n"
"ds_read_b64_tr_b16 v[170:171], v253 offset:8704\n"
"v_add_u32 v249, s69, v255\n"
"v_add_u32 v250, 65536, v249\n"
"v_add_u32 v251, 131072, v249\n"
"MA v,128,a,196,a,4\n"
"s_waitcnt vmcnt(31)\n"
"ds_write_b64 v252, v[36:37] offset:1024\n"
"v_mul_f32_dpp v42, v246, v42 row_newbcast:2 row_mask:0xf bank_mask:0xf\n"
"v_mul_f32_dpp v43, v247, v43 row_newbcast:2 row_mask:0xf bank_mask:0xf\n"
"MZ a,64,a,196,a,12\n"
"ds_read_b64_tr_b16 v[172:173], v253 offset:9216\n"
"CV 40,40,41\n"
"CV 41,42,43\n"
"MA a,64,a,192,a,8\n"
"s_waitcnt vmcnt(30)\n"
"ds_write_b64 v252, v[52:53] offset:1536\n"
"v_mul_f32_dpp v56, v244, v56 row_newbcast:2 row_mask:0xf bank_mask:0xf\n"
"MZ a,68,a,192,a,16\n"
"s_waitcnt vmcnt(29)\n"
"ds_write_b64 v252, v[40:41] offset:8192\n"
"v_mul_f32_dpp v57, v245, v57 row_newbcast:2 row_mask:0xf bank_mask:0xf\n"
"MA a,68,a,196,a,20\n"
"s_waitcnt vmcnt(27)\n"
"ds_read_b64_tr_b16 v[174:175], v253 offset:9728\n"
"MZ a,72,a,196,a,28\n"
"ds_read_b64_tr_b16 v[176:177], v253 offset:0\n"
"v_mul_f32_dpp v58, v246, v58 row_newbcast:2 row_mask:0xf bank_mask:0xf\n"
"v_mul_f32_dpp v59, v247, v59 row_newbcast:2 row_mask:0xf bank_mask:0xf\n"
"MA a,72,a,192,a,24\n"
"s_waitcnt vmcnt(22)\n"
"ds_read_b64_tr_b16 v[178:179], v253 offset:512\n"
"CV 56,56,57\n"
"CV 57,58,59\n"
"MZ v,132,a,192,a,32\n"
"s_waitcnt vmcnt(21)\n"
"ds_read_b64_tr_b16 v[180:181], v253 offset:1024\n"
"v_mul_f32_dpp v44, v244, v44 row_newbcast:3 row_mask:0xf bank_mask:0xf\n"
"MA v,132,a,196,a,36\n"
"s_waitcnt vmcnt(19)\n"
"ds_write_b64 v252, v[56:57] offset:8704\n"
"v_mul_f32_dpp v45, v245, v45 row_newbcast:3 row_mask:0xf bank_mask:0xf\n"
"MZ v,136,a,196,a,44\n"
"s_mov_b32 s80, 73728\n"
"ds_read_b64_tr_b16 v[182:183], v253 offset:1536\n"
"MA v,136,a,192,a,40\n"
"s_waitcnt vmcnt(18)\n"
"LG 184,36\n"
"v_mul_f32_dpp v46, v246, v46 row_newbcast:3 row_mask:0xf bank_mask:0xf\n"
"v_mul_f32_dpp v47, v247, v47 row_newbcast:3 row_mask:0xf bank_mask:0xf\n"
"MZ v,140,a,192,a,48\n"
"s_waitcnt vmcnt(18)\n"
"CV 44,44,45\n"
"CV 45,46,47\n"
"MA v,140,a,196,a,52\n"
"s_waitcnt vmcnt(16)\n"
"ds_read_b64_tr_b16 v[184:185], v253 offset:8192\n"
"v_mul_f32_dpp v60, v244, v60 row_newbcast:3 row_mask:0xf bank_mask:0xf\n"
"MZ v,144,a,196,a,60\n"
"ds_write_b64 v252, v[44:45] offset:9216\n"
"v_mul_f32_dpp v61, v245, v61 row_newbcast:3 row_mask:0xf bank_mask:0xf\n"
"MA v,144,a,192,a,56\n"
"s_add_u32 s80, s65, 32768\n"
"ds_read_b64_tr_b16 v[186:187], v253 offset:8704\n"
"MZ v,148,a,200,a,56\n"
"LG 188,44\n"
"v_mul_f32_dpp v62, v246, v62 row_newbcast:3 row_mask:0xf bank_mask:0xf\n"
"v_mul_f32_dpp v63, v247, v63 row_newbcast:3 row_mask:0xf bank_mask:0xf\n"
"MA v,148,a,204,a,60\n"
"CV 60,60,61\n"
"CV 61,62,63\n"
"MZ v,152,a,204,a,52\n"
"ds_read_b64_tr_b16 v[188:189], v253 offset:9216\n"
"v_mul_f32_dpp v64, v244, v64 row_newbcast:0 row_mask:0xf bank_mask:0xf\n"
"MA v,152,a,200,a,48\n"
"ds_write_b64 v252, v[60:61] offset:9728\n"
"v_mul_f32_dpp v65, v245, v65 row_newbcast:0 row_mask:0xf bank_mask:0xf\n"
"MZ v,156,a,200,a,40\n"
"s_mov_b32 s80, 82944\n"
"ds_read_b64_tr_b16 v[190:191], v253 offset:9728\n"
"MA v,156,a,204,a,44\n"
"LG 196,36\n"
"v_mul_f32_dpp v66, v246, v66 row_newbcast:0 row_mask:0xf bank_mask:0xf\n"
"v_mul_f32_dpp v67, v247, v67 row_newbcast:0 row_mask:0xf bank_mask:0xf\n"
"MZ v,224,a,204,a,36\n"
"CV 64,64,65\n"
"CV 65,66,67\n"
"MA v,224,a,200,a,32\n"
"v_mul_f32_dpp v80, v244, v80 row_newbcast:0 row_mask:0xf bank_mask:0xf\n"
"MZ a,76,a,200,a,24\n"
"ds_write_b64 v252, v[64:65] offset:0\n"
"v_mul_f32_dpp v81, v245, v81 row_newbcast:0 row_mask:0xf bank_mask:0xf\n"
"MA a,76,a,204,a,28\n"
"s_add_u32 s80, s65, 36864\n"
"ds_read_b64_tr_b16 v[192:193], v253 offset:0\n"
"MZ a,80,a,204,a,20\n"
"LG 192,44\n"
"v_mul_f32_dpp v82, v246, v82 row_newbcast:0 row_mask:0xf bank_mask:0xf\n"
"v_mul_f32_dpp v83, v247, v83 row_newbcast:0 row_mask:0xf bank_mask:0xf\n"
"MA a,80,a,200,a,16\n"
"CV 80,80,81\n"
"CV 81,82,83\n"
"MZ a,84,a,200,a,8\n"
"v_mul_f32_dpp v68, v244, v68 row_newbcast:1 row_mask:0xf bank_mask:0xf\n"
"MA a,84,a,204,a,12\n"
"ds_write_b64 v252, v[80:81] offset:512\n"
"v_mul_f32_dpp v69, v245, v69 row_newbcast:1 row_mask:0xf bank_mask:0xf\n"
"MZ v,228,a,204,a,4\n"
"s_add_u32 s80, s66, 36864\n"
"ds_read_b64_tr_b16 v[194:195], v253 offset:512\n"
"MA v,228,a,200,a,0\n"
"LG 204,44\n"
"v_mul_f32_dpp v70, v246, v70 row_newbcast:1 row_mask:0xf bank_mask:0xf\n"
"v_mul_f32_dpp v71, v247, v71 row_newbcast:1 row_mask:0xf bank_mask:0xf\n"
"MZ v,232,a,208,a,0\n"
"CV 68,68,69\n"
"CV 69,70,71\n"
"MA v,232,a,212,a,4\n"
"v_mul_f32_dpp v84, v244, v84 row_newbcast:1 row_mask:0xf bank_mask:0xf\n"
"MZ a,88,a,212,a,12\n"
"ds_write_b64 v252, v[68:69] offset:1024\n"
"v_mul_f32_dpp v85, v245, v85 row_newbcast:1 row_mask:0xf bank_mask:0xf\n"
"MA a,88,a,208,a,8\n"
"s_add_u32 s80, s66, 32768\n"
"ds_read_b64_tr_b16 v[196:197], v253 offset:1024\n"
"MZ a,92,a,208,a,16\n"
"LG 200,44\n"
"v_mul_f32_dpp v86, v246, v86 row_newbcast:1 row_mask:0xf bank_mask:0xf\n"
"v_mul_f32_dpp v87, v247, v87 row_newbcast:1 row_mask:0xf bank_mask:0xf\n"
"MA a,92,a,212,a,20\n"
"CV 84,84,85\n"
"CV 85,86,87\n"
"MZ a,96,a,212,a,28\n"
"v_mul_f32_dpp v72, v244, v72 row_newbcast:2 row_mask:0xf bank_mask:0xf\n"
"MA a,96,a,208,a,24\n"
"ds_write_b64 v252, v[84:85] offset:1536\n"
"v_mul_f32_dpp v73, v245, v73 row_newbcast:2 row_mask:0xf bank_mask:0xf\n"
"MZ v,236,a,208,a,32\n"
"ds_read_b64_tr_b16 v[198:199], v253 offset:1536\n"
"MA v,236,a,212,a,36\n"
"v_mul_f32_dpp v74, v246, v74 row_newbcast:2 row_mask:0xf bank_mask:0xf\n"
"v_mul_f32_dpp v75, v247, v75 row_newbcast:2 row_mask:0xf bank_mask:0xf\n"
"MZ v,240,a,212,a,44\n"
"s_mov_b32 s80, 16384\n"
"CV 72,72,73\n"
"CV 73,74,75\n"
"MA v,240,a,208,a,40\n"
"s_waitcnt lgkmcnt(14)\n"
"buffer_store_dwordx4 v[160:163], v255, s[56:59], s80 offen sc1\n"
"v_mul_f32_dpp v88, v244, v88 row_newbcast:2 row_mask:0xf bank_mask:0xf\n"
"MZ v,0,a,208,a,48\n"
"ds_write_b64 v252, v[72:73] offset:8192\n"
"v_mul_f32_dpp v89, v245, v89 row_newbcast:2 row_mask:0xf bank_mask:0xf\n"
"MA v,0,a,212,a,52\n"
"ds_read_b64_tr_b16 v[200:201], v253 offset:8192\n"
"MZ v,16,a,212,a,60\n"
"v_mul_f32_dpp v90, v246, v90 row_newbcast:2 row_mask:0xf bank_mask:0xf\n"
"v_mul_f32_dpp v91, v247, v91 row_newbcast:2 row_mask:0xf bank_mask:0xf\n"
"MA v,16,a,208,a,56\n"
"s_add_u32 s80, s67, 32768\n"
"CV 88,88,89\n"
"CV 89,90,91\n"
"MZ v,4,a,216,a,56\n"
"buffer_load_dwordx4 v[160:163], v255, s[44:47], s80 offen sc1\n"
"v_mul_f32_dpp v76, v244, v76 row_newbcast:3 row_mask:0xf bank_mask:0xf\n"
"MA v,4,a,220,a,60\n"
"ds_write_b64 v252, v[88:89] offset:8704\n"
"v_mul_f32_dpp v77, v245, v77 row_newbcast:3 row_mask:0xf bank_mask:0xf\n"
"MZ v,20,a,220,a,52\n"
"ds_read_b64_tr_b16 v[202:203], v253 offset:8704\n"
"MA v,20,a,216,a,48\n"
"v_mul_f32_dpp v78, v246, v78 row_newbcast:3 row_mask:0xf bank_mask:0xf\n"
"v_mul_f32_dpp v79, v247, v79 row_newbcast:3 row_mask:0xf bank_mask:0xf\n"
"MZ v,8,a,216,a,40\n"
"s_mov_b32 s80, 17408\n"
"CV 76,76,77\n"
"CV 77,78,79\n"
"MA v,8,a,220,a,44\n"
"buffer_store_dwordx4 v[164:167], v255, s[56:59], s80 offen sc1\n"
"v_mul_f32_dpp v92, v244, v92 row_newbcast:3 row_mask:0xf bank_mask:0xf\n"
"MZ v,24,a,220,a,36\n"
"ds_write_b64 v252, v[76:77] offset:9216\n"
"v_mul_f32_dpp v93, v245, v93 row_newbcast:3 row_mask:0xf bank_mask:0xf\n"
"MA v,24,a,216,a,32\n"
"ds_read_b64_tr_b16 v[204:205], v253 offset:9216\n"
"MZ a,100,a,216,a,24\n"
"v_mul_f32_dpp v94, v246, v94 row_newbcast:3 row_mask:0xf bank_mask:0xf\n"
"v_mul_f32_dpp v95, v247, v95 row_newbcast:3 row_mask:0xf bank_mask:0xf\n"
"MA a,100,a,220,a,28\n"
"s_add_u32 s80, s67, 36864\n"
"CV 92,92,93\n"
"CV 93,94,95\n"
"MZ a,104,a,220,a,20\n"
"buffer_load_dwordx4 v[164:167], v255, s[44:47], s80 offen sc1\n"
"v_mul_f32_dpp v96, v244, v96 row_newbcast:0 row_mask:0xf bank_mask:0xf\n"
"MA a,104,a,216,a,16\n"
"ds_write_b64 v252, v[92:93] offset:9728\n"
"v_mul_f32_dpp v97, v245, v97 row_newbcast:0 row_mask:0xf bank_mask:0xf\n"
"MZ a,108,a,216,a,8\n"
"ds_read_b64_tr_b16 v[206:207], v253 offset:9728\n"
"MA a,108,a,220,a,12\n"
"v_mul_f32_dpp v98, v246, v98 row_newbcast:0 row_mask:0xf bank_mask:0xf\n"
"v_mul_f32_dpp v99, v247, v99 row_newbcast:0 row_mask:0xf bank_mask:0xf\n"
"MZ v,12,a,220,a,4\n"
"s_mov_b32 s80, 18432\n"
"CV 96,96,97\n"
"CV 97,98,99\n"
"MA v,12,a,216,a,0\n"
"buffer_store_dwordx4 v[168:171], v255, s[56:59], s80 offen sc1\n"
"v_mul_f32_dpp v112, v244, v112 row_newbcast:0 row_mask:0xf bank_mask:0xf\n"
"MZ v,28,a,224,a,0\n"
"ds_write_b64 v252, v[96:97] offset:0\n"
"v_mul_f32_dpp v113, v245, v113 row_newbcast:0 row_mask:0xf bank_mask:0xf\n"
"MA v,28,a,228,a,4\n"
"ds_read_b64_tr_b16 v[208:209], v253 offset:0\n"
"MZ a,112,a,228,a,12\n"
"v_mul_f32_dpp v114, v246, v114 row_newbcast:0 row_mask:0xf bank_mask:0xf\n"
"v_mul_f32_dpp v115, v247, v115 row_newbcast:0 row_mask:0xf bank_mask:0xf\n"
"MA a,112,a,224,a,8\n"
"s_add_u32 s80, s78, 36864\n"
"CV 112,112,113\n"
"CV 113,114,115\n"
"MZ a,116,a,224,a,16\n"
"buffer_load_dwordx4 v[168:171], v255, s[44:47], s80 offen sc1\n"
"v_mul_f32_dpp v100, v244, v100 row_newbcast:1 row_mask:0xf bank_mask:0xf\n"
"MA a,116,a,228,a,20\n"
"ds_write_b64 v252, v[112:113] offset:512\n"
"v_mul_f32_dpp v101, v245, v101 row_newbcast:1 row_mask:0xf bank_mask:0xf\n"
"MZ a,120,a,228,a,28\n"
"ds_read_b64_tr_b16 v[210:211], v253 offset:512\n"
"MA a,120,a,224,a,24\n"
"v_mul_f32_dpp v102, v246, v102 row_newbcast:1 row_mask:0xf bank_mask:0xf\n"
"v_mul_f32_dpp v103, v247, v103 row_newbcast:1 row_mask:0xf bank_mask:0xf\n"
"MZ v,32,a,224,a,32\n"
"s_mov_b32 s80, 19456\n"
"CV 100,100,101\n"
"CV 101,102,103\n"
"MA v,32,a,228,a,36\n"
"buffer_store_dwordx4 v[172:175], v255, s[56:59], s80 offen sc1\n"
"v_mul_f32_dpp v116, v244, v116 row_newbcast:1 row_mask:0xf bank_mask:0xf\n"
"MZ v,48,a,228,a,44\n"
"ds_write_b64 v252, v[100:101] offset:1024\n"
"v_mul_f32_dpp v117, v245, v117 row_newbcast:1 row_mask:0xf bank_mask:0xf\n"
"MA v,48,a,224,a,40\n"
"ds_read_b64_tr_b16 v[212:213], v253 offset:1024\n"
"MZ v,36,a,224,a,48\n"
"v_mul_f32_dpp v118, v246, v118 row_newbcast:1 row_mask:0xf bank_mask:0xf\n"
"v_mul_f32_dpp v119, v247, v119 row_newbcast:1 row_mask:0xf bank_mask:0xf\n"
"MA v,36,a,228,a,52\n"
"s_add_u32 s80, s78, 32768\n"
"CV 116,116,117\n"
"CV 117,118,119\n"
"L_ST19_%=:\n"
"s_cbranch_execz L_VB4_%=\n"
"MZ v,52,a,228,a,60\n"
"buffer_load_dwordx4 v[172:175], v255, s[44:47], s80 offen sc1\n"
"v_mul_f32_dpp v104, v244, v104 row_newbcast:2 row_mask:0xf bank_mask:0xf\n"
"MA v,52,a,224,a,56\n"
"ds_write_b64 v252, v[116:117] offset:1536\n"
"v_mul_f32_dpp v105, v245, v105 row_newbcast:2 row_mask:0xf bank_mask:0xf\n"
"MZ v,40,a,232,a,56\n"
"s_waitcnt vmcnt(20)\n"
"ds_read_b64_tr_b16 v[214:215], v253 offset:1536\n"
"MA v,40,a,236,a,60\n"
"v_mul_f32_dpp v106, v246, v106 row_newbcast:2 row_mask:0xf bank_mask:0xf\n"
"v_mul_f32_dpp v107, v247, v107 row_newbcast:2 row_mask:0xf bank_mask:0xf\n"
"MZ v,56,a,236,a,52\n"
"s_mov_b32 s80, 20480\n"
"CV 104,104,105\n"
"CV 105,106,107\n"
"MA v,56,a,232,a,48\n"
"buffer_store_dwordx4 v[176:179], v255, s[56:59], s80 offen sc1\n"
"v_mul_f32_dpp v120, v244, v120 row_newbcast:2 row_mask:0xf bank_mask:0xf\n"
"MZ v,44,a,232,a,40\n"
"ds_write_b64 v252, v[104:105] offset:8192\n"
"v_mul_f32_dpp v121, v245, v121 row_newbcast:2 row_mask:0xf bank_mask:0xf\n"
"MA v,44,a,236,a,44\n"
"ds_read_b64_tr_b16 v[216:217], v253 offset:8192\n"
"MZ v,60,a,236,a,36\n"
"v_mul_f32_dpp v122, v246, v122 row_newbcast:2 row_mask:0xf bank_mask:0xf\n"
"v_mul_f32_dpp v123, v247, v123 row_newbcast:2 row_mask:0xf bank_mask:0xf\n"
"MA v,60,a,232,a,32\n"
"s_add_u32 s80, s65, 32768\n"
"CV 120,120,121\n"
"CV 121,122,123\n"
"MZ a,124,a,232,a,24\n"
"buffer_load_dwordx4 v[176:179], v255, s[40:43], s80 offen sc1\n"
"v_mul_f32_dpp v108, v244, v108 row_newbcast:3 row_mask:0xf bank_mask:0xf\n"
"MA a,124,a,236,a,28\n"
"ds_write_b64 v252, v[120:121] offset:8704\n"
"v_mul_f32_dpp v109, v245, v109 row_newbcast:3 row_mask:0xf bank_mask:0xf\n"
"MZ a,128,a,236,a,20\n"
"ds_read_b64_tr_b16 v[218:219], v253 offset:8704\n"
"MA a,128,a,232,a,16\n"
"v_mul_f32_dpp v110, v246, v110 row_newbcast:3 row_mask:0xf bank_mask:0xf\n"
"v_mul_f32_dpp v111, v247, v111 row_newbcast:3 row_mask:0xf bank_mask:0xf\n"
"MZ a,132,a,232,a,8\n"
"s_mov_b32 s80, 21504\n"
"CV 108,108,109\n"
"CV 109,110,111\n"
"MA a,132,a,236,a,12\n"
"buffer_store_dwordx4 v[180:183], v255, s[56:59], s80 offen sc1\n"
"v_mul_f32_dpp v124, v244, v124 row_newbcast:3 row_mask:0xf bank_mask:0xf\n"
"MZ v,64,a,236,a,4\n"
"ds_write_b64 v252, v[108:109] offset:9216\n"
"v_mul_f32_dpp v125, v245, v125 row_newbcast:3 row_mask:0xf bank_mask:0xf\n"
"MA v,64,a,232,a,0\n"
"s_waitcnt vmcnt(22)\n"
"ds_read_b64_tr_b16 v[220:221], v253 offset:9216\n"
"MZ v,80,a,240,a,0\n"
"s_waitcnt vmcnt(21)\n"
"v_mul_f32_dpp v126, v246, v126 row_newbcast:3 row_mask:0xf bank_mask:0xf\n"
"v_mul_f32_dpp v127, v247, v127 row_newbcast:3 row_mask:0xf bank_mask:0xf\n"
"MA v,80,a,244,a,4\n"
"CV 124,124,125\n"
"CV 125,126,127\n"
"MZ a,136,a,244,a,12\n"
"ds_write_b64 v252, v[124:125] offset:9728\n"
"MA a,136,a,240,a,8\n"
"s_mov_b32 s80, 22528\n"
"ds_read_b64_tr_b16 v[222:223], v253 offset:9728\n"
"MZ a,140,a,240,a,16\n"
"s_waitcnt lgkmcnt(14)\n"
"buffer_store_dwordx4 v[184:187], v255, s[56:59], s80 offen sc1\n"
"MA a,140,a,244,a,20\n"
"MZ a,144,a,244,a,28\n"
"MA a,144,a,240,a,24\n"
"MZ v,68,a,240,a,32\n"
"s_add_u32 s80, s65, 36864\n"
"MA v,68,a,244,a,36\n"
"buffer_load_dwordx4 v[180:183], v255, s[40:43], s80 offen sc1\n"
"MZ v,84,a,244,a,44\n"
"MA v,84,a,240,a,40\n"
"MZ v,72,a,240,a,48\n"
"MA v,72,a,244,a,52\n"
"s_mov_b32 s80, 23552\n"
"ds_read_b128 v[184:187], v249 offset:1024\n"
"MZ v,88,a,244,a,60\n"
"buffer_store_dwordx4 v[188:191], v255, s[56:59], s80 offen sc1\n"
"MA v,88,a,240,a,56\n"
"s_waitcnt vmcnt(23)\n"
"RD 212,249,32768\n"
"MZ v,76,a,248,a,56\n"
"s_waitcnt vmcnt(22)\n"
"MA v,76,a,172,a,60\n"
"ds_read_b128 v[188:191], v251 offset:0\n"
"MZ v,92,a,172,a,52\n"
"MA v,92,a,248,a,48\n"
"s_mov_b32 s80, 24576\n"
"RD 208,249,33792\n"
"MZ v,96,a,248,a,40\n"
"buffer_store_dwordx4 v[192:195], v255, s[56:59], s80 offen sc1\n"
"MA v,96,a,172,a,44\n"
"RD 220,250,50176\n"
"MZ v,112,a,172,a,36\n"
"MA v,112,a,248,a,32\n"
"ds_read_b128 v[192:195], v250 offset:49152\n"
"MZ a,148,a,248,a,24\n"
"MA a,148,a,172,a,28\n"
"s_mov_b32 s80, 25600\n"
"RD 216,249,40960\n"
"MZ a,152,a,172,a,20\n"
"buffer_store_dwordx4 v[196:199], v255, s[56:59], s80 offen sc1\n"
"MA a,152,a,248,a,16\n"
"RD 228,249,41984\n"
"MZ a,156,a,248,a,8\n"
"MA a,156,a,172,a,12\n"
"ds_read_b128 v[196:199], v250 offset:58368\n"
"MZ v,100,a,172,a,4\n"
"MA v,100,a,248,a,0\n"
"s_waitcnt vmcnt(23)\n"
"s_mov_b32 s80, 26624\n"
"RD 224,250,57344\n"
"MZ v,116,a,176,a,0\n"
"s_waitcnt vmcnt(22)\n"
"buffer_store_dwordx4 v[200:203], v255, s[56:59], s80 offen sc1\n"
"MA v,116,a,180,a,4\n"
"RD 236,249,49152\n"
"MZ a,160,a,180,a,12\n"
"MA a,160,a,176,a,8\n"
"ds_read_b128 v[200:203], v249 offset:50176\n"
"MZ a,164,a,176,a,16\n"
"MA a,164,a,180,a,20\n"
"s_mov_b32 s80, 27648\n"
"RD 232,249,58368\n"
"MZ a,168,a,180,a,28\n"
"buffer_store_dwordx4 v[204:207], v255, s[56:59], s80 offen sc1\n"
"MA a,168,a,176,a,24\n"
"RD 244,249,57344\n"
"MZ v,104,a,176,a,32\n"
"MA v,104,a,180,a,36\n"
"ds_read_b128 v[204:207], v250 offset:0\n"
"MZ v,120,a,180,a,44\n"
"MA v,120,a,176,a,40\n"
"s_mov_b32 s80, 28672\n"
"RD 240,250,1024\n"
"MZ v,108,a,176,a,48\n"
"s_waitcnt lgkmcnt(14)\n"
"buffer_store_dwordx4 v[208:211], v255, s[56:59], s80 offen sc1\n"
"MA v,108,a,180,a,52\n"
"RD 172,250,9216\n"
"MZ v,124,a,180,a,60\n"
"MA v,124,a,176,a,56\n"
"ds_read_b128 v[208:211], v250 offset:8192\n"
"MA v,128,v,184,a,212\n"
"s_waitcnt lgkmcnt(14)\n"
"MA v,128,v,188,a,208\n"
"s_waitcnt lgkmcnt(13)\n"
"s_mov_b32 s80, 29696\n"
"RD 248,251,1024\n"
"MA a,64,v,188,a,220\n"
"s_waitcnt lgkmcnt(13)\n"
"buffer_store_dwordx4 v[212:215], v255, s[56:59], s80 offen sc1\n"
"MA a,64,v,184,v,192\n"
"s_waitcnt lgkmcnt(12)\n"
"RD 0,249,8192\n"
"MA a,68,v,184,a,216\n"
"s_waitcnt lgkmcnt(12)\n"
"MA a,68,v,188,a,228\n"
"s_waitcnt lgkmcnt(11)\n"
"MA a,72,v,188,v,196\n"
"s_waitcnt lgkmcnt(10)\n"
"s_add_u32 s80, s66, 36864\n"
"MA a,72,v,184,a,224\n"
"s_waitcnt lgkmcnt(9)\n"
"buffer_load_dwordx4 v[212:215], v255, s[40:43], s80 offen sc1\n"
"MA v,132,v,184,a,236\n"
"s_waitcnt lgkmcnt(8)\n"
"MA v,132,v,188,v,200\n"
"s_waitcnt lgkmcnt(7)\n"
"MA v,136,v,188,a,232\n"
"s_waitcnt lgkmcnt(6)\n"
"s_mov_b32 s80, 30720\n"
"MA v,136,v,184,a,244\n"
"s_waitcnt lgkmcnt(5)\n"
"buffer_store_dwordx4 v[216:219], v255, s[56:59], s80 offen sc1\n"
"MA v,140,v,184,v,204\n"
"s_waitcnt lgkmcnt(4)\n"
"MA v,140,v,188,a,240\n"
"s_waitcnt lgkmcnt(3)\n"
"MA v,144,v,188,a,172\n"
"s_waitcnt lgkmcnt(2)\n"
"MA v,144,v,184,v,208\n"
"s_waitcnt lgkmcnt(1)\n"
"s_add_u32 s80, s66, 32768\n"
"MA v,148,a,248,v,208\n"
"s_waitcnt lgkmcnt(0)\n"
"buffer_load_dwordx4 v[216:219], v255, s[40:43], s80 offen sc1\n"
"MA v,148,a,0,a,172\n"
"ds_read_b128 v[188:191], v249 offset:9216\n"
"MA v,152,a,0,a,240\n"
"MA v,152,a,248,v,204\n"
"s_mov_b32 s80, 31744\n"
"ds_read_b128 v[184:187], v251 offset:8192\n"
"MA v,156,a,248,a,244\n"
"buffer_store_dwordx4 v[220:223], v255, s[56:59], s80 offen sc1\n"
"MA v,156,a,0,a,232\n"
"MA v,224,a,0,v,200\n"
"MA v,224,a,248,a,236\n"
"MA a,76,a,248,a,224\n"
"s_add_u32 s80, s67, 32768\n"
"MA a,76,a,0,v,196\n"
"buffer_load_dwordx4 v[220:223], v255, s[40:43], s80 offen sc1\n"
"MA a,80,a,0,a,228\n"
"MA a,80,a,248,a,216\n"
"MA a,84,a,248,v,192\n"
"s_add_u32 s80, s67, 36864\n"
"MA a,84,a,0,a,220\n"
"LG 4,40\n"
"MA v,228,a,0,a,208\n"
"MA v,228,a,248,a,212\n"
"s_waitcnt lgkmcnt(1)\n"
"MA v,232,v,188,a,212\n"
"s_waitcnt lgkmcnt(0)\n"
"s_mul_i32 s79, s64, 6144\n"
"MA v,232,v,184,a,208\n"
"s_add_u32 s80, s78, 36864\n"
"s_add_u32 s79, s79, 131072\n"
"RD 12,251,9216\n"
"MA a,88,v,184,a,220\n"
"LG 8,40\n"
"v_bfe_u32 v252, v255, 4, 4\n"
"MA a,88,v,188,v,192\n"
"RD 16,249,16384\n"
"v_mul_u32_u24 v252, 304, v252\n"
"MA a,92,v,188,a,216\n"
"v_lshrrev_b32 v248, 8, v255\n"
"MA a,92,v,184,a,228\n"
"s_add_u32 s80, s78, 32768\n"
"v_lshlrev_b32 v248, 3, v248\n"
"MA a,96,v,184,v,196\n"
"LG 20,40\n"
"v_add3_u32 v252, v252, v248, s79\n"
"MA a,96,v,188,a,224\n"
"v_mul_u32_u24 v248, 228, v255\n"
"MA v,236,v,188,a,236\n"
"v_lshrrev_b32 v248, 16, v248\n"
"MA v,236,v,184,v,200\n"
"s_mov_b32 s80, 74752\n"
"v_lshl_add_u32 v254, v248, 4, v255\n"
"MA v,240,v,184,a,232\n"
"LG 28,36\n"
"v_add_u32 v254, s79, v254\n"
"MA v,240,v,188,a,244\n"
"v_mul_u32_u24 v248, 2016, v248\n"
"MA v,0,v,188,v,204\n"
"v_cmp_gt_u32 vcc, 0x360, v255\n"
"MA v,0,v,184,a,240\n"
"s_mov_b32 s80, 83968\n"
"v_add_u32 v253, v248, v255\n"
"MA v,16,v,184,a,172\n"
"LG 24,36\n"
"v_mov_b32 v248, 0x100000\n"
"MA v,16,v,188,v,208\n"
"s_waitcnt lgkmcnt(1)\n"
"s_waitcnt vmcnt(16)\n"
"buffer_store_dwordx2 a[252:253], v255, s[52:55], s85 offen sc1\n"
"v_cndmask_b32 v253, v248, v253, vcc\n"
"MA v,4,a,12,v,208\n"
"s_waitcnt lgkmcnt(0)\n"
"MA v,4,a,16,a,172\n"
"ds_read_b128 v[184:187], v249 offset:17408\n"
"MA v,20,a,16,a,240\n"
"MA v,20,a,12,v,204\n"
"s_mov_b32 s80, 75776\n"
"ds_read_b128 v[188:191], v250 offset:32768\n"
"MA v,8,a,12,a,244\n"
"LG 32,36\n"
"MA v,8,a,16,a,232\n"
"MA v,24,a,16,v,200\n"
"s_mov_b32 s80, 84992\n"
"MA v,24,a,12,a,236\n"
"LG 36,36\n"
"MA a,100,a,12,a,224\n"
"s_lshr_b32 s79, s20, 16\n"
"MA a,100,a,16,v,196\n"
"s_add_u32 s79, s79, 8\n"
"MA a,104,a,16,a,228\n"
"v_and_b32 v246, 32, v255\n"
"MA a,104,a,12,a,216\n"
"s_mov_b32 s80, 76800\n"
"v_and_b32 v248, 16, v255\n"
"MA a,108,a,12,v,192\n"
"LG 44,36\n"
"v_lshl_or_b32 v248, v246, 3, v248\n"
"MA a,108,a,16,a,220\n"
"v_xor_b32 v248, s79, v248\n"
"MA v,12,a,16,a,208\n"
"MA v,12,a,12,a,212\n"
"s_waitcnt lgkmcnt(1)\n"
"s_mov_b32 s80, 86016\n"
"MA v,28,v,184,a,212\n"
"s_waitcnt lgkmcnt(0)\n"
"LG 40,36\n"
"MA v,28,v,188,a,208\n"
"RD 48,250,33792\n"
"MA a,112,v,188,a,220\n"
"MA a,112,v,184,v,192\n"
"s_mov_b32 s80, 77824\n"
"RD 52,251,16384\n"
"MA a,116,v,184,a,216\n"
"LG 60,36\n"
"MA a,116,v,188,a,228\n"
"MA a,120,v,188,v,196\n"
"MA a,120,v,184,a,224\n"
"s_add_u32 s85, s85, 24576\n"
"MA v,32,v,184,a,236\n"
"s_mov_b32 s80, 87040\n"
"s_add_u32 s79, s85, 8\n"
"MA v,32,v,188,v,200\n"
"s_cmp_eq_u32 s54, 0\n"
"s_cselect_b32 s54, 0, s79\n"
"LG 180,36\n"
"MA v,48,v,188,a,232\n"
"MA v,48,v,184,a,244\n"
"MA v,36,v,184,v,204\n"
"s_mov_b32 s80, 78848\n"
"MA v,36,v,188,a,240\n"
"LG 56,36\n"
"MA v,52,v,188,a,172\n"
"MA v,52,v,184,v,208\n"
"s_waitcnt lgkmcnt(1)\n"
"MA v,40,a,48,v,208\n"
"s_waitcnt lgkmcnt(0)\n"
"MA v,40,a,52,a,172\n"
"s_mov_b32 s80, 88064\n"
"ds_read_b128 v[188:191], v251 offset:17408\n"
"MA v,56,a,52,a,240\n"
"buffer_load_dwordx4 v[184:187], v255, s[36:39], s80 offen sc1\n"
"MA v,56,a,48,v,204\n"
"RD 176,251,24576\n"
"MA v,44,a,48,a,244\n"
"MA v,44,a,52,a,232\n"
"s_mov_b32 s80, 79872\n"
"MA v,60,a,52,v,200\n"
"LG 0,36\n"
"MA v,60,a,48,a,236\n"
"MA a,124,a,48,a,224\n"
"MA a,124,a,52,v,196\n"
"s_mov_b32 s80, 89088\n"
"MA a,128,a,52,a,228\n"
"LG 248,36\n"
"MA a,128,a,48,a,216\n"
"MA a,132,a,48,v,192\n"
"MA a,132,a,52,a,220\n"
"s_mov_b32 s80, 80896\n"
"MA v,64,a,52,a,208\n"
"LG 16,36\n"
"MA v,64,a,48,a,212\n"
"s_waitcnt lgkmcnt(1)\n"
"MA v,80,v,188,a,212\n"
"s_waitcnt lgkmcnt(0)\n"
"MA v,80,a,176,a,208\n"
"s_mov_b32 s80, 90112\n"
"RD 12,249,24576\n"
"MA a,136,a,176,a,220\n"
"LG 52,36\n"
"MA a,136,v,188,v,192\n"
"RD 48,249,25600\n"
"MA a,140,v,188,a,216\n"
"s_waitcnt vmcnt(19)\n"
"buffer_store_dwordx2 a[252:253], v255, s[52:55], s85 offen sc1\n"
"MA a,140,a,176,a,228\n"
"MA a,144,a,176,v,196\n"
"MA a,144,v,188,a,224\n"
"MA v,68,v,188,a,236\n"
"MA v,68,a,176,v,200\n"
"MA v,84,a,176,a,232\n"
"MA v,84,v,188,a,244\n"
"MA v,72,v,188,v,204\n"
"MA v,72,a,176,a,240\n"
"MA v,88,a,176,a,172\n"
"MA v,88,v,188,v,208\n"
"s_waitcnt lgkmcnt(1)\n"
"MA v,76,a,12,v,208\n"
"s_waitcnt lgkmcnt(0)\n"
"MA v,76,a,48,a,172\n"
"ds_read_b128 v[188:191], v250 offset:40960\n"
"MA v,92,a,48,a,240\n"
"MA v,92,a,12,v,204\n"
"RD 176,250,41984\n"
"MA v,96,a,12,a,244\n"
"MA v,96,a,48,a,232\n"
"MA v,112,a,48,v,200\n"
"MA v,112,a,12,a,236\n"
"MA a,148,a,12,a,224\n"
"MA a,148,a,48,v,196\n"
"MA a,152,a,48,a,228\n"
"MA a,152,a,12,a,216\n"
"MA a,156,a,12,v,192\n"
"MA a,156,a,48,a,220\n"
"MA v,100,a,48,a,208\n"
"s_mov_b32 s80, 81920\n"
"MA v,100,a,12,a,212\n"
"s_waitcnt lgkmcnt(1)\n"
"LG 48,36\n"
"MA v,116,v,188,a,212\n"
"s_waitcnt lgkmcnt(0)\n"
"MA v,116,a,176,a,208\n"
"s_mov_b32 s80, 92160\n"
"MA a,160,a,176,a,220\n"
"LG 12,36\n"
"MA a,160,v,188,v,192\n"
"MA a,164,v,188,a,216\n"
"MA a,164,a,176,a,228\n"
"MA a,168,a,176,v,196\n"
"s_add_u32 s80, s65, 40960\n"
"MA a,168,v,188,a,224\n"
"buffer_load_dwordx4 v[192:195], v255, s[44:47], s80 offen sc1\n"
"MA v,104,v,188,a,236\n"
"MA v,104,a,176,v,200\n"
"MA v,120,a,176,a,232\n"
"s_mov_b32 s80, 101376\n"
"MA v,120,v,188,a,244\n"
"buffer_load_dwordx4 v[196:199], v255, s[36:39], s80 offen sc1\n"
"MA v,108,v,188,v,204\n"
"MA v,108,a,176,a,240\n"
"MA v,124,a,176,a,172\n"
"s_add_u32 s80, s65, 45056\n"
"MA v,124,v,188,v,208\n"
"buffer_load_dwordx4 v[200:203], v255, s[44:47], s80 offen sc1\n"
"MA v,128,a,184,a,188\n"
"MA v,128,a,196,a,192\n"
"MA a,64,a,196,a,204\n"
"MA a,64,a,184,a,200\n"
"s_add_u32 s80, s66, 45056\n"
"MA a,68,a,184,v,160\n"
"buffer_load_dwordx4 v[204:207], v255, s[44:47], s80 offen sc1\n"
"MA a,68,a,196,v,164\n"
"MA a,72,a,196,v,168\n"
"MA a,72,a,184,v,172\n"
"s_add_u32 s80, s66, 40960\n"
"MA v,132,a,184,v,176\n"
"buffer_load_dwordx4 v[208:211], v255, s[44:47], s80 offen sc1\n"
"MA v,132,a,196,v,180\n"
"MA v,136,a,196,v,212\n"
"MA v,136,a,184,v,216\n"
"s_waitcnt vmcnt(26)\n"
"s_add_u32 s80, s67, 40960\n"
"MA v,140,a,184,v,220\n"
"s_waitcnt vmcnt(25)\n"
"buffer_load_dwordx4 v[188:191], v255, s[44:47], s80 offen sc1\n"
"MA v,140,a,196,a,4\n"
"s_waitcnt vmcnt(25)\n"
"MA v,144,a,196,a,8\n"
"s_waitcnt vmcnt(24)\n"
"MA v,144,a,184,a,20\n"
"s_waitcnt vmcnt(23)\n"
"MA v,148,a,28,a,20\n"
"s_waitcnt vmcnt(22)\n"
"s_add_u32 s80, s67, 45056\n"
"MA v,148,a,24,a,8\n"
"LG 212,44\n"
"MA v,152,a,24,a,4\n"
"MA v,152,a,28,v,220\n"
"MA v,156,a,28,v,216\n"
"s_add_u32 s80, s78, 45056\n"
"MA v,156,a,24,v,212\n"
"LG 208,44\n"
"MA v,224,a,24,v,180\n"
"MA v,224,a,28,v,176\n"
"MA a,76,a,28,v,172\n"
"s_add_u32 s80, s78, 40960\n"
"MA a,76,a,24,v,168\n"
"LG 220,44\n"
"MA a,80,a,24,v,164\n"
"MA a,80,a,28,v,160\n"
"MA a,84,a,28,a,200\n"
"s_add_u32 s80, s65, 40960\n"
"MA a,84,a,24,a,204\n"
"LG 216,40\n"
"MA v,228,a,24,a,192\n"
"MA v,228,a,28,a,188\n"
"s_waitcnt vmcnt(24)\n"
"MA v,232,a,32,a,188\n"
"s_waitcnt vmcnt(23)\n"
"MA v,232,a,36,a,192\n"
"s_add_u32 s80, s65, 45056\n"
"MA a,88,a,36,a,204\n"
"LG 228,40\n"
"MA a,88,a,32,a,200\n"
"MA a,92,a,32,v,160\n"
"MA a,92,a,36,v,164\n"
"s_add_u32 s80, s66, 45056\n"
"MA a,96,a,36,v,168\n"
"LG 224,40\n"
"MA a,96,a,32,v,172\n"
"MA v,236,a,32,v,176\n"
"MA v,236,a,36,v,180\n"
"s_add_u32 s80, s66, 40960\n"
"MA v,240,a,36,v,212\n"
"LG 236,40\n"
"MA v,240,a,32,v,216\n"
"MA v,0,a,32,v,220\n"
"MA v,0,a,36,a,4\n"
"MA v,16,a,36,a,8\n"
"s_add_u32 s80, s67, 40960\n"
"MA v,16,a,32,a,20\n"
"s_waitcnt vmcnt(25)\n"
"LG 232,40\n"
"MA v,4,a,44,a,20\n"
"s_waitcnt vmcnt(25)\n"
"MA v,4,a,40,a,8\n"
"MA v,20,a,40,a,4\n"
"s_add_u32 s80, s67, 45056\n"
"MA v,20,a,44,v,220\n"
"LG 244,40\n"
"MA v,8,a,44,v,216\n"
"MA v,8,a,40,v,212\n"
"MA v,24,a,40,v,180\n"
"s_add_u32 s80, s78, 45056\n"
"MA v,24,a,44,v,176\n"
"LG 240,40\n"
"MA a,100,a,44,v,172\n"
"MA a,100,a,40,v,168\n"
"MA a,104,a,40,v,164\n"
"MA a,104,a,44,v,160\n"
"s_add_u32 s80, s78, 40960\n"
"MA a,108,a,44,a,200\n"
"LG 172,40\n"
"MA a,108,a,40,a,204\n"
"MA v,12,a,40,a,192\n"
"MA v,12,a,44,a,188\n"
"s_waitcnt vmcnt(27)\n"
"s_mov_b32 s80, 93184\n"
"MA v,28,a,60,a,188\n"
"s_waitcnt vmcnt(26)\n"
"LG 176,36\n"
"MA v,28,a,180,a,192\n"
"MA a,112,a,180,a,204\n"
"MA a,112,a,60,a,200\n"
"s_mov_b32 s80, 102400\n"
"MA a,116,a,60,v,160\n"
"LG 196,36\n"
"MA a,116,a,180,v,164\n"
"L_ST20_%=:\n"
"s_cbranch_execz L_VB4_%=\n"
"MA a,120,a,180,v,168\n"
"MA a,120,a,60,v,172\n"
"s_mov_b32 s80, 94208\n"
"MA v,32,a,60,v,176\n"
"LG 184,36\n"
"MA v,32,a,180,v,180\n"
"MA v,48,a,180,v,212\n"
"MA v,48,a,60,v,216\n"
"global_load_dwordx2 v[244:245], v248, s[18:19] sc1\n"
"MA v,36,a,60,v,220\n"
"s_mov_b32 s80, 103424\n"
"MA v,36,a,180,a,4\n"
"LG 24,36\n"
"MA v,52,a,180,a,8\n"
"MA v,52,a,60,a,20\n"
"s_waitcnt vmcnt(30)\n"
"MA v,40,a,56,a,20\n"
"s_waitcnt vmcnt(29)\n"
"s_mov_b32 s80, 95232\n"
"MA v,40,v,184,a,8\n"
"LG 28,36\n"
"MA v,56,v,184,a,4\n"
"MA v,56,a,56,v,220\n"
"MA v,44,a,56,v,216\n"
"s_mov_b32 s80, 104448\n"
"MA v,44,v,184,v,212\n"
"LG 36,36\n"
"MA v,60,v,184,v,180\n"
"MA v,60,a,56,v,176\n"
"MA a,124,a,56,v,172\n"
"MA a,124,v,184,v,168\n"
"s_mov_b32 s80, 96256\n"
"MA a,128,v,184,v,164\n"
"LG 32,36\n"
"MA a,128,a,56,v,160\n"
"MA a,132,a,56,a,200\n"
"MA a,132,v,184,a,204\n"
"s_mov_b32 s80, 105472\n"
"MA v,64,v,184,a,192\n"
"LG 40,36\n"
"MA v,64,a,56,a,188\n"
"s_waitcnt vmcnt(32)\n"
"MA v,80,a,0,a,188\n"
"s_waitcnt vmcnt(31)\n"
"MA v,80,a,248,a,192\n"
"s_mov_b32 s80, 97280\n"
"MA a,136,a,248,a,204\n"
"buffer_load_dwordx4 v[184:187], v255, s[36:39], s80 offen sc1\n"
"MA a,136,a,0,a,200\n"
"MA a,140,a,0,v,160\n"
"MA a,140,a,248,v,164\n"
"MA a,144,a,248,v,168\n"
"s_mov_b32 s80, 106496\n"
"MA a,144,a,0,v,172\n"
"LG 44,36\n"
"MA v,68,a,0,v,176\n"
"MA v,68,a,248,v,180\n"
"MA v,84,a,248,v,212\n"
"s_mov_b32 s80, 98304\n"
"MA v,84,a,0,v,216\n"
"LG 180,36\n"
"MA v,72,a,0,v,220\n"
"MA v,72,a,248,a,4\n"
"MA v,88,a,248,a,8\n"
"s_mov_b32 s80, 107520\n"
"MA v,88,a,0,a,20\n"
"s_waitcnt vmcnt(33)\n"
"LG 60,36\n"
"MA v,76,a,16,a,20\n"
"s_waitcnt vmcnt(33)\n"
"MA v,76,a,52,a,8\n"
"MA v,92,a,52,a,4\n"
"MA v,92,a,16,v,220\n"
"s_mov_b32 s80, 99328\n"
"RD 56,249,0\n"
"MA v,96,a,16,v,216\n"
"LG 248,36\n"
"MA v,96,a,52,v,212\n"
"MA v,112,a,52,v,180\n"
"MA v,112,a,16,v,176\n"
"s_mov_b32 s80, 108544\n"
"MA a,148,a,16,v,172\n"
"LG 0,36\n"
"MA a,148,a,52,v,168\n"
"MA a,152,a,52,v,164\n"
"MA a,152,a,16,v,160\n"
"MA a,156,a,16,a,200\n"
"MA a,156,a,52,a,204\n"
"MA v,100,a,52,a,192\n"
"s_mov_b32 s80, 100352\n"
"MA v,100,a,16,a,188\n"
"s_waitcnt vmcnt(33)\n"
"LG 52,36\n"
"MA v,116,a,48,a,188\n"
"s_waitcnt lgkmcnt(0)\n"
"MA v,116,a,56,a,192\n"
"s_mov_b32 s80, 109568\n"
"MA a,160,a,56,a,204\n"
"LG 16,36\n"
"MA a,160,a,48,a,200\n"
"MA a,164,a,48,v,160\n"
"MA a,164,a,56,v,164\n"
"MA a,168,a,56,v,168\n"
"s_mov_b32 s80, 36864\n"
"MA a,168,a,48,v,172\n"
"buffer_load_dwordx4 v[160:163], v255, s[36:39], s80 offen sc1\n"
"MA v,104,a,48,v,176\n"
"MA v,104,a,56,v,180\n"
"MA v,120,a,56,v,212\n"
"s_mov_b32 s80, 46080\n"
"MA v,120,a,48,v,216\n"
"buffer_load_dwordx4 v[164:167], v255, s[36:39], s80 offen sc1\n"
"MA v,108,a,48,v,220\n"
"MA v,108,a,56,a,4\n"
"MA v,124,a,56,a,8\n"
"s_mov_b32 s80, 37888\n"
"MA v,124,a,48,a,20\n"
"s_waitcnt vmcnt(35)\n"
"buffer_load_dwordx4 v[168:171], v255, s[36:39], s80 offen sc1\n"
"MA v,128,a,12,v,192\n"
"s_waitcnt vmcnt(34)\n"
"MA v,128,v,196,v,200\n"
"s_waitcnt vmcnt(33)\n"
"MA a,64,v,196,v,204\n"
"s_waitcnt vmcnt(32)\n"
"MA a,64,a,12,v,208\n"
"s_waitcnt vmcnt(31)\n"
"s_mov_b32 s80, 47104\n"
"MA a,68,a,12,v,188\n"
"s_waitcnt vmcnt(30)\n"
"buffer_load_dwordx4 v[172:175], v255, s[36:39], s80 offen sc1\n"
"MA a,68,v,196,a,212\n"
"s_waitcnt vmcnt(30)\n"
"MA a,72,v,196,a,208\n"
"s_waitcnt vmcnt(29)\n"
"MA a,72,a,12,a,220\n"
"s_waitcnt vmcnt(28)\n"
"s_mov_b32 s80, 38912\n"
"MA v,132,a,12,a,216\n"
"s_waitcnt vmcnt(27)\n"
"buffer_load_dwordx4 v[176:179], v255, s[36:39], s80 offen sc1\n"
"MA v,132,v,196,a,228\n"
"s_waitcnt vmcnt(27)\n"
"MA v,136,v,196,a,224\n"
"s_waitcnt vmcnt(26)\n"
"MA v,136,a,12,a,236\n"
"s_waitcnt vmcnt(25)\n"
"s_mov_b32 s80, 48128\n"
"MA v,140,a,12,a,232\n"
"s_waitcnt vmcnt(24)\n"
"buffer_load_dwordx4 v[180:183], v255, s[36:39], s80 offen sc1\n"
"MA v,140,v,196,a,244\n"
"s_waitcnt vmcnt(24)\n"
"MA v,144,v,196,a,240\n"
"s_waitcnt vmcnt(19)\n"
"v_cmp_ne_u64 s[82:83], s[28:29], v[244:245]\n"
"MA v,144,a,12,a,172\n"
"MA v,148,a,176,a,172\n"
"s_mov_b32 s80, 39936\n"
"MA v,148,a,196,a,240\n"
"buffer_load_dwordx4 v[212:215], v255, s[36:39], s80 offen sc1\n"
"MA v,152,a,196,a,244\n"
"MA v,152,a,176,a,232\n"
"s_cmp_eq_u64 s[82:83], 0\n"
"s_cbranch_scc0 L_PB_SPIN_%=\n"
"L_PB_OK_%=:\n"
"MA v,156,a,176,a,236\n"
"s_add_u32 s80, s65, 16384\n"
"MA v,156,a,196,a,224\n"
"buffer_load_dwordx4 v[216:219], v255, s[44:47], s80 offen sc1\n"
"MA v,224,a,196,a,228\n"
"MA v,224,a,176,a,216\n"
"MA a,76,a,176,a,220\n"
"s_add_u32 s80, s65, 20480\n"
"MA a,76,a,196,a,208\n"
"buffer_load_dwordx4 v[220:223], v255, s[44:47], s80 offen sc1\n"
"MA a,80,a,196,a,212\n"
"MA a,80,a,176,v,188\n"
"MA a,84,a,176,v,208\n"
"s_add_u32 s80, s66, 20480\n"
"MA a,84,a,196,v,204\n"
"buffer_load_dwordx4 v[196:199], v255, s[44:47], s80 offen sc1\n"
"MA v,228,a,196,v,200\n"
"MA v,228,a,176,v,192\n"
"s_lshr_b32 s79, s20, 16\n"
"MA v,232,a,184,v,192\n"
"s_waitcnt vmcnt(22)\n"
"s_xor_b32 s79, s79, 152\n"
"MA v,232,a,24,v,200\n"
"s_add_u32 s80, s66, 16384\n"
"v_cmp_gt_u32 vcc, 0x100, v255\n"
"MA a,88,a,24,v,204\n"
"LG 188,44\n"
"v_mov_b32 v246, s79\n"
"MA a,88,a,184,v,208\n"
"s_nop 0\n"
"MA a,92,a,184,v,188\n"
"v_cndmask_b32 v248, v246, v248, vcc\n"
"MA a,92,a,24,a,212\n"
"s_add_u32 s80, s67, 16384\n"
"v_add_u32 v248, 24576, v248\n"
"MA a,96,a,24,a,208\n"
"LG 192,44\n"
"MA a,96,a,184,a,220\n"
"MA v,236,a,184,a,216\n"
"MA v,236,a,24,a,228\n"
"s_add_u32 s80, s67, 20480\n"
"MA v,240,a,24,a,224\n"
"LG 204,44\n"
"MA v,240,a,184,a,236\n"
"MA v,0,a,184,a,232\n"
"MA v,0,a,24,a,244\n"
"MA v,16,a,24,a,240\n"
"s_add_u32 s80, s78, 20480\n"
"MA v,16,a,184,a,172\n"
"s_waitcnt vmcnt(24)\n"
"LG 200,44\n"
"MA v,4,a,28,a,172\n"
"s_waitcnt vmcnt(24)\n"
"MA v,4,a,36,a,240\n"
"MA v,20,a,36,a,244\n"
"s_add_u32 s80, s78, 16384\n"
"MA v,20,a,28,a,232\n"
"LG 4,44\n"
"MA v,8,a,28,a,236\n"
"MA v,8,a,36,a,224\n"
"MA v,24,a,36,a,228\n"
"s_add_u32 s80, s65, 16384\n"
"MA v,24,a,28,a,216\n"
"LG 8,40\n"
"MA a,100,a,28,a,220\n"
"MA a,100,a,36,a,208\n"
"MA a,104,a,36,a,212\n"
"MA a,104,a,28,v,188\n"
"s_add_u32 s80, s65, 20480\n"
"MA a,108,a,28,v,208\n"
"LG 56,40\n"
"MA a,108,a,36,v,204\n"
"MA v,12,a,36,v,200\n"
"MA v,12,a,28,v,192\n"
"s_waitcnt vmcnt(26)\n"
"s_add_u32 s80, s66, 20480\n"
"MA v,28,a,32,v,192\n"
"s_waitcnt vmcnt(25)\n"
"LG 20,40\n"
"MA v,28,a,40,v,200\n"
"MA a,112,a,40,v,204\n"
"MA a,112,a,32,v,208\n"
"s_add_u32 s80, s66, 16384\n"
"MA a,116,a,32,v,188\n"
"LG 48,40\n"
"MA a,116,a,40,a,212\n"
"MA a,120,a,40,a,208\n"
"MA a,120,a,32,a,220\n"
"s_add_u32 s80, s67, 16384\n"
"MA v,32,a,32,a,216\n"
"LG 12,40\n"
"MA v,32,a,40,a,228\n"
"MA v,48,a,40,a,224\n"
"MA v,48,a,32,a,236\n"
"MA v,36,a,32,a,232\n"
"s_add_u32 s80, s67, 20480\n"
"MA v,36,a,40,a,244\n"
"LG 196,40\n"
"MA v,52,a,40,a,240\n"
"MA v,52,a,32,a,172\n"
"s_waitcnt vmcnt(28)\n"
"MA v,40,v,184,a,172\n"
"s_waitcnt vmcnt(27)\n"
"s_add_u32 s80, s78, 20480\n"
"MA v,40,a,44,a,240\n"
"LG 176,40\n"
"MA v,56,a,44,a,244\n"
"MA v,56,v,184,a,232\n"
"MA v,44,v,184,a,236\n"
"s_add_u32 s80, s78, 16384\n"
"MA v,44,a,44,a,224\n"
"LG 24,40\n"
"MA v,60,a,44,a,228\n"
"MA v,60,v,184,a,216\n"
"MA a,124,v,184,a,220\n"
"MA a,124,a,44,a,208\n"
"s_mov_b32 s80, 49152\n"
"MA a,128,a,44,a,212\n"
"LG 184,36\n"
"MA a,128,v,184,v,188\n"
"MA a,132,v,184,v,208\n"
"MA a,132,a,44,v,204\n"
"s_mov_b32 s80, 40960\n"
"MA v,64,a,44,v,200\n"
"LG 36,36\n"
"MA v,64,v,184,v,192\n"
"s_waitcnt vmcnt(30)\n"
"MA v,80,a,180,v,192\n"
"s_waitcnt vmcnt(29)\n"
"MA v,80,a,60,v,200\n"
"s_mov_b32 s80, 50176\n"
"MA a,136,a,60,v,204\n"
"buffer_load_dwordx4 v[184:187], v255, s[36:39], s80 offen sc1\n"
"MA a,136,a,180,v,208\n"
"MA a,140,a,180,v,188\n"
"MA a,140,a,60,a,212\n"
"MA a,144,a,60,a,208\n"
"s_mov_b32 s80, 41984\n"
"MA a,144,a,180,a,220\n"
"LG 28,36\n"
"MA v,68,a,180,a,216\n"
"MA v,68,a,60,a,228\n"
"MA v,84,a,60,a,224\n"
"s_mov_b32 s80, 51200\n"
"MA v,84,a,180,a,236\n"
"LG 40,36\n"
"MA v,72,a,180,a,232\n"
"MA v,72,a,60,a,244\n"
"MA v,88,a,60,a,240\n"
"s_mov_b32 s80, 43008\n"
"MA v,88,a,180,a,172\n"
"s_waitcnt vmcnt(31)\n"
"LG 32,36\n"
"MA v,76,a,248,a,172\n"
"s_waitcnt vmcnt(31)\n"
"MA v,76,a,0,a,240\n"
"MA v,92,a,0,a,244\n"
"s_mov_b32 s80, 52224\n"
"MA v,92,a,248,a,232\n"
"LG 44,36\n"
"MA v,96,a,248,a,236\n"
"MA v,96,a,0,a,224\n"
"MA v,112,a,0,a,228\n"
"MA v,112,a,248,a,216\n"
"s_mov_b32 s80, 44032\n"
"MA a,148,a,248,a,220\n"
"LG 60,36\n"
"MA a,148,a,0,a,208\n"
"MA a,152,a,0,a,212\n"
"MA a,152,a,248,v,188\n"
"s_mov_b32 s80, 53248\n"
"MA a,156,a,248,v,208\n"
"LG 180,36\n"
"MA a,156,a,0,v,204\n"
"MA v,100,a,0,v,200\n"
"MA v,100,a,248,v,192\n"
"s_waitcnt vmcnt(33)\n"
"s_mov_b32 s80, 45056\n"
"MA v,116,a,52,v,192\n"
"s_waitcnt vmcnt(32)\n"
"LG 0,36\n"
"MA v,116,a,16,v,200\n"
"global_load_dword a255, v255, s[34:35]\n"
"MA a,160,a,16,v,204\n"
"MA a,160,a,52,v,208\n"
"MA a,164,a,52,v,188\n"
"s_mov_b32 s80, 54272\n"
"MA a,164,a,16,a,212\n"
"buffer_load_dwordx4 v[192:195], v255, s[36:39], s80 offen sc1\n"
"MA a,168,a,16,a,208\n"
"MA a,168,a,52,a,220\n"
"MA v,104,a,52,a,216\n"
"s_mov_b32 s80, 110592\n"
"MA v,104,a,16,a,228\n"
"buffer_load_dwordx4 v[200:203], v255, s[36:39], s80 offen sc1\n"
"MA v,120,a,16,a,224\n"
"MA v,120,a,52,a,236\n"
"MA v,108,a,52,a,232\n"
"s_add_u32 s80, s65, 49152\n"
"MA v,108,a,16,a,244\n"
"buffer_load_dwordx4 v[204:207], v255, s[44:47], s80 offen sc1\n"
"MA v,124,a,16,a,240\n"
"MA v,124,a,52,a,172\n"
"s_waitcnt vmcnt(29)\n"
"MA v,128,v,160,v,216\n"
"s_waitcnt vmcnt(28)\n"
"MA v,128,v,164,v,220\n"
"s_waitcnt vmcnt(27)\n"
"s_mov_b32 s80, 119808\n"
"MA a,64,v,164,v,196\n"
"s_waitcnt vmcnt(26)\n"
"buffer_load_dwordx4 v[208:211], v255, s[36:39], s80 offen sc1\n"
"MA a,64,v,160,a,188\n"
"s_waitcnt vmcnt(26)\n"
"MA a,68,v,160,a,192\n"
"s_waitcnt vmcnt(25)\n"
"MA a,68,v,164,a,204\n"
"s_waitcnt vmcnt(24)\n"
"s_add_u32 s80, s65, 53248\n"
"MA a,72,v,164,a,200\n"
"s_waitcnt vmcnt(23)\n"
"buffer_load_dwordx4 v[188:191], v255, s[44:47], s80 offen sc1\n"
"MA a,72,v,160,a,4\n"
"s_waitcnt vmcnt(23)\n"
"MA v,132,v,160,a,8\n"
"s_waitcnt vmcnt(22)\n"
"MA v,132,v,164,a,56\n"
"s_waitcnt vmcnt(21)\n"
"s_add_u32 s80, s66, 53248\n"
"MA v,136,v,164,a,20\n"
"s_waitcnt vmcnt(20)\n"
"LG 248,44\n"
"MA v,136,v,160,a,48\n"
"s_waitcnt vmcnt(20)\n"
"MA v,140,v,160,a,12\n"
"s_waitcnt vmcnt(19)\n"
"MA v,140,v,164,a,196\n"
"s_waitcnt vmcnt(18)\n"
"s_add_u32 s80, s66, 49152\n"
"MA v,144,v,164,a,176\n"
"s_waitcnt vmcnt(17)\n"
"LG 212,44\n"
"MA v,144,v,160,a,24\n"
"MA v,148,v,168,a,24\n"
"MA v,148,v,172,a,176\n"
"MA v,152,v,172,a,196\n"
"s_add_u32 s80, s67, 49152\n"
"MA v,152,v,168,a,12\n"
"buffer_load_dwordx4 v[164:167], v255, s[44:47], s80 offen sc1\n"
"MA v,156,v,168,a,48\n"
"MA v,156,v,172,a,20\n"
"MA v,224,v,172,a,56\n"
"s_add_u32 s80, s67, 53248\n"
"MA v,224,v,168,a,8\n"
"buffer_load_dwordx4 v[160:163], v255, s[44:47], s80 offen sc1\n"
"MA a,76,v,168,a,4\n"
"MA a,76,v,172,a,200\n"
"MA a,80,v,172,a,204\n"
"s_add_u32 s80, s78, 53248\n"
"MA a,80,v,168,a,192\n"
"LG 208,44\n"
"MA a,84,v,168,a,188\n"
"MA a,84,v,172,v,196\n"
"MA v,228,v,172,v,220\n"
"MA v,228,v,168,v,216\n"
"s_add_u32 s80, s78, 49152\n"
"MA v,232,v,176,v,216\n"
"buffer_load_dwordx4 v[172:175], v255, s[44:47], s80 offen sc1\n"
"MA v,232,v,180,v,220\n"
"MA a,88,v,180,v,196\n"
"MA a,88,v,176,a,188\n"
"s_add_u32 s80, s65, 49152\n"
"MA a,92,v,176,a,192\n"
"buffer_load_dwordx4 v[168:171], v255, s[40:43], s80 offen sc1\n"
"MA a,92,v,180,a,204\n"
"MA a,96,v,180,a,200\n"
"MA a,96,v,176,a,4\n"
"s_add_u32 s80, s65, 53248\n"
"MA v,236,v,176,a,8\n"
"LG 220,40\n"
"MA v,236,v,180,a,56\n"
"MA v,240,v,180,a,20\n"
"MA v,240,v,176,a,48\n"
"MA v,0,v,176,a,12\n"
"s_add_u32 s80, s66, 53248\n"
"MA v,0,v,180,a,196\n"
"LG 216,40\n"
"MA v,16,v,180,a,176\n"
"MA v,16,v,176,a,24\n"
"MA v,4,v,212,a,24\n"
"s_waitcnt vmcnt(24)\n"
"s_add_u32 s80, s66, 49152\n"
"MA v,4,a,184,a,176\n"
"buffer_load_dwordx4 v[180:183], v255, s[40:43], s80 offen sc1\n"
"MA v,20,a,184,a,196\n"
"MA v,20,v,212,a,12\n"
"MA v,8,v,212,a,48\n"
"s_add_u32 s80, s67, 49152\n"
"MA v,8,a,184,a,20\n"
"buffer_load_dwordx4 v[176:179], v255, s[40:43], s80 offen sc1\n"
"MA v,24,a,184,a,56\n"
"MA v,24,v,212,a,8\n"
"MA a,100,v,212,a,4\n"
"s_add_u32 s80, s67, 53248\n"
"MA a,100,a,184,a,200\n"
"LG 228,40\n"
"MA a,104,a,184,a,204\n"
"global_load_dwordx2 v[244:245], v248, s[18:19] sc1\n"
"MA a,104,v,212,a,192\n"
"MA a,108,v,212,a,188\n"
"MA a,108,a,184,v,196\n"
"s_add_u32 s80, s78, 53248\n"
"MA v,12,a,184,v,220\n"
"LG 224,40\n"
"MA v,12,v,212,v,216\n"
"s_waitcnt vmcnt(28)\n"
"MA v,28,a,36,v,216\n"
"s_waitcnt vmcnt(27)\n"
"MA v,28,v,184,v,220\n"
"s_add_u32 s80, s78, 49152\n"
"MA a,112,v,184,v,196\n"
"buffer_load_dwordx4 v[212:215], v255, s[40:43], s80 offen sc1\n"
"MA a,112,a,36,a,188\n"
"MA a,116,a,36,a,192\n"
"MA a,116,v,184,a,204\n"
"s_mov_b32 s80, 111616\n"
"MA a,120,v,184,a,200\n"
"LG 236,36\n"
"MA a,120,a,36,a,4\n"
"MA v,32,a,36,a,8\n"
"MA v,32,v,184,a,56\n"
"MA v,48,v,184,a,20\n"
"s_mov_b32 s80, 120832\n"
"MA v,48,a,36,a,48\n"
"LG 232,36\n"
"MA v,36,a,36,a,12\n"
"MA v,36,v,184,a,196\n"
"MA v,52,v,184,a,176\n"
"s_mov_b32 s80, 112640\n"
"MA v,52,a,36,a,24\n"
"s_waitcnt vmcnt(29)\n"
"buffer_load_dwordx4 v[184:187], v255, s[36:39], s80 offen sc1\n"
"MA v,40,a,28,a,24\n"
"s_waitcnt vmcnt(29)\n"
"MA v,40,a,40,a,176\n"
"MA v,56,a,40,a,196\n"
"s_mov_b32 s80, 121856\n"
"MA v,56,a,28,a,12\n"
"LG 244,36\n"
"MA v,44,a,28,a,48\n"
"MA v,44,a,40,a,20\n"
"MA v,60,a,40,a,56\n"
"MA v,60,a,28,a,8\n"
"s_mov_b32 s80, 113664\n"
"MA a,124,a,28,a,4\n"
"LG 240,36\n"
"MA a,124,a,40,a,200\n"
"MA a,128,a,40,a,204\n"
"MA a,128,a,28,a,192\n"
"s_mov_b32 s80, 122880\n"
"MA a,132,a,28,a,188\n"
"LG 16,36\n"
"MA a,132,a,40,v,196\n"
"MA v,64,a,40,v,220\n"
"MA v,64,a,28,v,216\n"
"s_waitcnt vmcnt(31)\n"
"s_mov_b32 s80, 114688\n"
"MA v,80,a,32,v,216\n"
"s_waitcnt vmcnt(30)\n"
"LG 172,36\n"
"MA v,80,a,44,v,220\n"
"MA a,136,a,44,v,196\n"
"MA a,136,a,32,a,188\n"
"s_mov_b32 s80, 123904\n"
"MA a,140,a,32,a,192\n"
"LG 52,36\n"
"MA a,140,a,44,a,204\n"
"MA a,144,a,44,a,200\n"
"MA a,144,a,32,a,4\n"
"L_ST21_%=:\n"
"s_cbranch_execz L_VB5_%=\n"
"MA v,68,a,32,a,8\n"
"s_mov_b32 s80, 115712\n"
"MA v,68,a,44,a,56\n"
"LG 184,36\n"
"MA v,84,a,44,a,20\n"
"MA v,84,a,32,a,48\n"
"MA v,72,a,32,a,12\n"
"s_mov_b32 s80, 124928\n"
"MA v,72,a,44,a,196\n"
"LG 36,36\n"
"MA v,88,a,44,a,176\n"
"MA v,88,a,32,a,24\n"
"s_waitcnt vmcnt(33)\n"
"MA v,76,a,60,a,24\n"
"s_waitcnt vmcnt(32)\n"
"s_mov_b32 s80, 116736\n"
"MA v,76,a,180,a,176\n"
"LG 40,36\n"
"MA v,92,a,180,a,196\n"
"MA v,92,a,60,a,12\n"
"MA v,96,a,60,a,48\n"
"MA v,96,a,180,a,20\n"
"s_mov_b32 s80, 125952\n"
"MA v,112,a,180,a,56\n"
"LG 28,36\n"
"MA v,112,a,60,a,8\n"
"MA a,148,a,60,a,4\n"
"MA a,148,a,180,a,200\n"
"s_mov_b32 s80, 117760\n"
"MA a,152,a,180,a,204\n"
"LG 44,36\n"
"MA a,152,a,60,a,192\n"
"MA a,156,a,60,a,188\n"
"MA a,156,a,180,v,196\n"
"s_mov_b32 s80, 126976\n"
"MA v,100,a,180,v,220\n"
"LG 32,36\n"
"MA v,100,a,60,v,216\n"
"s_waitcnt vmcnt(35)\n"
"MA v,116,a,0,v,216\n"
"s_waitcnt vmcnt(33)\n"
"MA v,116,v,192,v,220\n"
"MA a,160,v,192,v,196\n"
"s_mov_b32 s80, 118784\n"
"MA a,160,a,0,a,188\n"
"buffer_load_dwordx4 v[216:219], v255, s[36:39], s80 offen sc1\n"
"MA a,164,a,0,a,192\n"
"MA a,164,v,192,a,204\n"
"MA a,168,v,192,a,200\n"
"s_mov_b32 s80, 128000\n"
"MA a,168,a,0,a,4\n"
"buffer_load_dwordx4 v[220:223], v255, s[36:39], s80 offen sc1\n"
"MA v,104,a,0,a,8\n"
"MA v,104,v,192,a,56\n"
"MA v,120,v,192,a,20\n"
"s_mov_b32 s80, 55296\n"
"MA v,120,a,0,a,48\n"
"buffer_load_dwordx4 v[196:199], v255, s[36:39], s80 offen sc1\n"
"MA v,108,a,0,a,12\n"
"MA v,108,v,192,a,196\n"
"s_waitcnt vmcnt(19)\n"
"v_cmp_ne_u64 s[82:83], s[28:29], v[244:245]\n"
"MA v,124,v,192,a,176\n"
"s_mov_b32 s80, 64512\n"
"MA v,124,a,0,a,24\n"
"buffer_load_dwordx4 v[192:195], v255, s[36:39], s80 offen sc1\n"
"MA v,128,v,200,v,204\n"
"MA v,128,v,208,v,188\n"
"MA a,64,v,208,a,248\n"
"s_cmp_eq_u64 s[82:83], 0\n"
"s_cbranch_scc0 L_PC_SPIN_%=\n"
"L_PC_OK_%=:\n"
"MA a,64,v,200,a,212\n"
"s_add_u32 s80, s65, 24576\n"
"MA a,68,v,200,v,164\n"
"LG 180,44\n"
"MA a,68,v,208,v,160\n"
"MA a,72,v,208,a,208\n"
"MA a,72,v,200,v,172\n"
"s_add_u32 s80, s65, 28672\n"
"MA v,132,v,200,v,168\n"
"LG 60,44\n"
"MA v,132,v,208,a,220\n"
"MA v,136,v,208,a,216\n"
"MA v,136,v,200,v,180\n"
"s_add_u32 s80, s66, 28672\n"
"MA v,140,v,200,v,176\n"
"LG 188,44\n"
"MA v,140,v,208,a,228\n"
"s_waitcnt vmcnt(22)\n"
"MA v,144,v,208,a,224\n"
"s_waitcnt vmcnt(21)\n"
"MA v,144,v,200,v,212\n"
"s_waitcnt vmcnt(20)\n"
"MA v,148,a,236,v,212\n"
"s_waitcnt vmcnt(19)\n"
"s_add_u32 s80, s66, 24576\n"
"MA v,148,a,232,a,224\n"
"buffer_load_dwordx4 v[208:211], v255, s[44:47], s80 offen sc1\n"
"MA v,152,a,232,a,228\n"
"MA v,152,a,236,v,176\n"
"MA v,156,a,236,v,180\n"
"s_add_u32 s80, s67, 24576\n"
"MA v,156,a,232,a,216\n"
"buffer_load_dwordx4 v[200:203], v255, s[44:47], s80 offen sc1\n"
"MA v,224,a,232,a,220\n"
"MA v,224,a,236,v,168\n"
"MA a,76,a,236,v,172\n"
"s_add_u32 s80, s67, 28672\n"
"MA a,76,a,232,a,208\n"
"LG 192,44\n"
"MA a,80,a,232,v,160\n"
"MA a,80,a,236,v,164\n"
"MA a,84,a,236,a,212\n"
"MA a,84,a,232,a,248\n"
"s_add_u32 s80, s78, 28672\n"
"MA v,228,a,232,v,188\n"
"LG 204,44\n"
"MA v,228,a,236,v,204\n"
"s_waitcnt vmcnt(22)\n"
"MA v,232,v,184,v,204\n"
"s_waitcnt vmcnt(21)\n"
"MA v,232,a,244,v,188\n"
"s_add_u32 s80, s78, 24576\n"
"MA a,88,a,244,a,248\n"
"LG 200,44\n"
"MA a,88,v,184,a,212\n"
"MA a,92,v,184,v,164\n"
"MA a,92,a,244,v,160\n"
"s_mov_b32 s80, 56320\n"
"MA a,96,a,244,a,208\n"
"LG 4,36\n"
"MA a,96,v,184,v,172\n"
"MA v,236,v,184,v,168\n"
"MA v,236,a,244,a,220\n"
"s_mov_b32 s80, 65536\n"
"MA v,240,a,244,a,216\n"
"LG 8,36\n"
"MA v,240,v,184,v,180\n"
"MA v,0,v,184,v,176\n"
"MA v,0,a,244,a,228\n"
"MA v,16,a,244,a,224\n"
"s_mov_b32 s80, 57344\n"
"MA v,16,v,184,v,212\n"
"s_waitcnt vmcnt(23)\n"
"LG 56,36\n"
"MA v,4,a,240,v,212\n"
"s_waitcnt vmcnt(23)\n"
"MA v,4,a,16,a,224\n"
"MA v,20,a,16,a,228\n"
"s_mov_b32 s80, 66560\n"
"MA v,20,a,240,v,176\n"
"buffer_load_dwordx4 v[184:187], v255, s[36:39], s80 offen sc1\n"
"MA v,8,a,240,v,180\n"
"MA v,8,a,16,a,216\n"
"MA v,24,a,16,a,220\n"
"s_mov_b32 s80, 58368\n"
"MA v,24,a,240,v,168\n"
"LG 20,36\n"
"MA a,100,a,240,v,172\n"
"MA a,100,a,16,a,208\n"
"MA a,104,a,16,v,160\n"
"MA a,104,a,240,v,164\n"
"s_mov_b32 s80, 67584\n"
"MA a,108,a,240,a,212\n"
"LG 48,36\n"
"MA a,108,a,16,a,248\n"
"MA v,12,a,16,v,188\n"
"MA v,12,a,240,v,204\n"
"s_waitcnt vmcnt(25)\n"
"s_mov_b32 s80, 59392\n"
"MA v,28,a,172,v,204\n"
"s_waitcnt vmcnt(24)\n"
"LG 12,36\n"
"MA v,28,a,52,v,188\n"
"MA a,112,a,52,a,248\n"
"MA a,112,a,172,a,212\n"
"s_mov_b32 s80, 68608\n"
"MA a,116,a,172,v,164\n"
"LG 196,36\n"
"MA a,116,a,52,v,160\n"
"MA a,120,a,52,a,208\n"
"MA a,120,a,172,v,172\n"
"MA v,32,a,172,v,168\n"
"s_mov_b32 s80, 60416\n"
"MA v,32,a,52,a,220\n"
"LG 176,36\n"
"MA v,48,a,52,a,216\n"
"MA v,48,a,172,v,180\n"
"MA v,36,a,172,v,176\n"
"s_mov_b32 s80, 69632\n"
"MA v,36,a,52,a,228\n"
"LG 24,36\n"
"MA v,52,a,52,a,224\n"
"MA v,52,a,172,v,212\n"
"s_waitcnt vmcnt(27)\n"
"MA v,40,a,184,v,212\n"
"s_waitcnt vmcnt(26)\n"
"s_mov_b32 s80, 61440\n"
"MA v,40,a,36,a,224\n"
"LG 0,36\n"
"MA v,56,a,36,a,228\n"
"MA v,56,a,184,v,176\n"
"s_and_b32 s79, s20, 0xffff\n"
"MA v,44,a,184,v,180\n"
"s_mov_b32 s80, 70656\n"
"s_xor_b32 s79, s79, 512\n"
"MA v,44,a,36,a,216\n"
"s_mul_i32 s79, s79, 1152\n"
"LG 232,36\n"
"MA v,60,a,36,a,220\n"
"s_add_u32 s79, s79, 34603008\n"
"MA v,60,a,184,v,168\n"
"s_mov_b64 s[56:57], s[18:19]\n"
"MA a,124,a,184,v,172\n"
"s_add_u32 s56, s56, s79\n"
"s_addc_u32 s57, s57, 0\n"
"MA a,124,a,36,a,208\n"
"s_mov_b32 s80, 62464\n"
"s_and_b32 s57, s57, 0xffff\n"
"MA a,128,a,36,v,160\n"
"s_mov_b32 s58, 147456\n"
"LG 236,36\n"
"MA a,128,a,184,v,164\n"
"s_mov_b32 s59, 0x20000\n"
"MA a,132,a,184,a,212\n"
"s_lshr_b32 s79, s20, 16\n"
"MA a,132,a,36,a,248\n"
"s_mov_b32 s80, 71680\n"
"s_xor_b32 s79, s79, 128\n"
"MA v,64,a,36,v,188\n"
"s_lshl_b32 s79, s79, 11\n"
"LG 244,36\n"
"MA v,64,a,184,v,204\n"
"s_waitcnt vmcnt(29)\n"
"s_sub_u32 s79, s79, 15728640\n"
"MA v,80,a,40,v,204\n"
"s_waitcnt vmcnt(28)\n"
"s_mov_b64 s[44:45], s[18:19]\n"
"MA v,80,a,28,v,188\n"
"s_mov_b32 s80, 63488\n"
"s_add_u32 s44, s44, s79\n"
"s_addc_u32 s45, s45, 0\n"
"MA a,136,a,28,a,248\n"
"s_and_b32 s45, s45, 0xffff\n"
"LG 16,36\n"
"MA a,136,a,40,a,212\n"
"s_mov_b32 s46, 65536\n"
"MA a,140,a,40,v,164\n"
"s_mov_b32 s47, 0x20000\n"
"MA a,140,a,28,v,160\n"
"MA a,144,a,28,a,208\n"
"s_mov_b32 s80, 72704\n"
"MA a,144,a,40,v,172\n"
"LG 240,36\n"
"MA v,68,a,40,v,168\n"
"MA v,68,a,28,a,220\n"
"MA v,84,a,28,a,216\n"
"s_add_u32 s80, s65, 24576\n"
"MA v,84,a,40,v,180\n"
"LG 52,40\n"
"MA v,72,a,40,v,176\n"
"MA v,72,a,28,a,228\n"
"MA v,88,a,28,a,224\n"
"s_add_u32 s80, s65, 28672\n"
"MA v,88,a,40,v,212\n"
"s_waitcnt vmcnt(30)\n"
"LG 172,40\n"
"MA v,76,a,44,v,212\n"
"s_waitcnt vmcnt(30)\n"
"MA v,76,a,32,a,224\n"
"MA v,92,a,32,a,228\n"
"MA v,92,a,44,v,176\n"
"s_add_u32 s80, s66, 28672\n"
"MA v,96,a,44,v,180\n"
"LG 36,40\n"
"MA v,96,a,32,a,216\n"
"MA v,112,a,32,a,220\n"
"MA v,112,a,44,v,168\n"
"s_add_u32 s80, s66, 24576\n"
"s_mul_i32 s79, s65, 27\n"
"MA a,148,a,44,v,172\n"
"LG 184,40\n"
"v_add_u32 v244, s79, v255\n"
"MA a,148,a,32,a,208\n"
"s_mul_i32 s79, s66, 27\n"
"MA a,152,a,32,v,160\n"
"v_add_u32 v245, s79, v255\n"
"MA a,152,a,44,v,164\n"
"s_add_u32 s80, s67, 24576\n"
"s_mul_i32 s79, s67, 27\n"
"MA a,156,a,44,a,212\n"
"LG 28,40\n"
"v_add_u32 v246, s79, v255\n"
"MA a,156,a,32,a,248\n"
"s_mul_i32 s79, s78, 27\n"
"MA v,100,a,32,v,188\n"
"v_add_u32 v247, s79, v255\n"
"MA v,100,a,44,v,204\n"
"s_waitcnt vmcnt(32)\n"
"s_add_u32 s80, s67, 28672\n"
"s_add_u32 s60, s16, s23\n"
"s_addc_u32 s61, s17, 0\n"
"MA v,116,v,216,v,204\n"
"s_waitcnt vmcnt(31)\n"
"s_and_b32 s61, s61, 0xffff\n"
"LG 40,40\n"
"MA v,116,v,220,v,188\n"
"s_mov_b32 s62, 34848\n"
"MA a,160,v,220,a,248\n"
"s_mov_b32 s63, 0x20000\n"
"MA a,160,v,216,a,212\n"
"s_and_b32 s79, s14, 0x3ff\n"
"MA a,164,v,216,v,164\n"
"s_add_u32 s80, s78, 28672\n"
"s_mul_i32 s70, s79, 9216\n"
"MA a,164,v,220,v,160\n"
"s_lshl_b32 s85, s79, 3\n"
"buffer_load_dwordx4 v[204:207], v255, s[40:43], s80 offen sc1\n"
"MA a,168,v,220,a,208\n"
"s_add_u32 s85, s85, 24576\n"
"MA a,168,v,216,v,172\n"
"s_add_u32 s54, s85, 8\n"
"MA v,104,v,216,v,168\n"
"s_add_u32 s80, s78, 24576\n"
"s_xor_b32 s79, s79, 4\n"
"MA v,104,v,220,a,220\n"
"s_mul_i32 s71, s79, 9216\n"
"buffer_load_dwordx4 v[188:191], v255, s[40:43], s80 offen sc1\n"
"MA v,120,v,220,a,216\n"
"s_xor_b32 s79, s85, 32\n"
"MA v,120,v,216,v,180\n"
"v_mov_b32 v248, s79\n"
"MA v,108,v,216,v,176\n"
"s_mov_b32 s80, 129024\n"
"MA v,108,v,220,a,228\n"
"buffer_load_dwordx4 v[180:183], v255, s[36:39], s80 offen sc1\n"
"MA v,124,v,220,a,224\n"
"MA v,124,v,216,v,212\n"
"s_waitcnt vmcnt(32)\n"
"s_waitcnt lgkmcnt(0)\n"
"MA v,128,v,196,a,180\n"
"s_waitcnt vmcnt(31)\n"
"MA v,128,v,192,a,60\n"
"s_waitcnt vmcnt(30)\n"
"s_barrier\n"
"s_add_u32 s80, s66, 57344\n"
"MA a,64,v,192,a,188\n"
"s_waitcnt vmcnt(29)\n"
"buffer_load_dwordx4 v[220:223], v255, s[40:43], s80 offen sc1\n"
"MA a,64,v,196,v,208\n"
"s_waitcnt vmcnt(29)\n"
"MA a,68,v,196,v,200\n"
"s_waitcnt vmcnt(28)\n"
"MA a,68,v,192,a,192\n"
"s_waitcnt vmcnt(27)\n"
"s_mov_b32 s80, 138240\n"
"MA a,72,v,192,a,204\n"
"s_waitcnt vmcnt(26)\n"
"buffer_load_dwordx4 v[212:215], v255, s[36:39], s80 offen sc1\n"
"MA a,72,v,196,a,200\n"
"s_waitcnt vmcnt(26)\n"
"MA a,76,a,4,a,200\n"
"s_waitcnt vmcnt(25)\n"
"MA a,76,a,8,a,204\n"
"s_add_u32 s80, s66, 61440\n"
"MA a,80,a,8,a,192\n"
"buffer_load_dwordx4 v[216:219], v255, s[40:43], s80 offen sc1\n"
"MA a,80,a,4,v,200\n"
"MA a,84,a,4,v,208\n"
"MA a,84,a,8,a,188\n"
"MA v,228,a,8,a,60\n"
"s_mov_b32 s80, 129024\n"
"MA v,228,a,4,a,180\n"
"s_waitcnt vmcnt(25)\n"
"LG 32,56\n"
"MA v,232,a,56,a,180\n"
"s_waitcnt vmcnt(25)\n"
"ds_write_b128 v245, a[64:67] offset:0\n"
"MA v,232,v,184,a,60\n"
"MA a,88,v,184,a,188\n"
"s_add_u32 s80, s66, 57344\n"
"MA a,88,a,56,v,208\n"
"LG 44,44\n"
"MA a,92,a,56,v,200\n"
"ds_write_b128 v246, a[68:71] offset:9216\n"
"MA a,92,v,184,a,192\n"
"MA a,96,v,184,a,204\n"
"s_mov_b32 s80, 138240\n"
"MA a,96,a,56,a,200\n"
"s_waitcnt vmcnt(25)\n"
"LG 248,56\n"
"MA a,100,a,20,a,200\n"
"s_waitcnt vmcnt(25)\n"
"ds_write_b128 v247, a[72:75] offset:18432\n"
"MA a,100,a,48,a,204\n"
"MA a,104,a,48,a,192\n"
"s_add_u32 s80, s66, 61440\n"
"MA a,104,a,20,v,200\n"
"LG 212,44\n"
"MA a,108,a,20,v,208\n"
"ds_write_b128 v245, a[84:87] offset:1024\n"
"MA a,108,a,48,a,188\n"
"MA v,12,a,48,a,60\n"
"MA v,12,a,20,a,180\n"
"s_waitcnt vmcnt(25)\n"
"MA v,28,a,12,a,180\n"
"s_waitcnt vmcnt(24)\n"
"s_add_u32 s80, s67, 61440\n"
"ds_write_b128 v246, a[80:83] offset:10240\n"
"MA v,28,a,196,a,60\n"
"LG 208,44\n"
"MA a,112,a,196,a,188\n"
"MA a,112,a,12,v,208\n"
"MA a,116,a,12,v,200\n"
"s_add_u32 s80, s67, 57344\n"
"ds_write_b128 v247, a[76:79] offset:19456\n"
"MA a,116,a,196,a,192\n"
"LG 220,44\n"
"MA a,120,a,196,a,204\n"
"MA a,120,a,12,a,200\n"
"s_waitcnt vmcnt(25)\n"
"MA a,124,a,176,a,200\n"
"s_waitcnt vmcnt(24)\n"
"s_add_u32 s80, s67, 61440\n"
"ds_write_b128 v245, a[88:91] offset:2048\n"
"MA a,124,a,24,a,204\n"
"LG 216,40\n"
"MA a,128,a,24,a,192\n"
"MA a,128,a,176,v,200\n"
"MA a,132,a,176,v,208\n"
"s_add_u32 s80, s67, 57344\n"
"ds_write_b128 v246, a[92:95] offset:11264\n"
"MA a,132,a,24,a,188\n"
"LG 228,40\n"
"MA v,64,a,24,a,60\n"
"MA v,64,a,176,a,180\n"
"s_waitcnt vmcnt(25)\n"
"MA v,80,a,0,a,180\n"
"s_waitcnt vmcnt(24)\n"
"s_add_u32 s80, s78, 57344\n"
"ds_write_b128 v247, a[96:99] offset:20480\n"
"MA v,80,a,232,a,60\n"
"LG 224,40\n"
"MA a,136,a,232,a,188\n"
"MA a,136,a,0,v,208\n"
"MA a,140,a,0,v,200\n"
"ds_write_b128 v245, a[108:111] offset:3072\n"
"MA a,140,a,232,a,192\n"
"s_add_u32 s80, s78, 61440\n"
"MA a,144,a,232,a,204\n"
"LG 64,40\n"
"MA a,144,a,0,a,200\n"
"s_waitcnt vmcnt(25)\n"
"MA a,148,a,236,a,200\n"
"s_waitcnt vmcnt(24)\n"
"ds_write_b128 v246, a[104:107] offset:12288\n"
"MA a,148,a,244,a,204\n"
"s_waitcnt lgkmcnt(2)\n"
"s_add_u32 s80, s78, 57344\n"
"MA a,152,a,244,a,192\n"
"s_barrier\n"
"LG 68,44\n"
"MA a,152,a,236,v,200\n"
"ds_read_b128 v[160:163], v244 offset:0\n"
"MA a,156,a,236,v,208\n"
"ds_write_b128 v247, a[100:103] offset:21504\n"
"MA a,156,a,244,a,188\n"
"s_add_u32 s80, s78, 61440\n"
"MA v,100,a,244,a,60\n"
"LG 72,44\n"
"MA v,100,a,236,a,180\n"
"s_waitcnt vmcnt(25)\n"
"ds_read_b128 v[164:167], v244 offset:9216\n"
"MA v,116,a,16,a,180\n"
"s_waitcnt vmcnt(24)\n"
"ds_write_b128 v245, a[112:115] offset:4096\n"
"MA v,116,a,240,a,60\n"
"MA a,160,a,240,a,188\n"
"s_waitcnt lgkmcnt(3)\n"
"AD 128,160\n"
"AD 129,161\n"
"MA a,160,a,16,v,208\n"
"ds_read_b128 v[168:171], v244 offset:18432\n"
"AD 130,162\n"
"MA a,164,a,16,v,200\n"
"s_add_u32 s80, s65, 61440\n"
"ds_write_b128 v246, a[116:119] offset:13312\n"
"MA a,164,a,240,a,192\n"
"buffer_load_dwordx4 v[208:211], v255, s[44:47], s80 offen sc1\n"
"AD 131,163\n"
"MA a,168,a,240,a,204\n"
"s_waitcnt lgkmcnt(3)\n"
"AD 128,164\n"
"AD 129,165\n"
"MA a,168,a,16,a,200\n"
"s_waitcnt vmcnt(24)\n"
"ds_read_b128 v[172:175], v244 offset:1024\n"
"AD 130,166\n"
"MA v,132,v,196,a,52\n"
"s_waitcnt vmcnt(23)\n"
"s_add_u32 s80, s65, 57344\n"
"ds_write_b128 v247, a[120:123] offset:22528\n"
"MA v,132,v,192,a,172\n"
"s_waitcnt vmcnt(22)\n"
"buffer_load_dwordx4 v[200:203], v255, s[44:47], s80 offen sc1\n"
"AD 131,167\n"
"MA v,136,v,192,a,36\n"
"s_waitcnt vmcnt(22) lgkmcnt(3)\n"
"AD 128,168\n"
"AD 129,169\n"
"MA v,136,v,196,a,184\n"
"s_waitcnt vmcnt(21)\n"
"ds_read_b128 v[176:179], v244 offset:10240\n"
"AD 130,170\n"
"MA v,140,v,196,a,28\n"
"s_waitcnt vmcnt(20)\n"
"s_add_u32 s80, s65, 61440\n"
"ds_write_b128 v245, a[132:135] offset:5120\n"
"MA v,140,v,192,a,40\n"
"s_waitcnt vmcnt(19)\n"
"LG 84,40\n"
"AD 131,171\n"
"MA v,144,v,192,v,204\n"
"s_waitcnt vmcnt(19) lgkmcnt(3)\n"
"AD 228,172\n"
"AD 229,173\n"
"CV 128,128,129\n"
"MA v,144,v,196,v,188\n"
"ds_read_b128 v[160:163], v244 offset:19456\n"
"AD 230,174\n"
"CV 129,130,131\n"
"MA v,148,a,4,v,188\n"
"s_add_u32 s80, s65, 57344\n"
"ds_write_b128 v246, a[128:131] offset:14336\n"
"MA v,148,a,8,v,204\n"
"buffer_load_dwordx4 v[192:195], v255, s[40:43], s80 offen sc1\n"
"AD 231,175\n"
"MA v,152,a,8,a,40\n"
"s_waitcnt lgkmcnt(3)\n"
"AD 228,176\n"
"AD 229,177\n"
"MA v,152,a,4,a,28\n"
"ds_read_b128 v[164:167], v244 offset:2048\n"
"AD 230,178\n"
"MA v,156,a,4,a,184\n"
"s_mov_b32 s80, 130048\n"
"ds_write_b128 v247, a[124:127] offset:23552\n"
"MA v,156,a,8,a,36\n"
"buffer_load_dwordx4 v[196:199], v255, s[36:39], s80 offen sc1\n"
"AD 231,179\n"
"MA v,224,a,8,a,172\n"
"s_waitcnt lgkmcnt(3)\n"
"AD 228,160\n"
"AD 229,161\n"
"MA v,224,a,4,a,52\n"
"ds_read_b128 v[168:171], v244 offset:11264\n"
"AD 230,162\n"
"MA v,236,a,56,a,52\n"
"ds_write_b128 v245, a[136:139] offset:6144\n"
"MA v,236,v,184,a,172\n"
"AD 231,163\n"
"MA v,240,v,184,a,36\n"
"s_waitcnt lgkmcnt(3)\n"
"AD 232,164\n"
"AD 233,165\n"
"CV 130,228,229\n"
"MA v,240,a,56,a,184\n"
"ds_read_b128 v[172:175], v244 offset:20480\n"
"AD 234,166\n"
"CV 131,230,231\n"
"MA v,0,a,56,a,28\n"
"s_add_u32 s80, s70, 0\n"
"ds_write_b128 v246, a[140:143] offset:15360\n"
"MA v,0,v,184,a,40\n"
"s_waitcnt lgkmcnt(4)\n"
"L_ST22_%=:\n"
"s_cbranch_execz L_VB5_%=\n"
"buffer_store_dwordx4 v[128:131], v255, s[48:51], s80 offen sc1\n"
"AD 235,167\n"
"MA v,16,v,184,v,204\n"
"s_waitcnt lgkmcnt(3)\n"
"s_barrier\n"
"AD 232,168\n"
"AD 233,169\n"
"MA v,16,a,56,v,188\n"
"ds_read_b128 v[176:179], v244 offset:3072\n"
"AD 234,170\n"
"MA v,4,a,20,v,188\n"
"s_mov_b32 s80, 139264\n"
"ds_write_b128 v247, a[144:147] offset:24576\n"
"MA v,4,a,48,v,204\n"
"buffer_load_dwordx4 v[184:187], v255, s[36:39], s80 offen sc1\n"
"AD 235,171\n"
"MA v,20,a,48,a,40\n"
"s_waitcnt lgkmcnt(3)\n"
"AD 232,172\n"
"AD 233,173\n"
"MA v,20,a,20,a,28\n"
"ds_read_b128 v[160:163], v244 offset:12288\n"
"AD 234,174\n"
"MA v,8,a,20,a,184\n"
"s_mov_b32 s80, 130048\n"
"ds_write_b128 v245, a[156:159] offset:7168\n"
"MA v,8,a,48,a,36\n"
"LG 80,56\n"
"AD 235,175\n"
"MA v,24,a,48,a,172\n"
"s_waitcnt lgkmcnt(3)\n"
"AD 12,176\n"
"AD 13,177\n"
"CV 232,232,233\n"
"MA v,24,a,20,a,52\n"
"ds_read_b128 v[164:167], v244 offset:21504\n"
"AD 14,178\n"
"CV 233,234,235\n"
"MA v,32,a,12,a,52\n"
"s_mov_b32 s80, 139264\n"
"ds_write_b128 v246, a[152:155] offset:16384\n"
"MA v,32,a,196,a,172\n"
"LG 76,56\n"
"AD 15,179\n"
"MA v,48,a,196,a,36\n"
"s_waitcnt lgkmcnt(3)\n"
"AD 12,160\n"
"AD 13,161\n"
"MA v,48,a,12,a,184\n"
"ds_read_b128 v[168:171], v244 offset:4096\n"
"AD 14,162\n"
"MA v,36,a,12,a,28\n"
"s_mov_b32 s80, 131072\n"
"ds_write_b128 v247, a[148:151] offset:25600\n"
"MA v,36,a,196,a,40\n"
"LG 88,36\n"
"AD 15,163\n"
"MA v,52,a,196,v,204\n"
"s_waitcnt lgkmcnt(3)\n"
"AD 12,164\n"
"AD 13,165\n"
"MA v,52,a,12,v,188\n"
"ds_read_b128 v[172:175], v244 offset:13312\n"
"AD 14,166\n"
"MA v,40,a,176,v,188\n"
"s_mov_b32 s80, 140288\n"
"ds_write_b128 v245, a[160:163] offset:8192\n"
"MA v,40,a,24,v,204\n"
"LG 92,36\n"
"AD 15,167\n"
"MA v,56,a,24,a,40\n"
"s_waitcnt lgkmcnt(3)\n"
"AD 28,168\n"
"AD 29,169\n"
"CV 234,12,13\n"
"MA v,56,a,176,a,28\n"
"ds_read_b128 v[176:179], v244 offset:22528\n"
"AD 30,170\n"
"CV 235,14,15\n"
"MA v,44,a,176,a,184\n"
"s_add_u32 s80, s70, 1024\n"
"ds_write_b128 v246, a[164:167] offset:17408\n"
"MA v,44,a,24,a,36\n"
"buffer_store_dwordx4 v[232:235], v255, s[48:51], s80 offen sc1\n"
"AD 31,171\n"
"MA v,60,a,24,a,172\n"
"s_waitcnt lgkmcnt(3)\n"
"AD 28,172\n"
"AD 29,173\n"
"MA v,60,a,176,a,52\n"
"ds_read_b128 v[160:163], v244 offset:5120\n"
"AD 30,174\n"
"MA v,68,a,0,a,52\n"
"s_mov_b32 s80, 131072\n"
"ds_write_b128 v247, a[168:171] offset:26624\n"
"MA v,68,a,232,a,172\n"
"LG 96,56\n"
"AD 31,175\n"
"MA v,84,a,232,a,36\n"
"s_waitcnt lgkmcnt(3)\n"
"AD 28,176\n"
"AD 29,177\n"
"MA v,84,a,0,a,184\n"
"s_mov_b32 s80, 140288\n"
"ds_read_b128 v[164:167], v244 offset:14336\n"
"AD 30,178\n"
"MA v,72,a,0,a,28\n"
"LG 108,56\n"
"AD 31,179\n"
"MA v,72,a,232,a,40\n"
"CV 28,28,29\n"
"MA v,88,a,232,v,204\n"
"s_waitcnt lgkmcnt(2)\n"
"ds_read_b128 v[168:171], v244 offset:23552\n"
"AD 64,160\n"
"CV 29,30,31\n"
"MA v,88,a,0,v,188\n"
"AD 65,161\n"
"AD 66,162\n"
"MA v,76,a,236,v,188\n"
"s_mov_b32 s80, 132096\n"
"AD 67,163\n"
"MA v,76,a,244,v,204\n"
"s_waitcnt lgkmcnt(2)\n"
"LG 104,36\n"
"MA v,92,a,244,a,40\n"
"s_waitcnt lgkmcnt(1)\n"
"s_barrier\n"
"AD 64,164\n"
"AD 65,165\n"
"MA v,92,a,236,a,28\n"
"ds_read_b128 v[172:175], v244 offset:6144\n"
"AD 66,166\n"
"MA v,96,a,236,a,184\n"
"AD 67,167\n"
"MA v,96,a,244,a,36\n"
"s_waitcnt lgkmcnt(1)\n"
"AD 64,168\n"
"AD 65,169\n"
"MA v,112,a,244,a,172\n"
"s_mov_b32 s80, 141312\n"
"ds_read_b128 v[176:179], v244 offset:15360\n"
"AD 66,170\n"
"MA v,112,a,236,a,52\n"
"LG 100,36\n"
"AD 67,171\n"
"MA v,104,a,16,a,52\n"
"CV 30,64,65\n"
"MA v,104,a,240,a,172\n"
"ds_read_b128 v[160:163], v244 offset:24576\n"
"CV 31,66,67\n"
"MA v,120,a,240,a,36\n"
"s_waitcnt lgkmcnt(2)\n"
"s_add_u32 s80, s70, 2048\n"
"AD 80,172\n"
"AD 81,173\n"
"MA v,120,a,16,a,184\n"
"buffer_store_dwordx4 v[28:31], v255, s[48:51], s80 offen sc1\n"
"AD 82,174\n"
"AD 83,175\n"
"MA v,108,a,16,a,28\n"
"s_mov_b32 s80, 132096\n"
"ds_read_b128 v[164:167], v244 offset:7168\n"
"MA v,108,a,240,a,40\n"
"s_waitcnt lgkmcnt(2)\n"
"LG 180,56\n"
"AD 80,176\n"
"AD 81,177\n"
"MA v,124,a,240,v,204\n"
"AD 82,178\n"
"AD 83,179\n"
"MA v,124,a,16,v,188\n"
"s_waitcnt vmcnt(32)\n"
"ds_read_b128 v[168:171], v244 offset:16384\n"
"MA v,136,v,180,v,220\n"
"s_waitcnt vmcnt(30) lgkmcnt(2)\n"
"AD 80,160\n"
"AD 81,161\n"
"MA v,136,v,212,v,216\n"
"s_waitcnt vmcnt(28)\n"
"AD 82,162\n"
"AD 83,163\n"
"MA v,136,a,32,a,44\n"
"s_waitcnt vmcnt(26)\n"
"s_mov_b32 s80, 141312\n"
"ds_read_b128 v[172:175], v244 offset:25600\n"
"CV 80,80,81\n"
"MA v,136,a,248,a,212\n"
"s_waitcnt vmcnt(25) lgkmcnt(2)\n"
"buffer_load_dwordx4 v[204:207], v255, s[56:59], s80 offen sc1\n"
"AD 100,164\n"
"AD 101,165\n"
"CV 81,82,83\n"
"MA v,140,a,248,a,208\n"
"s_waitcnt vmcnt(25)\n"
"AD 102,166\n"
"AD 103,167\n"
"MA v,140,a,32,a,220\n"
"s_waitcnt vmcnt(24)\n"
"ds_read_b128 v[176:179], v244 offset:8192\n"
"MA v,140,v,212,a,216\n"
"s_waitcnt vmcnt(23) lgkmcnt(2)\n"
"AD 100,168\n"
"AD 101,169\n"
"MA v,140,v,180,a,228\n"
"s_waitcnt vmcnt(22)\n"
"AD 102,170\n"
"AD 103,171\n"
"MA v,144,v,180,a,224\n"
"s_waitcnt vmcnt(21)\n"
"s_mov_b32 s80, 133120\n"
"ds_read_b128 v[160:163], v244 offset:17408\n"
"MA v,144,v,212,a,64\n"
"s_waitcnt vmcnt(20) lgkmcnt(2)\n"
"buffer_load_dwordx4 v[188:191], v255, s[36:39], s80 offen sc1\n"
"AD 100,172\n"
"AD 101,173\n"
"MA v,144,a,32,a,68\n"
"s_waitcnt vmcnt(20)\n"
"AD 102,174\n"
"AD 103,175\n"
"MA v,144,a,248,a,72\n"
"s_waitcnt vmcnt(19)\n"
"ds_read_b128 v[164:167], v244 offset:26624\n"
"CV 82,100,101\n"
"MA v,132,a,248,v,208\n"
"s_waitcnt vmcnt(18) lgkmcnt(2)\n"
"AD 116,176\n"
"AD 117,177\n"
"CV 83,102,103\n"
"MA v,132,a,32,v,200\n"
"s_waitcnt vmcnt(17)\n"
"s_add_u32 s80, s70, 3072\n"
"AD 118,178\n"
"AD 119,179\n"
"MA v,132,v,212,a,84\n"
"s_waitcnt vmcnt(16)\n"
"buffer_store_dwordx4 v[80:83], v255, s[48:51], s80 offen sc1\n"
"MA v,132,v,180,v,192\n"
"s_waitcnt vmcnt(16) lgkmcnt(1)\n"
"s_mov_b32 s80, 142336\n"
"AD 116,160\n"
"AD 117,161\n"
"MA v,156,v,196,v,220\n"
"s_waitcnt vmcnt(14)\n"
"buffer_load_dwordx4 v[212:215], v255, s[36:39], s80 offen sc1\n"
"AD 118,162\n"
"AD 119,163\n"
"MA v,156,v,184,v,216\n"
"s_waitcnt vmcnt(14)\n"
"MA v,156,a,80,a,44\n"
"s_waitcnt vmcnt(13) lgkmcnt(0)\n"
"AD 116,164\n"
"AD 117,165\n"
"MA v,156,a,76,a,212\n"
"s_barrier\n"
"s_mov_b32 s80, 133120\n"
"AD 118,166\n"
"AD 119,167\n"
"MA v,152,a,76,a,208\n"
"buffer_load_dwordx4 v[180:183], v255, s[56:59], s80 offen sc1\n"
"CV 116,116,117\n"
"MA v,152,a,80,a,220\n"
"CV 117,118,119\n"
"MA v,152,v,184,a,216\n"
"s_add_u32 s80, s70, 4096\n"
"MA v,152,v,196,a,228\n"
"buffer_store_dwordx4 v[116:119], v255, s[48:51], s80 offen sc1\n"
"MA v,148,v,196,a,224\n"
"s_mov_b32 s80, 142336\n"
"ds_write_b128 v245, v[136:139] offset:0\n"
"MA v,148,v,184,a,64\n"
"LG 60,56\n"
"MA v,148,a,80,a,68\n"
"MA v,148,a,76,a,72\n"
"MA v,224,a,76,v,208\n"
"s_mov_b32 s80, 134144\n"
"ds_write_b128 v246, v[140:143] offset:9216\n"
"MA v,224,a,80,v,200\n"
"LG 112,36\n"
"MA v,224,v,184,a,84\n"
"s_and_b32 s73, s14, 0x3ff\n"
"MA v,224,v,196,v,192\n"
"s_waitcnt vmcnt(16)\n"
"MA v,240,a,88,v,220\n"
"s_waitcnt vmcnt(15)\n"
"s_mov_b32 s80, 143360\n"
"ds_write_b128 v247, v[144:147] offset:18432\n"
"MA v,240,a,92,v,216\n"
"s_waitcnt vmcnt(13)\n"
"buffer_load_dwordx4 v[184:187], v255, s[36:39], s80 offen sc1\n"
"MA v,240,a,96,a,44\n"
"s_waitcnt vmcnt(13)\n"
"MA v,240,a,108,a,212\n"
"MA v,0,a,108,a,208\n"
"s_mov_b32 s80, 134144\n"
"ds_write_b128 v245, v[156:159] offset:1024\n"
"MA v,0,a,96,a,220\n"
"buffer_load_dwordx4 v[196:199], v255, s[56:59], s80 offen sc1\n"
"MA v,0,a,92,a,216\n"
"MA v,0,a,88,a,228\n"
"MA v,16,a,88,a,224\n"
"s_mov_b32 s80, 143360\n"
"ds_write_b128 v246, v[152:155] offset:10240\n"
"MA v,16,a,92,a,64\n"
"LG 188,56\n"
"MA v,16,a,96,a,68\n"
"s_cmp_eq_u32 s73, 0\n"
"s_cbranch_scc1 L_HNL_%=\n"
"L_HBNL_%=:\n"
"MA v,16,a,108,a,72\n"
"MA v,236,a,108,v,208\n"
"ds_write_b128 v247, v[148:151] offset:19456\n"
"MA v,236,a,96,v,200\n"
"s_mov_b32 s80, 135168\n"
"MA v,236,a,92,a,84\n"
"LG 192,36\n"
"MA v,236,a,88,v,192\n"
"s_waitcnt vmcnt(15)\n"
"MA v,8,a,104,v,220\n"
"s_waitcnt vmcnt(14)\n"
"ds_write_b128 v245, v[240:243] offset:2048\n"
"MA v,8,a,100,v,216\n"
"s_waitcnt vmcnt(12)\n"
"s_mov_b32 s80, 144384\n"
"MA v,8,a,180,a,44\n"
"s_waitcnt vmcnt(11)\n"
"LG 116,36\n"
"MA v,8,v,204,a,212\n"
"MA v,20,v,204,a,208\n"
"ds_write_b128 v246, v[0:3] offset:11264\n"
"MA v,20,a,180,a,220\n"
"s_mov_b32 s80, 135168\n"
"MA v,20,a,100,a,216\n"
"LG 204,56\n"
"MA v,20,a,104,a,228\n"
"MA v,4,a,104,a,224\n"
"ds_write_b128 v247, v[16:19] offset:20480\n"
"MA v,4,a,100,a,64\n"
"MA v,4,a,180,a,68\n"
"s_mov_b32 s80, 144384\n"
"MA v,4,v,204,a,72\n"
"LG 200,56\n"
"MA v,24,v,204,v,208\n"
"ds_write_b128 v245, v[8:11] offset:3072\n"
"MA v,24,a,180,v,200\n"
"MA v,24,a,100,a,84\n"
"s_mov_b32 s80, 136192\n"
"MA v,24,a,104,v,192\n"
"s_waitcnt vmcnt(13)\n"
"buffer_load_dwordx4 v[204:207], v255, s[36:39], s80 offen sc1\n"
"MA v,48,v,188,v,220\n"
"s_waitcnt vmcnt(12)\n"
"ds_write_b128 v246, v[20:23] offset:12288\n"
"MA v,48,v,212,v,216\n"
"s_waitcnt vmcnt(11)\n"
"MA v,48,v,180,a,44\n"
"s_waitcnt vmcnt(9)\n"
"s_mov_b32 s80, 145408\n"
"MA v,48,a,60,a,212\n"
"LG 120,36\n"
"MA v,36,a,60,a,208\n"
"ds_write_b128 v247, v[4:7] offset:21504\n"
"MA v,36,v,180,a,220\n"
"MA v,36,v,212,a,216\n"
"s_mov_b32 s80, 136192\n"
"MA v,36,v,188,a,228\n"
"LG 132,56\n"
"MA v,52,v,188,a,224\n"
"MA v,52,v,212,a,64\n"
"MA v,52,v,180,a,68\n"
"MA v,52,a,60,a,72\n"
"MA v,32,a,60,v,208\n"
"s_mov_b32 s80, 145408\n"
"ds_write_b128 v245, v[48:51] offset:4096\n"
"MA v,32,v,180,v,200\n"
"s_waitcnt lgkmcnt(1)\n"
"LG 128,56\n"
"MA v,32,v,212,a,84\n"
"s_barrier\n"
"MA v,32,v,188,v,192\n"
"s_waitcnt vmcnt(11)\n"
"ds_read_b128 v[136:139], v244 offset:0\n"
"MA v,44,a,112,v,220\n"
"s_waitcnt vmcnt(10)\n"
"s_mov_b32 s80, 137216\n"
"ds_write_b128 v246, v[36:39] offset:13312\n"
"MA v,44,v,184,v,216\n"
"s_waitcnt vmcnt(9)\n"
"buffer_load_dwordx4 v[180:183], v255, s[36:39], s80 offen sc1\n"
"MA v,44,v,196,a,44\n"
"s_waitcnt vmcnt(9)\n"
"MA v,44,a,188,a,212\n"
"ds_read_b128 v[140:143], v244 offset:9216\n"
"MA v,56,a,188,a,208\n"
"s_mov_b32 s80, 146432\n"
"ds_write_b128 v247, v[52:55] offset:22528\n"
"MA v,56,v,196,a,220\n"
"buffer_load_dwordx4 v[212:215], v255, s[36:39], s80 offen sc1\n"
"MA v,56,v,184,a,216\n"
"s_waitcnt lgkmcnt(3)\n"
"s_waitcnt vmcnt(63)\n"
"buffer_store_dwordx2 a[252:253], v255, s[52:55], s85 offen sc1\n"
"AD 132,136\n"
"AD 133,137\n"
"MA v,56,a,112,a,228\n"
"ds_read_b128 v[144:147], v244 offset:18432\n"
"AD 134,138\n"
"MA v,40,a,112,a,224\n"
"s_mov_b32 s80, 137216\n"
"AD 135,139\n"
"MA v,40,v,184,a,64\n"
"buffer_load_dwordx4 v[188:191], v255, s[56:59], s80 offen sc1\n"
"MA v,40,v,196,a,68\n"
"s_waitcnt lgkmcnt(2)\n"
"AD 132,140\n"
"AD 133,141\n"
"MA v,40,a,188,a,72\n"
"ds_read_b128 v[156:159], v244 offset:1024\n"
"AD 134,142\n"
"MA v,60,a,188,v,208\n"
"s_mov_b32 s80, 146432\n"
"ds_write_b128 v245, v[44:47] offset:5120\n"
"MA v,60,v,196,v,200\n"
"LG 124,56\n"
"AD 135,143\n"
"MA v,60,v,184,a,84\n"
"s_waitcnt lgkmcnt(2)\n"
"AD 132,144\n"
"AD 133,145\n"
"MA v,60,a,112,v,192\n"
"s_waitcnt vmcnt(12)\n"
"ds_read_b128 v[152:155], v244 offset:10240\n"
"AD 134,146\n"
"MA v,84,a,192,v,220\n"
"s_waitcnt vmcnt(11)\n"
"ds_write_b128 v246, v[56:59] offset:14336\n"
"MA v,84,a,116,v,216\n"
"s_waitcnt vmcnt(10)\n"
"AD 135,147\n"
"MA v,84,a,204,a,44\n"
"s_waitcnt vmcnt(9) lgkmcnt(3)\n"
"AD 224,156\n"
"AD 225,157\n"
"MA v,84,a,200,a,212\n"
"ds_read_b128 v[148:151], v244 offset:19456\n"
"AD 226,158\n"
"MA v,72,a,200,a,208\n"
"ds_write_b128 v247, v[40:43] offset:23552\n"
"MA v,72,a,204,a,220\n"
"global_load_dwordx2 v[160:161], v248, s[18:19] sc1\n"
"AD 227,159\n"
"MA v,72,a,116,a,216\n"
"s_waitcnt lgkmcnt(3)\n"
"AD 224,152\n"
"AD 225,153\n"
"MA v,72,a,192,a,228\n"
"ds_read_b128 v[240:243], v244 offset:2048\n"
"AD 226,154\n"
"MA v,88,a,192,a,224\n"
"AD 227,155\n"
"MA v,88,a,116,a,64\n"
"MA v,88,a,204,a,68\n"
"s_waitcnt lgkmcnt(2)\n"
"AD 224,148\n"
"AD 225,149\n"
"MA v,88,a,200,a,72\n"
"ds_read_b128 v[0:3], v244 offset:11264\n"
"AD 226,150\n"
"MA v,68,a,200,v,208\n"
"ds_write_b128 v245, v[84:87] offset:6144\n"
"MA v,68,a,204,v,200\n"
"AD 227,151\n"
"MA v,68,a,116,a,84\n"
"s_waitcnt lgkmcnt(2)\n"
"AD 236,240\n"
"AD 237,241\n"
"MA v,68,a,192,v,192\n"
"s_waitcnt vmcnt(9)\n"
"ds_read_b128 v[16:19], v244 offset:20480\n"
"AD 238,242\n"
"MA v,96,v,204,v,220\n"
"s_waitcnt vmcnt(8)\n"
"ds_write_b128 v246, v[72:75] offset:15360\n"
"MA v,96,a,120,v,216\n"
"s_waitcnt vmcnt(7)\n"
"AD 239,243\n"
"MA v,96,a,132,a,44\n"
"s_waitcnt vmcnt(6) lgkmcnt(3)\n"
"AD 236,0\n"
"AD 237,1\n"
"MA v,96,a,128,a,212\n"
"ds_read_b128 v[8:11], v244 offset:3072\n"
"AD 238,2\n"
"MA v,92,a,128,a,208\n"
"ds_write_b128 v247, v[88:91] offset:24576\n"
"MA v,92,a,132,a,220\n"
"AD 239,3\n"
"MA v,92,a,120,a,216\n"
"s_waitcnt lgkmcnt(3)\n"
"AD 236,16\n"
"AD 237,17\n"
"MA v,92,v,204,a,228\n"
"ds_read_b128 v[20:23], v244 offset:12288\n"
"AD 238,18\n"
"MA v,76,v,204,a,224\n"
"AD 239,19\n"
"MA v,76,a,120,a,64\n"
"MA v,76,a,132,a,68\n"
"s_waitcnt lgkmcnt(2)\n"
"AD 24,8\n"
"AD 25,9\n"
"MA v,76,a,128,a,72\n"
"ds_read_b128 v[4:7], v244 offset:21504\n"
"AD 26,10\n"
"MA v,112,a,128,v,208\n"
"ds_write_b128 v245, v[96:99] offset:7168\n"
"MA v,112,a,132,v,200\n"
"s_waitcnt lgkmcnt(3)\n"
"AD 27,11\n"
"MA v,112,a,120,a,84\n"
"s_waitcnt lgkmcnt(2)\n"
"s_barrier\n"
"s_cmp_eq_u32 s73, 0\n"
"s_cbranch_scc1 L_HNC_%=\n"
"L_HBNC_%=:\n"
"AD 24,20\n"
"AD 25,21\n"
"MA v,112,v,204,v,192\n"
"s_waitcnt vmcnt(5)\n"
"ds_read_b128 v[48:51], v244 offset:4096\n"
"AD 26,22\n"
"AD 27,23\n"
"MA v,120,v,180,v,220\n"
"s_waitcnt vmcnt(4)\n"
"ds_write_b128 v246, v[92:95] offset:16384\n"
"MA v,120,v,212,v,216\n"
"s_waitcnt vmcnt(2)\n"
"MA v,120,v,188,a,44\n"
"s_waitcnt vmcnt(1) lgkmcnt(3)\n"
"AD 24,4\n"
"AD 25,5\n"
"MA v,120,a,124,a,212\n"
"ds_read_b128 v[36:39], v244 offset:13312\n"
"AD 26,6\n"
"AD 27,7\n"
"MA v,108,a,124,a,208\n"
"ds_write_b128 v247, v[76:79] offset:25600\n"
"MA v,108,v,188,a,220\n"
"MA v,108,v,212,a,216\n"
"s_waitcnt lgkmcnt(3)\n"
"AD 32,48\n"
"AD 33,49\n"
"MA v,108,v,180,a,228\n"
"AD 34,50\n"
"AD 35,51\n"
"MA v,124,v,180,a,224\n"
"MA v,124,v,212,a,64\n"
"MA v,124,v,188,a,68\n"
"s_waitcnt vmcnt(0) lgkmcnt(1)\n"
"AD 32,36\n"
"AD 33,37\n"
"v_cmp_ne_u64 s[82:83], s[28:29], v[160:161]\n"
"MA v,124,a,124,a,72\n"
"AD 34,38\n"
"AD 35,39\n"
"MA v,104,a,124,v,208\n"
"ds_write_b128 v245, v[120:123] offset:8192\n"
"MA v,104,v,188,v,200\n"
"MA v,104,v,212,a,84\n"
"MA v,104,v,180,v,192\n"
"s_cmp_eq_u64 s[82:83], 0\n"
"s_cbranch_scc0 L_PX_SPIN_%=\n"
"L_PX_OK_%=:\n"
"ds_read_b128 v[52:55], v244 offset:22528\n"
"ds_read_b128 v[44:47], v244 offset:5120\n"
"ds_read_b128 v[56:59], v244 offset:14336\n"
"ds_read_b128 v[40:43], v244 offset:23552\n"
"ds_read_b128 v[84:87], v244 offset:6144\n"
"ds_read_b128 v[72:75], v244 offset:15360\n"
"ds_read_b128 v[88:91], v244 offset:24576\n"
"s_add_u32 s80, s71, 0\n"
"buffer_load_dwordx4 v[164:167], v255, s[48:51], s80 offen sc1\n"
"s_add_u32 s80, s71, 1024\n"
"buffer_load_dwordx4 v[168:171], v255, s[48:51], s80 offen sc1\n"
"s_add_u32 s80, s71, 2048\n"
"buffer_load_dwordx4 v[172:175], v255, s[48:51], s80 offen sc1\n"
"s_add_u32 s80, s71, 3072\n"
"buffer_load_dwordx4 v[176:179], v255, s[48:51], s80 offen sc1\n"
"s_add_u32 s80, s71, 4096\n"
"buffer_load_dwordx2 v[180:181], v255, s[48:51], s80 offen sc1\n"
"ds_write_b128 v246, v[108:111] offset:17408\n"
"ds_write_b128 v247, v[124:127] offset:26624\n"
"s_waitcnt lgkmcnt(8)\n"
"v_pk_add_f32 v[32:33], v[32:33], v[52:53]\n"
"v_pk_add_f32 v[34:35], v[34:35], v[54:55]\n"
"s_waitcnt lgkmcnt(7)\n"
"v_pk_add_f32 v[60:61], v[60:61], v[44:45]\n"
"v_pk_add_f32 v[62:63], v[62:63], v[46:47]\n"
"s_waitcnt lgkmcnt(6)\n"
"v_pk_add_f32 v[60:61], v[60:61], v[56:57]\n"
"v_pk_add_f32 v[62:63], v[62:63], v[58:59]\n"
"s_waitcnt lgkmcnt(5)\n"
"v_pk_add_f32 v[60:61], v[60:61], v[40:41]\n"
"v_pk_add_f32 v[62:63], v[62:63], v[42:43]\n"
"s_waitcnt lgkmcnt(4)\n"
"v_pk_add_f32 v[68:69], v[68:69], v[84:85]\n"
"v_pk_add_f32 v[70:71], v[70:71], v[86:87]\n"
"s_waitcnt lgkmcnt(3)\n"
"v_pk_add_f32 v[68:69], v[68:69], v[72:73]\n"
"v_pk_add_f32 v[70:71], v[70:71], v[74:75]\n"
"s_waitcnt lgkmcnt(2)\n"
"v_pk_add_f32 v[68:69], v[68:69], v[88:89]\n"
"v_pk_add_f32 v[70:71], v[70:71], v[90:91]\n"
"s_waitcnt lgkmcnt(0)\n"
"s_barrier\n"
"ds_read_b128 v[96:99], v244 offset:7168\n"
"ds_read_b128 v[92:95], v244 offset:16384\n"
"ds_read_b128 v[76:79], v244 offset:25600\n"
"ds_read_b128 v[120:123], v244 offset:8192\n"
"ds_read_b128 v[108:111], v244 offset:17408\n"
"ds_read_b128 v[124:127], v244 offset:26624\n"
"s_waitcnt vmcnt(4)\n"
"v_lshlrev_b32 v128, 16, v164\n"
"v_and_b32 v129, 0xffff0000, v164\n"
"v_lshlrev_b32 v130, 16, v165\n"
"v_and_b32 v131, 0xffff0000, v165\n"
"v_pk_add_f32 v[132:133], v[132:133], v[128:129]\n"
"v_pk_add_f32 v[134:135], v[134:135], v[130:131]\n"
"CV 132,132,133\n"
"CV 133,134,135\n"
"ds_write_b64 v252, v[132:133] offset:0\n"
"v_lshlrev_b32 v228, 16, v166\n"
"v_and_b32 v229, 0xffff0000, v166\n"
"L_ST23_%=:\n"
"s_cbranch_execz L_VB5_%=\n"
"v_lshlrev_b32 v230, 16, v167\n"
"v_and_b32 v231, 0xffff0000, v167\n"
"v_pk_add_f32 v[224:225], v[224:225], v[228:229]\n"
"v_pk_add_f32 v[226:227], v[226:227], v[230:231]\n"
"CV 224,224,225\n"
"CV 225,226,227\n"
"ds_write_b64 v252, v[224:225] offset:32\n"
"s_waitcnt vmcnt(3)\n"
"v_lshlrev_b32 v232, 16, v168\n"
"v_and_b32 v233, 0xffff0000, v168\n"
"v_lshlrev_b32 v234, 16, v169\n"
"v_and_b32 v235, 0xffff0000, v169\n"
"v_pk_add_f32 v[236:237], v[236:237], v[232:233]\n"
"v_pk_add_f32 v[238:239], v[238:239], v[234:235]\n"
"CV 236,236,237\n"
"CV 237,238,239\n"
"ds_write_b64 v252, v[236:237] offset:64\n"
"v_lshlrev_b32 v12, 16, v170\n"
"v_and_b32 v13, 0xffff0000, v170\n"
"v_lshlrev_b32 v14, 16, v171\n"
"v_and_b32 v15, 0xffff0000, v171\n"
"v_pk_add_f32 v[24:25], v[24:25], v[12:13]\n"
"v_pk_add_f32 v[26:27], v[26:27], v[14:15]\n"
"CV 24,24,25\n"
"CV 25,26,27\n"
"ds_write_b64 v252, v[24:25] offset:96\n"
"s_waitcnt vmcnt(2)\n"
"v_lshlrev_b32 v28, 16, v172\n"
"v_and_b32 v29, 0xffff0000, v172\n"
"v_lshlrev_b32 v30, 16, v173\n"
"v_and_b32 v31, 0xffff0000, v173\n"
"v_pk_add_f32 v[32:33], v[32:33], v[28:29]\n"
"v_pk_add_f32 v[34:35], v[34:35], v[30:31]\n"
"CV 32,32,33\n"
"CV 33,34,35\n"
"ds_write_b64 v252, v[32:33] offset:128\n"
"v_lshlrev_b32 v64, 16, v174\n"
"v_and_b32 v65, 0xffff0000, v174\n"
"v_lshlrev_b32 v66, 16, v175\n"
"v_and_b32 v67, 0xffff0000, v175\n"
"v_pk_add_f32 v[60:61], v[60:61], v[64:65]\n"
"v_pk_add_f32 v[62:63], v[62:63], v[66:67]\n"
"CV 60,60,61\n"
"CV 61,62,63\n"
"ds_write_b64 v252, v[60:61] offset:160\n"
"s_waitcnt vmcnt(1)\n"
"v_lshlrev_b32 v80, 16, v176\n"
"v_and_b32 v81, 0xffff0000, v176\n"
"v_lshlrev_b32 v82, 16, v177\n"
"v_and_b32 v83, 0xffff0000, v177\n"
"v_pk_add_f32 v[68:69], v[68:69], v[80:81]\n"
"v_pk_add_f32 v[70:71], v[70:71], v[82:83]\n"
"CV 68,68,69\n"
"CV 69,70,71\n"
"ds_write_b64 v252, v[68:69] offset:192\n"
"s_waitcnt lgkmcnt(12)\n"
"v_pk_add_f32 v[112:113], v[112:113], v[96:97]\n"
"v_pk_add_f32 v[114:115], v[114:115], v[98:99]\n"
"s_waitcnt lgkmcnt(11)\n"
"v_pk_add_f32 v[112:113], v[112:113], v[92:93]\n"
"v_pk_add_f32 v[114:115], v[114:115], v[94:95]\n"
"s_waitcnt lgkmcnt(10)\n"
"v_pk_add_f32 v[112:113], v[112:113], v[76:77]\n"
"v_pk_add_f32 v[114:115], v[114:115], v[78:79]\n"
"v_lshlrev_b32 v100, 16, v178\n"
"v_and_b32 v101, 0xffff0000, v178\n"
"v_lshlrev_b32 v102, 16, v179\n"
"v_and_b32 v103, 0xffff0000, v179\n"
"v_pk_add_f32 v[112:113], v[112:113], v[100:101]\n"
"v_pk_add_f32 v[114:115], v[114:115], v[102:103]\n"
"CV 112,112,113\n"
"CV 113,114,115\n"
"ds_write_b64 v252, v[112:113] offset:224\n"
"s_waitcnt lgkmcnt(10)\n"
"v_pk_add_f32 v[104:105], v[104:105], v[120:121]\n"
"v_pk_add_f32 v[106:107], v[106:107], v[122:123]\n"
"s_waitcnt lgkmcnt(9)\n"
"v_pk_add_f32 v[104:105], v[104:105], v[108:109]\n"
"v_pk_add_f32 v[106:107], v[106:107], v[110:111]\n"
"s_waitcnt lgkmcnt(8)\n"
"v_pk_add_f32 v[104:105], v[104:105], v[124:125]\n"
"v_pk_add_f32 v[106:107], v[106:107], v[126:127]\n"
"s_waitcnt vmcnt(0)\n"
"v_lshlrev_b32 v116, 16, v180\n"
"v_and_b32 v117, 0xffff0000, v180\n"
"v_lshlrev_b32 v118, 16, v181\n"
"v_and_b32 v119, 0xffff0000, v181\n"
"v_pk_add_f32 v[104:105], v[104:105], v[116:117]\n"
"v_pk_add_f32 v[106:107], v[106:107], v[118:119]\n"
"CV 104,104,105\n"
"CV 105,106,107\n"
"ds_write_b64 v252, v[104:105] offset:256\n"
"ds_read_b128 v[160:163], v254 offset:0\n"
"ds_read_b128 v[164:167], v254 offset:912\n"
"ds_read_b128 v[168:171], v254 offset:1824\n"
"ds_read_b128 v[172:175], v254 offset:2736\n"
"ds_read_b128 v[176:179], v254 offset:3648\n"
"ds_read_b128 v[180:183], v254 offset:4560\n"
"s_mov_b32 s80, 0\n"
"s_waitcnt lgkmcnt(5)\n"
"buffer_store_dwordx4 v[160:163], v253, s[60:63], s80 offen\n"
"s_mov_b32 s80, 6912\n"
"s_waitcnt lgkmcnt(4)\n"
"buffer_store_dwordx4 v[164:167], v253, s[60:63], s80 offen\n"
"s_mov_b32 s80, 13824\n"
"s_waitcnt lgkmcnt(3)\n"
"buffer_store_dwordx4 v[168:171], v253, s[60:63], s80 offen\n"
"s_mov_b32 s80, 20736\n"
"s_waitcnt lgkmcnt(2)\n"
"buffer_store_dwordx4 v[172:175], v253, s[60:63], s80 offen\n"
"s_mov_b32 s80, 27648\n"
"s_waitcnt lgkmcnt(1)\n"
"buffer_store_dwordx4 v[176:179], v253, s[60:63], s80 offen\n"
"s_mov_b32 s80, 34560\n"
"s_waitcnt lgkmcnt(0)\n"
"buffer_store_dwordx4 v[180:183], v253, s[60:63], s80 offen\n"
"s_cmp_eq_u32 s73, 0\n"
"s_cbranch_scc0 L_END_%=\n"
"s_mov_b32 s72, 0x20000\n"
"L_NONCE_%=:\n"
"v_add_u32 v212, 4096, v255\n"
"global_load_dwordx4 v[204:207], v255, s[18:19] sc1\n"
"global_load_dwordx4 v[208:211], v212, s[18:19] sc1\n"
"s_waitcnt vmcnt(0)\n"
"v_cmp_ne_u64 s[26:27], s[28:29], v[204:205]\n"
"v_cmp_ne_u64 s[76:77], s[28:29], v[206:207]\n"
"s_nop 4\n"
"s_or_b64 s[24:25], s[26:27], s[76:77]\n"
"v_cmp_ne_u64 s[26:27], s[28:29], v[208:209]\n"
"v_cmp_ne_u64 s[76:77], s[28:29], v[210:211]\n"
"s_nop 4\n"
"s_or_b64 s[24:25], s[24:25], s[26:27]\n"
"s_or_b64 s[24:25], s[24:25], s[76:77]\n"
"s_cbranch_scc0 L_NONCE_OK_%=\n"
"s_sleep 2\n"
"s_sub_u32 s72, s72, 1\n"
"s_cmp_lg_u32 s72, 0\n"
"s_cbranch_scc1 L_NONCE_%=\n"
"L_NONCE_OK_%=:\n"
"s_mov_b32 s85, 49152\n"
"s_mov_b32 s54, 49160\n"
"buffer_store_dwordx2 a[252:253], v255, s[52:55], s85 offen sc1\n"
"s_branch L_END_%=\n"
"L_RETRY_%=:\n"
"s_nop 7\n"
"ds_read_b64_tr_b16 v[170:171], v253 offset:8704\n"
"ds_write_b64 v252, v[36:37] offset:1024\n"
"v_mul_f32_dpp v42, v246, v42 row_newbcast:2 row_mask:0xf bank_mask:0xf\n"
"v_mul_f32_dpp v43, v247, v43 row_newbcast:2 row_mask:0xf bank_mask:0xf\n"
"ds_read_b64_tr_b16 v[172:173], v253 offset:9216\n"
"CV 40,40,41\n"
"CV 41,42,43\n"
"ds_write_b64 v252, v[52:53] offset:1536\n"
"v_mul_f32_dpp v56, v244, v56 row_newbcast:2 row_mask:0xf bank_mask:0xf\n"
"ds_write_b64 v252, v[40:41] offset:8192\n"
"v_mul_f32_dpp v57, v245, v57 row_newbcast:2 row_mask:0xf bank_mask:0xf\n"
"ds_read_b64_tr_b16 v[174:175], v253 offset:9728\n"
"ds_read_b64_tr_b16 v[176:177], v253 offset:0\n"
"v_mul_f32_dpp v58, v246, v58 row_newbcast:2 row_mask:0xf bank_mask:0xf\n"
"v_mul_f32_dpp v59, v247, v59 row_newbcast:2 row_mask:0xf bank_mask:0xf\n"
"ds_read_b64_tr_b16 v[178:179], v253 offset:512\n"
"CV 56,56,57\n"
"CV 57,58,59\n"
"ds_read_b64_tr_b16 v[180:181], v253 offset:1024\n"
"v_mul_f32_dpp v44, v244, v44 row_newbcast:3 row_mask:0xf bank_mask:0xf\n"
"ds_write_b64 v252, v[56:57] offset:8704\n"
"v_mul_f32_dpp v45, v245, v45 row_newbcast:3 row_mask:0xf bank_mask:0xf\n"
"ds_read_b64_tr_b16 v[182:183], v253 offset:1536\n"
"v_mul_f32_dpp v46, v246, v46 row_newbcast:3 row_mask:0xf bank_mask:0xf\n"
"v_mul_f32_dpp v47, v247, v47 row_newbcast:3 row_mask:0xf bank_mask:0xf\n"
"CV 44,44,45\n"
"CV 45,46,47\n"
"ds_read_b64_tr_b16 v[184:185], v253 offset:8192\n"
"v_mul_f32_dpp v60, v244, v60 row_newbcast:3 row_mask:0xf bank_mask:0xf\n"
"ds_write_b64 v252, v[44:45] offset:9216\n"
"v_mul_f32_dpp v61, v245, v61 row_newbcast:3 row_mask:0xf bank_mask:0xf\n"
"ds_read_b64_tr_b16 v[186:187], v253 offset:8704\n"
"v_mul_f32_dpp v62, v246, v62 row_newbcast:3 row_mask:0xf bank_mask:0xf\n"
"v_mul_f32_dpp v63, v247, v63 row_newbcast:3 row_mask:0xf bank_mask:0xf\n"
"CV 60,60,61\n"
"CV 61,62,63\n"
"ds_read_b64_tr_b16 v[188:189], v253 offset:9216\n"
"v_mul_f32_dpp v64, v244, v64 row_newbcast:0 row_mask:0xf bank_mask:0xf\n"
"ds_write_b64 v252, v[60:61] offset:9728\n"
"v_mul_f32_dpp v65, v245, v65 row_newbcast:0 row_mask:0xf bank_mask:0xf\n"
"ds_read_b64_tr_b16 v[190:191], v253 offset:9728\n"
"v_mul_f32_dpp v66, v246, v66 row_newbcast:0 row_mask:0xf bank_mask:0xf\n"
"v_mul_f32_dpp v67, v247, v67 row_newbcast:0 row_mask:0xf bank_mask:0xf\n"
"CV 64,64,65\n"
"CV 65,66,67\n"
"v_mul_f32_dpp v80, v244, v80 row_newbcast:0 row_mask:0xf bank_mask:0xf\n"
"ds_write_b64 v252, v[64:65] offset:0\n"
"v_mul_f32_dpp v81, v245, v81 row_newbcast:0 row_mask:0xf bank_mask:0xf\n"
"ds_read_b64_tr_b16 v[192:193], v253 offset:0\n"
"v_mul_f32_dpp v82, v246, v82 row_newbcast:0 row_mask:0xf bank_mask:0xf\n"
"v_mul_f32_dpp v83, v247, v83 row_newbcast:0 row_mask:0xf bank_mask:0xf\n"
"CV 80,80,81\n"
"CV 81,82,83\n"
"v_mul_f32_dpp v68, v244, v68 row_newbcast:1 row_mask:0xf bank_mask:0xf\n"
"ds_write_b64 v252, v[80:81] offset:512\n"
"v_mul_f32_dpp v69, v245, v69 row_newbcast:1 row_mask:0xf bank_mask:0xf\n"
"ds_read_b64_tr_b16 v[194:195], v253 offset:512\n"
"v_mul_f32_dpp v70, v246, v70 row_newbcast:1 row_mask:0xf bank_mask:0xf\n"
"v_mul_f32_dpp v71, v247, v71 row_newbcast:1 row_mask:0xf bank_mask:0xf\n"
"CV 68,68,69\n"
"CV 69,70,71\n"
"v_mul_f32_dpp v84, v244, v84 row_newbcast:1 row_mask:0xf bank_mask:0xf\n"
"ds_write_b64 v252, v[68:69] offset:1024\n"
"v_mul_f32_dpp v85, v245, v85 row_newbcast:1 row_mask:0xf bank_mask:0xf\n"
"ds_read_b64_tr_b16 v[196:197], v253 offset:1024\n"
"v_mul_f32_dpp v86, v246, v86 row_newbcast:1 row_mask:0xf bank_mask:0xf\n"
"v_mul_f32_dpp v87, v247, v87 row_newbcast:1 row_mask:0xf bank_mask:0xf\n"
"CV 84,84,85\n"
"CV 85,86,87\n"
"v_mul_f32_dpp v72, v244, v72 row_newbcast:2 row_mask:0xf bank_mask:0xf\n"
"ds_write_b64 v252, v[84:85] offset:1536\n"
"v_mul_f32_dpp v73, v245, v73 row_newbcast:2 row_mask:0xf bank_mask:0xf\n"
"ds_read_b64_tr_b16 v[198:199], v253 offset:1536\n"
"v_mul_f32_dpp v74, v246, v74 row_newbcast:2 row_mask:0xf bank_mask:0xf\n"
"v_mul_f32_dpp v75, v247, v75 row_newbcast:2 row_mask:0xf bank_mask:0xf\n"
"CV 72,72,73\n"
"CV 73,74,75\n"
"v_mul_f32_dpp v88, v244, v88 row_newbcast:2 row_mask:0xf bank_mask:0xf\n"
"ds_write_b64 v252, v[72:73] offset:8192\n"
"v_mul_f32_dpp v89, v245, v89 row_newbcast:2 row_mask:0xf bank_mask:0xf\n"
"ds_read_b64_tr_b16 v[200:201], v253 offset:8192\n"
"v_mul_f32_dpp v90, v246, v90 row_newbcast:2 row_mask:0xf bank_mask:0xf\n"
"v_mul_f32_dpp v91, v247, v91 row_newbcast:2 row_mask:0xf bank_mask:0xf\n"
"CV 88,88,89\n"
"CV 89,90,91\n"
"v_mul_f32_dpp v76, v244, v76 row_newbcast:3 row_mask:0xf bank_mask:0xf\n"
"ds_write_b64 v252, v[88:89] offset:8704\n"
"v_mul_f32_dpp v77, v245, v77 row_newbcast:3 row_mask:0xf bank_mask:0xf\n"
"ds_read_b64_tr_b16 v[202:203], v253 offset:8704\n"
"v_mul_f32_dpp v78, v246, v78 row_newbcast:3 row_mask:0xf bank_mask:0xf\n"
"v_mul_f32_dpp v79, v247, v79 row_newbcast:3 row_mask:0xf bank_mask:0xf\n"
"CV 76,76,77\n"
"CV 77,78,79\n"
"v_mul_f32_dpp v92, v244, v92 row_newbcast:3 row_mask:0xf bank_mask:0xf\n"
"ds_write_b64 v252, v[76:77] offset:9216\n"
"v_mul_f32_dpp v93, v245, v93 row_newbcast:3 row_mask:0xf bank_mask:0xf\n"
"ds_read_b64_tr_b16 v[204:205], v253 offset:9216\n"
"v_mul_f32_dpp v94, v246, v94 row_newbcast:3 row_mask:0xf bank_mask:0xf\n"
"v_mul_f32_dpp v95, v247, v95 row_newbcast:3 row_mask:0xf bank_mask:0xf\n"
"CV 92,92,93\n"
"CV 93,94,95\n"
"v_mul_f32_dpp v96, v244, v96 row_newbcast:0 row_mask:0xf bank_mask:0xf\n"
"ds_write_b64 v252, v[92:93] offset:9728\n"
"v_mul_f32_dpp v97, v245, v97 row_newbcast:0 row_mask:0xf bank_mask:0xf\n"
"ds_read_b64_tr_b16 v[206:207], v253 offset:9728\n"
"v_mul_f32_dpp v98, v246, v98 row_newbcast:0 row_mask:0xf bank_mask:0xf\n"
"v_mul_f32_dpp v99, v247, v99 row_newbcast:0 row_mask:0xf bank_mask:0xf\n"
"CV 96,96,97\n"
"CV 97,98,99\n"
"v_mul_f32_dpp v112, v244, v112 row_newbcast:0 row_mask:0xf bank_mask:0xf\n"
"ds_write_b64 v252, v[96:97] offset:0\n"
"v_mul_f32_dpp v113, v245, v113 row_newbcast:0 row_mask:0xf bank_mask:0xf\n"
"ds_read_b64_tr_b16 v[208:209], v253 offset:0\n"
"v_mul_f32_dpp v114, v246, v114 row_newbcast:0 row_mask:0xf bank_mask:0xf\n"
"v_mul_f32_dpp v115, v247, v115 row_newbcast:0 row_mask:0xf bank_mask:0xf\n"
"CV 112,112,113\n"
"CV 113,114,115\n"
"v_mul_f32_dpp v100, v244, v100 row_newbcast:1 row_mask:0xf bank_mask:0xf\n"
"ds_write_b64 v252, v[112:113] offset:512\n"
"v_mul_f32_dpp v101, v245, v101 row_newbcast:1 row_mask:0xf bank_mask:0xf\n"
"ds_read_b64_tr_b16 v[210:211], v253 offset:512\n"
"v_mul_f32_dpp v102, v246, v102 row_newbcast:1 row_mask:0xf bank_mask:0xf\n"
"v_mul_f32_dpp v103, v247, v103 row_newbcast:1 row_mask:0xf bank_mask:0xf\n"
"CV 100,100,101\n"
"CV 101,102,103\n"
"v_mul_f32_dpp v116, v244, v116 row_newbcast:1 row_mask:0xf bank_mask:0xf\n"
"ds_write_b64 v252, v[100:101] offset:1024\n"
"v_mul_f32_dpp v117, v245, v117 row_newbcast:1 row_mask:0xf bank_mask:0xf\n"
"ds_read_b64_tr_b16 v[212:213], v253 offset:1024\n"
"v_mul_f32_dpp v118, v246, v118 row_newbcast:1 row_mask:0xf bank_mask:0xf\n"
"v_mul_f32_dpp v119, v247, v119 row_newbcast:1 row_mask:0xf bank_mask:0xf\n"
"CV 116,116,117\n"
"CV 117,118,119\n"
"v_mul_f32_dpp v104, v244, v104 row_newbcast:2 row_mask:0xf bank_mask:0xf\n"
"ds_write_b64 v252, v[116:117] offset:1536\n"
"v_mul_f32_dpp v105, v245, v105 row_newbcast:2 row_mask:0xf bank_mask:0xf\n"
"ds_read_b64_tr_b16 v[214:215], v253 offset:1536\n"
"v_mul_f32_dpp v106, v246, v106 row_newbcast:2 row_mask:0xf bank_mask:0xf\n"
"v_mul_f32_dpp v107, v247, v107 row_newbcast:2 row_mask:0xf bank_mask:0xf\n"
"CV 104,104,105\n"
"CV 105,106,107\n"
"v_mul_f32_dpp v120, v244, v120 row_newbcast:2 row_mask:0xf bank_mask:0xf\n"
"ds_write_b64 v252, v[104:105] offset:8192\n"
"v_mul_f32_dpp v121, v245, v121 row_newbcast:2 row_mask:0xf bank_mask:0xf\n"
"ds_read_b64_tr_b16 v[216:217], v253 offset:8192\n"
"v_mul_f32_dpp v122, v246, v122 row_newbcast:2 row_mask:0xf bank_mask:0xf\n"
"v_mul_f32_dpp v123, v247, v123 row_newbcast:2 row_mask:0xf bank_mask:0xf\n"
"CV 120,120,121\n"
"CV 121,122,123\n"
"v_mul_f32_dpp v108, v244, v108 row_newbcast:3 row_mask:0xf bank_mask:0xf\n"
"ds_write_b64 v252, v[120:121] offset:8704\n"
"v_mul_f32_dpp v109, v245, v109 row_newbcast:3 row_mask:0xf bank_mask:0xf\n"
"ds_read_b64_tr_b16 v[218:219], v253 offset:8704\n"
"v_mul_f32_dpp v110, v246, v110 row_newbcast:3 row_mask:0xf bank_mask:0xf\n"
"v_mul_f32_dpp v111, v247, v111 row_newbcast:3 row_mask:0xf bank_mask:0xf\n"
"CV 108,108,109\n"
"CV 109,110,111\n"
"v_mul_f32_dpp v124, v244, v124 row_newbcast:3 row_mask:0xf bank_mask:0xf\n"
"ds_write_b64 v252, v[108:109] offset:9216\n"
"v_mul_f32_dpp v125, v245, v125 row_newbcast:3 row_mask:0xf bank_mask:0xf\n"
"ds_read_b64_tr_b16 v[220:221], v253 offset:9216\n"
"v_mul_f32_dpp v126, v246, v126 row_newbcast:3 row_mask:0xf bank_mask:0xf\n"
"v_mul_f32_dpp v127, v247, v127 row_newbcast:3 row_mask:0xf bank_mask:0xf\n"
"CV 124,124,125\n"
"CV 125,126,127\n"
"ds_write_b64 v252, v[124:125] offset:9728\n"
"ds_read_b64_tr_b16 v[222:223], v253 offset:9728\n"
"s_mov_b32 s80, 16384\n"
"s_waitcnt lgkmcnt(14)\n"
"buffer_store_dwordx4 v[160:163], v255, s[56:59], s80 offen sc1\n"
"s_mov_b32 s80, 17408\n"
"buffer_store_dwordx4 v[164:167], v255, s[56:59], s80 offen sc1\n"
"s_mov_b32 s80, 18432\n"
"buffer_store_dwordx4 v[168:171], v255, s[56:59], s80 offen sc1\n"
"s_mov_b32 s80, 19456\n"
"buffer_store_dwordx4 v[172:175], v255, s[56:59], s80 offen sc1\n"
"s_mov_b32 s80, 20480\n"
"buffer_store_dwordx4 v[176:179], v255, s[56:59], s80 offen sc1\n"
"s_mov_b32 s80, 21504\n"
"buffer_store_dwordx4 v[180:183], v255, s[56:59], s80 offen sc1\n"
"s_mov_b32 s80, 22528\n"
"buffer_store_dwordx4 v[184:187], v255, s[56:59], s80 offen sc1\n"
"s_mov_b32 s80, 23552\n"
"buffer_store_dwordx4 v[188:191], v255, s[56:59], s80 offen sc1\n"
"s_mov_b32 s80, 24576\n"
"buffer_store_dwordx4 v[192:195], v255, s[56:59], s80 offen sc1\n"
"s_mov_b32 s80, 25600\n"
"buffer_store_dwordx4 v[196:199], v255, s[56:59], s80 offen sc1\n"
"s_mov_b32 s80, 26624\n"
"buffer_store_dwordx4 v[200:203], v255, s[56:59], s80 offen sc1\n"
"s_mov_b32 s80, 27648\n"
"buffer_store_dwordx4 v[204:207], v255, s[56:59], s80 offen sc1\n"
"s_mov_b32 s80, 28672\n"
"s_waitcnt lgkmcnt(12)\n"
"buffer_store_dwordx4 v[208:211], v255, s[56:59], s80 offen sc1\n"
"s_mov_b32 s80, 29696\n"
"s_waitcnt lgkmcnt(8)\n"
"buffer_store_dwordx4 v[212:215], v255, s[56:59], s80 offen sc1\n"
"s_mov_b32 s80, 30720\n"
"s_waitcnt lgkmcnt(4)\n"
"buffer_store_dwordx4 v[216:219], v255, s[56:59], s80 offen sc1\n"
"s_mov_b32 s80, 31744\n"
"s_waitcnt lgkmcnt(0)\n"
"buffer_store_dwordx4 v[220:223], v255, s[56:59], s80 offen sc1\n"
"s_waitcnt vmcnt(0)\n"
"buffer_store_dwordx2 a[252:253], v255, s[52:55], s85 offen sc1\n"
"s_add_u32 s85, s85, 24576\n"
"s_add_u32 s79, s85, 8\n"
"s_cmp_eq_u32 s54, 0\n"
"s_cselect_b32 s54, 0, s79\n"
"buffer_store_dwordx2 a[252:253], v255, s[52:55], s85 offen sc1\n"
"s_mov_b32 s54, 0\n"
"s_mov_b32 s74, 1\n"
"s_waitcnt vmcnt(0)\n"
"s_barrier\n"
"v_mbcnt_lo_u32_b32 v246, -1, 0\n"
"v_mbcnt_hi_u32_b32 v246, -1, v246\n"
"s_lshl_b32 s65, s64, 11\n"
"s_lshl_b32 s81, s64, 17\n"
"v_lshrrev_b32 v247, 4, v246\n"
"v_lshlrev_b32 v248, 2, v247\n"
"v_and_b32 v242, 15, v246\n"
"v_xor_b32 v242, v242, v248\n"
"v_lshlrev_b32 v242, 4, v242\n"
"v_lshl_add_u32 v242, v247, 13, v242\n"
"v_add_u32 v242, s81, v242\n"
"s_mov_b32 s66, 0\n"
"s_mul_i32 s81, s64, 73728\n"
"v_and_b32 v243, 7, v246\n"
"v_xor_b32 v243, v243, v247\n"
"v_lshlrev_b32 v243, 4, v243\n"
"v_xor_b32 v244, 64, v243\n"
"v_lshrrev_b32 v247, 3, v246\n"
"v_mul_u32_u24 v248, 1152, v247\n"
"v_add3_u32 v243, v243, v248, s81\n"
"v_add3_u32 v244, v244, v248, s81\n"
"s_lshl_b32 s67, s64, 13\n"
"s_add_u32 s67, s67, 65664\n"
"s_mov_b32 s68, 0\n"
"v_lshlrev_b32 v255, 4, v246\n"
"s_lshl_b32 s78, s64, 10\n"
"s_branch L_START2_%=\n"
"L_LATE_%=:\n"
"s_mov_b32 s72, 0x20000\n"
"s_lshr_b32 s79, s20, 16\n"
"s_and_b32 s83, s20, 0xffff\n"
"v_and_b32 v149, 32, v255\n"
"v_and_b32 v148, 16, v255\n"
"v_lshl_or_b32 v148, v149, 3, v148\n"
"v_xor_b32 v148, s79, v148\n"
"v_and_b32 v248, 0x100, v255\n"
"v_bfe_u32 v149, v255, 1, 7\n"
"v_cmp_gt_u32 vcc, 0x200, v255\n"
"v_lshl_or_b32 v149, v248, 1, v149\n"
"v_xor_b32 v149, s83, v149\n"
"v_cndmask_b32 v248, v148, v149, vcc\n"
"L_PA_AGAIN_%=:\n"
"s_sleep 2\n"
"global_load_dwordx2 v[148:149], v248, s[18:19] sc1\n"
"s_waitcnt vmcnt(0)\n"
"v_cmp_ne_u64 s[82:83], s[28:29], v[148:149]\n"
"s_nop 4\n"
"s_cmp_eq_u64 s[82:83], 0\n"
"s_cbranch_scc1 L_PA_GOT_%=\n"
"s_sub_u32 s72, s72, 1\n"
"s_cmp_lg_u32 s72, 0\n"
"s_cbranch_scc1 L_PA_AGAIN_%=\n"
"L_PA_GOT_%=:\n"
"s_mov_b32 s38, 147456\n"
"s_mov_b32 s42, 65536\n"
"s_mov_b32 s46, 65536\n"
"s_waitcnt vmcnt(0)\n"
"s_add_u32 m0, s69, 0\n"
"s_mov_b32 s80, 91136\n"
"buffer_load_dwordx4 v255, s[36:39], s80 offen sc1 lds\n"
"s_add_u32 m0, s69, 1024\n"
"s_mov_b32 s80, 18432\n"
"buffer_load_dwordx4 v255, s[36:39], s80 offen sc1 lds\n"
"s_add_u32 m0, s69, 131072\n"
"s_mov_b32 s80, 27648\n"
"buffer_load_dwordx4 v255, s[36:39], s80 offen sc1 lds\n"
"s_add_u32 m0, s69, 132096\n"
"s_mov_b32 s80, 19456\n"
"buffer_load_dwordx4 v255, s[36:39], s80 offen sc1 lds\n"
"s_add_u32 m0, s69, 8192\n"
"s_mov_b32 s80, 28672\n"
"buffer_load_dwordx4 v255, s[36:39], s80 offen sc1 lds\n"
"s_add_u32 m0, s69, 9216\n"
"s_mov_b32 s80, 20480\n"
"buffer_load_dwordx4 v255, s[36:39], s80 offen sc1 lds\n"
"s_add_u32 m0, s69, 139264\n"
"s_mov_b32 s80, 29696\n"
"buffer_load_dwordx4 v255, s[36:39], s80 offen sc1 lds\n"
"s_add_u32 m0, s69, 140288\n"
"s_mov_b32 s80, 21504\n"
"buffer_load_dwordx4 v255, s[36:39], s80 offen sc1 lds\n"
"s_add_u32 m0, s69, 16384\n"
"s_mov_b32 s80, 30720\n"
"buffer_load_dwordx4 v255, s[36:39], s80 offen sc1 lds\n"
"s_add_u32 m0, s69, 17408\n"
"s_mov_b32 s80, 22528\n"
"buffer_load_dwordx4 v255, s[36:39], s80 offen sc1 lds\n"
"s_add_u32 m0, s69, 98304\n"
"s_mov_b32 s80, 31744\n"
"buffer_load_dwordx4 v255, s[36:39], s80 offen sc1 lds\n"
"s_add_u32 m0, s69, 99328\n"
"s_mov_b32 s80, 23552\n"
"buffer_load_dwordx4 v255, s[36:39], s80 offen sc1 lds\n"
"s_add_u32 m0, s69, 147456\n"
"s_mov_b32 s80, 32768\n"
"buffer_load_dwordx4 v255, s[36:39], s80 offen sc1 lds\n"
"s_add_u32 m0, s69, 148480\n"
"s_mov_b32 s80, 24576\n"
"buffer_load_dwordx4 v255, s[36:39], s80 offen sc1 lds\n"
"s_add_u32 m0, s69, 155648\n"
"s_mov_b32 s80, 33792\n"
"buffer_load_dwordx4 v255, s[36:39], s80 offen sc1 lds\n"
"s_mov_b32 s80, 0\n"
"LG 192,36\n"
"s_mov_b32 s80, 9216\n"
"LG 196,36\n"
"s_mov_b32 s80, 1024\n"
"LG 200,36\n"
"s_mov_b32 s80, 10240\n"
"LG 204,36\n"
"s_mov_b32 s80, 2048\n"
"LG 208,36\n"
"s_add_u32 m0, s69, 24576\n"
"s_mov_b32 s80, 25600\n"
"buffer_load_dwordx4 v255, s[36:39], s80 offen sc1 lds\n"
"s_add_u32 m0, s69, 25600\n"
"s_mov_b32 s80, 34816\n"
"buffer_load_dwordx4 v255, s[36:39], s80 offen sc1 lds\n"
"s_add_u32 m0, s69, 106496\n"
"s_mov_b32 s80, 26624\n"
"buffer_load_dwordx4 v255, s[36:39], s80 offen sc1 lds\n"
"s_add_u32 m0, s69, 107520\n"
"s_mov_b32 s80, 35840\n"
"buffer_load_dwordx4 v255, s[36:39], s80 offen sc1 lds\n"
"s_mov_b32 s80, 11264\n"
"LG 212,36\n"
"s_mov_b32 s80, 3072\n"
"LG 216,36\n"
"s_mov_b32 s80, 12288\n"
"LG 220,36\n"
"s_mov_b32 s80, 4096\n"
"LG 224,36\n"
"s_mov_b32 s80, 13312\n"
"LG 228,36\n"
"s_mov_b32 s80, 5120\n"
"LG 232,36\n"
"s_add_u32 m0, s69, 32768\n"
"s_add_u32 s80, s65, 8192\n"
"buffer_load_dwordx4 v255, s[44:47], s80 offen sc1 lds\n"
"s_add_u32 m0, s69, 33792\n"
"s_add_u32 s80, s65, 12288\n"
"buffer_load_dwordx4 v255, s[44:47], s80 offen sc1 lds\n"
"s_add_u32 m0, s69, 114688\n"
"s_add_u32 s80, s66, 8192\n"
"buffer_load_dwordx4 v255, s[44:47], s80 offen sc1 lds\n"
"s_add_u32 m0, s69, 115712\n"
"s_add_u32 s80, s66, 12288\n"
"buffer_load_dwordx4 v255, s[44:47], s80 offen sc1 lds\n"
"s_add_u32 s80, s65, 0\n"
"LG 0,44\n"
"s_add_u32 s80, s65, 4096\n"
"LG 4,44\n"
"s_add_u32 s80, s66, 0\n"
"LG 8,44\n"
"s_add_u32 s80, s66, 4096\n"
"LG 12,44\n"
"s_add_u32 s80, s67, 0\n"
"LG 16,44\n"
"s_add_u32 s80, s67, 4096\n"
"LG 20,44\n"
"s_add_u32 s80, s78, 0\n"
"LG 24,44\n"
"s_add_u32 s80, s78, 4096\n"
"LG 28,44\n"
"s_add_u32 m0, s69, 40960\n"
"s_add_u32 s80, s67, 8192\n"
"buffer_load_dwordx4 v255, s[44:47], s80 offen sc1 lds\n"
"s_add_u32 m0, s69, 41984\n"
"s_add_u32 s80, s67, 12288\n"
"buffer_load_dwordx4 v255, s[44:47], s80 offen sc1 lds\n"
"s_add_u32 m0, s69, 122880\n"
"s_add_u32 s80, s78, 8192\n"
"buffer_load_dwordx4 v255, s[44:47], s80 offen sc1 lds\n"
"s_add_u32 m0, s69, 123904\n"
"s_add_u32 s80, s78, 12288\n"
"buffer_load_dwordx4 v255, s[44:47], s80 offen sc1 lds\n"
"s_add_u32 s80, s65, 0\n"
"LG 32,40\n"
"s_add_u32 s80, s65, 4096\n"
"LG 36,40\n"
"s_add_u32 s80, s66, 0\n"
"LG 40,40\n"
"s_add_u32 s80, s66, 4096\n"
"LG 44,40\n"
"s_add_u32 s80, s67, 0\n"
"LG 48,40\n"
"s_add_u32 s80, s67, 4096\n"
"LG 52,40\n"
"s_add_u32 s80, s78, 0\n"
"LG 56,40\n"
"s_add_u32 s80, s78, 4096\n"
"LG 60,40\n"
"s_add_u32 m0, s69, 49152\n"
"s_add_u32 s80, s65, 8192\n"
"buffer_load_dwordx4 v255, s[40:43], s80 offen sc1 lds\n"
"s_add_u32 m0, s69, 50176\n"
"s_add_u32 s80, s65, 12288\n"
"buffer_load_dwordx4 v255, s[40:43], s80 offen sc1 lds\n"
"s_add_u32 m0, s69, 57344\n"
"s_add_u32 s80, s66, 8192\n"
"buffer_load_dwordx4 v255, s[40:43], s80 offen sc1 lds\n"
"s_add_u32 m0, s69, 58368\n"
"s_add_u32 s80, s66, 12288\n"
"buffer_load_dwordx4 v255, s[40:43], s80 offen sc1 lds\n"
"s_add_u32 m0, s69, 65536\n"
"s_add_u32 s80, s67, 8192\n"
"buffer_load_dwordx4 v255, s[40:43], s80 offen sc1 lds\n"
"s_add_u32 m0, s69, 66560\n"
"s_add_u32 s80, s67, 12288\n"
"buffer_load_dwordx4 v255, s[40:43], s80 offen sc1 lds\n"
"s_add_u32 m0, s69, 73728\n"
"s_add_u32 s80, s78, 8192\n"
"buffer_load_dwordx4 v255, s[40:43], s80 offen sc1 lds\n"
"s_add_u32 m0, s69, 74752\n"
"s_add_u32 s80, s78, 12288\n"
"buffer_load_dwordx4 v255, s[40:43], s80 offen sc1 lds\n"
"s_mov_b32 s80, 14336\n"
"LG 236,36\n"
"s_mov_b32 s80, 6144\n"
"LG 240,36\n"
"s_mov_b32 s80, 15360\n"
"LG 244,36\n"
"s_waitcnt vmcnt(0)\n"
"s_branch L_PA_OK_%=\n"
"L_PB_SPIN_%=:\n"
"s_mov_b32 s72, 0x20000\n"
"L_PB_AGAIN_%=:\n"
"s_sleep 2\n"
"global_load_dwordx2 v[244:245], v248, s[18:19] sc1\n"
"s_waitcnt vmcnt(0)\n"
"v_cmp_ne_u64 s[82:83], s[28:29], v[244:245]\n"
"s_nop 4\n"
"s_cmp_eq_u64 s[82:83], 0\n"
"s_cbranch_scc1 L_PB_GOT_%=\n"
"s_sub_u32 s72, s72, 1\n"
"s_cmp_lg_u32 s72, 0\n"
"s_cbranch_scc1 L_PB_AGAIN_%=\n"
"L_PB_GOT_%=:\n"
"s_branch L_PB_OK_%=\n"
"L_PC_SPIN_%=:\n"
"s_mov_b32 s72, 0x20000\n"
"L_PCX_AGAIN_%=:\n"
"s_sleep 2\n"
"global_load_dwordx2 v[244:245], v248, s[18:19] sc1\n"
"s_waitcnt vmcnt(0)\n"
"v_cmp_ne_u64 s[82:83], s[28:29], v[244:245]\n"
"s_nop 4\n"
"s_cmp_eq_u64 s[82:83], 0\n"
"s_cbranch_scc1 L_PCX_GOT_%=\n"
"s_sub_u32 s72, s72, 1\n"
"s_cmp_lg_u32 s72, 0\n"
"s_cbranch_scc1 L_PCX_AGAIN_%=\n"
"L_PCX_GOT_%=:\n"
"s_branch L_PC_OK_%=\n"
"L_PX_SPIN_%=:\n"
"s_mov_b32 s72, 0x20000\n"
"L_PX_AGAIN_%=:\n"
"s_sleep 2\n"
"global_load_dwordx2 v[160:161], v248, s[18:19] sc1\n"
"s_waitcnt vmcnt(0)\n"
"v_cmp_ne_u64 s[82:83], s[28:29], v[160:161]\n"
"s_nop 4\n"
"s_cmp_eq_u64 s[82:83], 0\n"
"s_cbranch_scc1 L_PX_GOT_%=\n"
"s_sub_u32 s72, s72, 1\n"
"s_cmp_lg_u32 s72, 0\n"
"s_cbranch_scc1 L_PX_AGAIN_%=\n"
"L_PX_GOT_%=:\n"
"s_branch L_PX_OK_%=\n"
"L_HNL_%=:\n"
"v_add_u32 v172, 4096, v255\n"
"global_load_dwordx4 v[164:167], v255, s[18:19] sc1\n"
"global_load_dwordx4 v[168:171], v172, s[18:19] sc1\n"
"s_branch L_HBNL_%=\n"
"L_HNC_%=:\n"
"s_waitcnt vmcnt(0)\n"
"v_cmp_ne_u64 s[26:27], s[28:29], v[164:165]\n"
"v_cmp_ne_u64 s[76:77], s[28:29], v[166:167]\n"
"s_nop 4\n"
"s_or_b64 s[24:25], s[26:27], s[76:77]\n"
"v_cmp_ne_u64 s[26:27], s[28:29], v[168:169]\n"
"v_cmp_ne_u64 s[76:77], s[28:29], v[170:171]\n"
"s_nop 4\n"
"s_or_b64 s[24:25], s[24:25], s[26:27]\n"
"s_or_b64 s[24:25], s[24:25], s[76:77]\n"
"s_cbranch_scc1 L_HBNC_%=\n"
"s_mov_b32 s73, 1\n"
"s_mov_b32 s85, 49152\n"
"s_mov_b32 s54, 49160\n"
"buffer_store_dwordx2 a[252:253], v255, s[52:55], s85 offen sc1\n"
"s_branch L_HBNC_%=\n"
"L_PACE0_%=:\n"
"s_min_i32 s33, s33, s77\n"
"s_add_u32 s72, vcc_lo, s77\n"
"s_add_u32 s72, s72, s33\n"
"s_add_u32 s33, s33, vcc_lo\n"
"L_PACEL0_%=:\n"
"s_sleep 1\n"
"s_memrealtime vcc\n"
"s_waitcnt lgkmcnt(0)\n"
"s_sub_u32 vcc_hi, s33, vcc_lo\n"
"s_cmp_gt_i32 vcc_hi, -4\n"
"s_cbranch_scc1 L_PACEL0_%=\n"
"s_mov_b32 s33, 0\n"
"s_branch L_PACEB0_%=\n"
"L_PACE1_%=:\n"
"s_min_i32 s33, s33, s77\n"
"s_add_u32 s72, vcc_lo, s77\n"
"s_add_u32 s72, s72, s33\n"
"s_add_u32 s33, s33, vcc_lo\n"
"L_PACEL1_%=:\n"
"s_sleep 1\n"
"s_memrealtime vcc\n"
"s_waitcnt lgkmcnt(0)\n"
"s_sub_u32 vcc_hi, s33, vcc_lo\n"
"s_cmp_gt_i32 vcc_hi, -4\n"
"s_cbranch_scc1 L_PACEL1_%=\n"
"s_mov_b32 s33, 0\n"
"s_branch L_PACEB1_%=\n"
"L_PACE2_%=:\n"
"s_min_i32 s33, s33, s77\n"
"s_add_u32 s72, vcc_lo, s77\n"
"s_add_u32 s72, s72, s33\n"
"s_add_u32 s33, s33, vcc_lo\n"
"L_PACEL2_%=:\n"
"s_sleep 1\n"
"s_memrealtime vcc\n"
"s_waitcnt lgkmcnt(0)\n"
"s_sub_u32 vcc_hi, s33, vcc_lo\n"
"s_cmp_gt_i32 vcc_hi, -4\n"
"s_cbranch_scc1 L_PACEL2_%=\n"
"s_mov_b32 s33, 0\n"
"s_branch L_PACEB2_%=\n"
"L_PACE3_%=:\n"
"s_min_i32 s33, s33, s77\n"
"s_add_u32 s72, vcc_lo, s77\n"
"s_add_u32 s72, s72, s33\n"
"s_add_u32 s33, s33, vcc_lo\n"
"L_PACEL3_%=:\n"
"s_sleep 1\n"
"s_memrealtime vcc\n"
"s_waitcnt lgkmcnt(0)\n"
"s_sub_u32 vcc_hi, s33, vcc_lo\n"
"s_cmp_gt_i32 vcc_hi, -4\n"
"s_cbranch_scc1 L_PACEL3_%=\n"
"s_mov_b32 s33, 0\n"
"s_branch L_PACEB3_%=\n"
"L_PACE4_%=:\n"
"s_min_i32 s33, s33, s77\n"
"s_add_u32 s72, vcc_lo, s77\n"
"s_add_u32 s72, s72, s33\n"
"s_add_u32 s33, s33, vcc_lo\n"
"L_PACEL4_%=:\n"
"s_sleep 1\n"
"s_memrealtime vcc\n"
"s_waitcnt lgkmcnt(0)\n"
"s_sub_u32 vcc_hi, s33, vcc_lo\n"
"s_cmp_gt_i32 vcc_hi, -4\n"
"s_cbranch_scc1 L_PACEL4_%=\n"
"s_mov_b32 s33, 0\n"
"s_branch L_PACEB4_%=\n"
"L_PACE5_%=:\n"
"s_min_i32 s33, s33, s77\n"
"s_add_u32 s72, vcc_lo, s77\n"
"s_add_u32 s72, s72, s33\n"
"s_add_u32 s33, s33, vcc_lo\n"
"L_PACEL5_%=:\n"
"s_sleep 1\n"
"s_memrealtime vcc\n"
"s_waitcnt lgkmcnt(0)\n"
"s_sub_u32 vcc_hi, s33, vcc_lo\n"
"s_cmp_gt_i32 vcc_hi, -4\n"
"s_cbranch_scc1 L_PACEL5_%=\n"
"s_mov_b32 s33, 0\n"
"s_branch L_PACEB5_%=\n"
"L_PACE6_%=:\n"
"s_min_i32 s33, s33, s77\n"
"s_add_u32 s72, vcc_lo, s77\n"
"s_add_u32 s72, s72, s33\n"
"s_add_u32 s33, s33, vcc_lo\n"
"L_PACEL6_%=:\n"
"s_sleep 1\n"
"s_memrealtime vcc\n"
"s_waitcnt lgkmcnt(0)\n"
"s_sub_u32 vcc_hi, s33, vcc_lo\n"
"s_cmp_gt_i32 vcc_hi, -4\n"
"s_cbranch_scc1 L_PACEL6_%=\n"
"s_mov_b32 s33, 0\n"
"s_branch L_PACEB6_%=\n"
"L_PACE7_%=:\n"
"s_min_i32 s33, s33, s77\n"
"s_add_u32 s72, vcc_lo, s77\n"
"s_add_u32 s72, s72, s33\n"
"s_add_u32 s33, s33, vcc_lo\n"
"L_PACEL7_%=:\n"
"s_sleep 1\n"
"s_memrealtime vcc\n"
"s_waitcnt lgkmcnt(0)\n"
"s_sub_u32 vcc_hi, s33, vcc_lo\n"
"s_cmp_gt_i32 vcc_hi, -4\n"
"s_cbranch_scc1 L_PACEL7_%=\n"
"s_mov_b32 s33, 0\n"
"s_branch L_PACEB7_%=\n"
"L_PACE8_%=:\n"
"s_min_i32 s33, s33, s77\n"
"s_add_u32 s72, vcc_lo, s77\n"
"s_add_u32 s72, s72, s33\n"
"s_add_u32 s33, s33, vcc_lo\n"
"L_PACEL8_%=:\n"
"s_sleep 1\n"
"s_memrealtime vcc\n"
"s_waitcnt lgkmcnt(0)\n"
"s_sub_u32 vcc_hi, s33, vcc_lo\n"
"s_cmp_gt_i32 vcc_hi, -4\n"
"s_cbranch_scc1 L_PACEL8_%=\n"
"s_mov_b32 s33, 0\n"
"s_branch L_PACEB8_%=\n"
"L_PACE9_%=:\n"
"s_min_i32 s33, s33, s77\n"
"s_add_u32 s72, vcc_lo, s77\n"
"s_add_u32 s72, s72, s33\n"
"s_add_u32 s33, s33, vcc_lo\n"
"L_PACEL9_%=:\n"
"s_sleep 1\n"
"s_memrealtime vcc\n"
"s_waitcnt lgkmcnt(0)\n"
"s_sub_u32 vcc_hi, s33, vcc_lo\n"
"s_cmp_gt_i32 vcc_hi, -4\n"
"s_cbranch_scc1 L_PACEL9_%=\n"
"s_mov_b32 s33, 0\n"
"s_branch L_PACEB9_%=\n"
"L_PACE10_%=:\n"
"s_min_i32 s33, s33, s77\n"
"s_add_u32 s72, vcc_lo, s77\n"
"s_add_u32 s72, s72, s33\n"
"s_add_u32 s33, s33, vcc_lo\n"
"L_PACEL10_%=:\n"
"s_sleep 1\n"
"s_memrealtime vcc\n"
"s_waitcnt lgkmcnt(0)\n"
"s_sub_u32 vcc_hi, s33, vcc_lo\n"
"s_cmp_gt_i32 vcc_hi, -4\n"
"s_cbranch_scc1 L_PACEL10_%=\n"
"s_mov_b32 s33, 0\n"
"s_branch L_PACEB10_%=\n"
"L_PACE11_%=:\n"
"s_min_i32 s33, s33, s77\n"
"s_add_u32 s72, vcc_lo, s77\n"
"s_add_u32 s72, s72, s33\n"
"s_add_u32 s33, s33, vcc_lo\n"
"L_PACEL11_%=:\n"
"s_sleep 1\n"
"s_memrealtime vcc\n"
"s_waitcnt lgkmcnt(0)\n"
"s_sub_u32 vcc_hi, s33, vcc_lo\n"
"s_cmp_gt_i32 vcc_hi, -4\n"
"s_cbranch_scc1 L_PACEL11_%=\n"
"s_mov_b32 s33, 0\n"
"s_branch L_PACEB11_%=\n"
"L_PACE12_%=:\n"
"s_min_i32 s33, s33, s77\n"
"s_add_u32 s72, vcc_lo, s77\n"
"s_add_u32 s72, s72, s33\n"
"s_add_u32 s33, s33, vcc_lo\n"
"L_PACEL12_%=:\n"
"s_sleep 1\n"
"s_memrealtime vcc\n"
"s_waitcnt lgkmcnt(0)\n"
"s_sub_u32 vcc_hi, s33, vcc_lo\n"
"s_cmp_gt_i32 vcc_hi, -4\n"
"s_cbranch_scc1 L_PACEL12_%=\n"
"s_mov_b32 s33, 0\n"
"s_branch L_PACEB12_%=\n"
"L_PACE13_%=:\n"
"s_min_i32 s33, s33, s77\n"
"s_add_u32 s72, vcc_lo, s77\n"
"s_add_u32 s72, s72, s33\n"
"s_add_u32 s33, s33, vcc_lo\n"
"L_PACEL13_%=:\n"
"s_sleep 1\n"
"s_memrealtime vcc\n"
"s_waitcnt lgkmcnt(0)\n"
"s_sub_u32 vcc_hi, s33, vcc_lo\n"
"s_cmp_gt_i32 vcc_hi, -4\n"
"s_cbranch_scc1 L_PACEL13_%=\n"
"s_mov_b32 s33, 0\n"
"s_branch L_PACEB13_%=\n"
"L_PACE14_%=:\n"
"s_min_i32 s33, s33, s77\n"
"s_add_u32 s72, vcc_lo, s77\n"
"s_add_u32 s72, s72, s33\n"
"s_add_u32 s33, s33, vcc_lo\n"
"L_PACEL14_%=:\n"
"s_sleep 1\n"
"s_memrealtime vcc\n"
"s_waitcnt lgkmcnt(0)\n"
"s_sub_u32 vcc_hi, s33, vcc_lo\n"
"s_cmp_gt_i32 vcc_hi, -4\n"
"s_cbranch_scc1 L_PACEL14_%=\n"
"s_mov_b32 s33, 0\n"
"s_branch L_PACEB14_%=\n"
"L_PACE15_%=:\n"
"s_min_i32 s33, s33, s77\n"
"s_add_u32 s72, vcc_lo, s77\n"
"s_add_u32 s72, s72, s33\n"
"s_add_u32 s33, s33, vcc_lo\n"
"L_PACEL15_%=:\n"
"s_sleep 1\n"
"s_memrealtime vcc\n"
"s_waitcnt lgkmcnt(0)\n"
"s_sub_u32 vcc_hi, s33, vcc_lo\n"
"s_cmp_gt_i32 vcc_hi, -4\n"
"s_cbranch_scc1 L_PACEL15_%=\n"
"s_mov_b32 s33, 0\n"
"s_branch L_PACEB15_%=\n"
"L_PACE16_%=:\n"
"s_min_i32 s33, s33, s77\n"
"s_add_u32 s72, vcc_lo, s77\n"
"s_add_u32 s72, s72, s33\n"
"s_add_u32 s33, s33, vcc_lo\n"
"L_PACEL16_%=:\n"
"s_sleep 1\n"
"s_memrealtime vcc\n"
"s_waitcnt lgkmcnt(0)\n"
"s_sub_u32 vcc_hi, s33, vcc_lo\n"
"s_cmp_gt_i32 vcc_hi, -4\n"
"s_cbranch_scc1 L_PACEL16_%=\n"
"s_mov_b32 s33, 0\n"
"s_branch L_PACEB16_%=\n"
"L_SWEEP0_%=:\n"
"s_mov_b32 s80, 0\n"
"s_mov_b32 s72, 32\n"
"v_add_u32 v249, 131072, v249\n"
"v_add_u32 v250, 131072, v250\n"
"v_mov_b32 v244, 0xff800000\n"
"v_mov_b32 v245, 0xff800000\n"
"v_mov_b32 v246, 0xff800000\n"
"v_mov_b32 v247, 0xff800000\n"
"L_SW0_%=:\n"
"s_add_u32 m0, s69, 131072\n"
"s_nop 0\n"
"buffer_load_dwordx4 v253, s[44:47], s80 offen lds\n"
"s_add_u32 m0, s69, 131968\n"
"s_nop 0\n"
"buffer_load_dwordx4 v253, s[44:47], s80 offen offset:128 lds\n"
"s_waitcnt vmcnt(0)\n"
"s_barrier\n"
"ds_read_b128 v[160:163], v249 offset:0\n"
"ds_read_b128 v[164:167], v250 offset:0\n"
"ds_read_b128 v[168:171], v249 offset:1024\n"
"ds_read_b128 v[172:175], v250 offset:1024\n"
"s_waitcnt lgkmcnt(0)\n"
"MZ v,128,v,160,a,0\n"
"MZ v,132,v,160,a,16\n"
"MZ v,136,v,160,a,32\n"
"MZ v,140,v,160,a,48\n"
"MA v,128,v,164,a,4\n"
"MA v,132,v,164,a,20\n"
"MA v,136,v,164,a,36\n"
"MA v,140,v,164,a,52\n"
"MA v,128,v,168,a,8\n"
"MA v,132,v,168,a,24\n"
"MA v,136,v,168,a,40\n"
"MA v,140,v,168,a,56\n"
"MA v,128,v,172,a,12\n"
"MA v,132,v,172,a,28\n"
"MA v,136,v,172,a,44\n"
"MA v,140,v,172,a,60\n"
"ds_read_b128 v[160:163], v249 offset:4096\n"
"ds_read_b128 v[164:167], v250 offset:4096\n"
"ds_read_b128 v[168:171], v249 offset:5120\n"
"ds_read_b128 v[172:175], v250 offset:5120\n"
"s_waitcnt lgkmcnt(0)\n"
"MZ v,144,v,160,a,0\n"
"MZ v,148,v,160,a,16\n"
"MZ v,152,v,160,a,32\n"
"MZ v,156,v,160,a,48\n"
"MA v,144,v,164,a,4\n"
"MA v,148,v,164,a,20\n"
"MA v,152,v,164,a,36\n"
"MA v,156,v,164,a,52\n"
"MA v,144,v,168,a,8\n"
"MA v,148,v,168,a,24\n"
"MA v,152,v,168,a,40\n"
"MA v,156,v,168,a,56\n"
"MA v,144,v,172,a,12\n"
"MA v,148,v,172,a,28\n"
"MA v,152,v,172,a,44\n"
"MA v,156,v,172,a,60\n"
"s_barrier\n"
"s_nop 7\n"
"v_max_f32 v244, v244, v128\n"
"v_max_f32 v244, v244, v129\n"
"v_max_f32 v244, v244, v130\n"
"v_max_f32 v244, v244, v131\n"
"v_max_f32 v245, v245, v132\n"
"v_max_f32 v245, v245, v133\n"
"v_max_f32 v245, v245, v134\n"
"v_max_f32 v245, v245, v135\n"
"v_max_f32 v246, v246, v136\n"
"v_max_f32 v246, v246, v137\n"
"v_max_f32 v246, v246, v138\n"
"v_max_f32 v246, v246, v139\n"
"v_max_f32 v247, v247, v140\n"
"v_max_f32 v247, v247, v141\n"
"v_max_f32 v247, v247, v142\n"
"v_max_f32 v247, v247, v143\n"
"v_max_f32 v244, v244, v144\n"
"v_max_f32 v244, v244, v145\n"
"v_max_f32 v244, v244, v146\n"
"v_max_f32 v244, v244, v147\n"
"v_max_f32 v245, v245, v148\n"
"v_max_f32 v245, v245, v149\n"
"v_max_f32 v245, v245, v150\n"
"v_max_f32 v245, v245, v151\n"
"v_max_f32 v246, v246, v152\n"
"v_max_f32 v246, v246, v153\n"
"v_max_f32 v246, v246, v154\n"
"v_max_f32 v246, v246, v155\n"
"v_max_f32 v247, v247, v156\n"
"v_max_f32 v247, v247, v157\n"
"v_max_f32 v247, v247, v158\n"
"v_max_f32 v247, v247, v159\n"
"s_add_u32 s80, s80, 8192\n"
"s_sub_u32 s72, s72, 1\n"
"s_cmp_lg_u32 s72, 0\n"
"s_cbranch_scc1 L_SW0_%=\n"
"v_accvgpr_write_b32 a228, v244\n"
"v_accvgpr_write_b32 a229, v245\n"
"v_accvgpr_write_b32 a230, v246\n"
"v_accvgpr_write_b32 a231, v247\n"
"v_subrev_u32 v249, 131072, v249\n"
"v_subrev_u32 v250, 131072, v250\n"
"s_nop 1\n"
"s_branch L_NOSWEEP_%=\n"
"L_SWEEP1_%=:\n"
"s_mov_b32 s80, 262144\n"
"s_mov_b32 s72, 32\n"
"v_add_u32 v249, 131072, v249\n"
"v_add_u32 v250, 131072, v250\n"
"v_mov_b32 v244, 0xff800000\n"
"v_mov_b32 v245, 0xff800000\n"
"v_mov_b32 v246, 0xff800000\n"
"v_mov_b32 v247, 0xff800000\n"
"L_SW1_%=:\n"
"s_add_u32 m0, s69, 131072\n"
"s_nop 0\n"
"buffer_load_dwordx4 v253, s[44:47], s80 offen lds\n"
"s_add_u32 m0, s69, 131968\n"
"s_nop 0\n"
"buffer_load_dwordx4 v253, s[44:47], s80 offen offset:128 lds\n"
"s_waitcnt vmcnt(0)\n"
"s_barrier\n"
"RD 144,249,0\n"
"RD 148,250,0\n"
"RD 152,249,1024\n"
"RD 156,250,1024\n"
"s_waitcnt lgkmcnt(0)\n"
"MZ a,160,a,144,a,64\n"
"MZ a,164,a,144,a,80\n"
"MZ a,168,a,144,a,96\n"
"MZ a,172,a,144,a,112\n"
"MA a,160,a,148,a,68\n"
"MA a,164,a,148,a,84\n"
"MA a,168,a,148,a,100\n"
"MA a,172,a,148,a,116\n"
"MA a,160,a,152,a,72\n"
"MA a,164,a,152,a,88\n"
"MA a,168,a,152,a,104\n"
"MA a,172,a,152,a,120\n"
"MA a,160,a,156,a,76\n"
"MA a,164,a,156,a,92\n"
"MA a,168,a,156,a,108\n"
"MA a,172,a,156,a,124\n"
"RD 144,249,4096\n"
"RD 148,250,4096\n"
"RD 152,249,5120\n"
"RD 156,250,5120\n"
"s_waitcnt lgkmcnt(0)\n"
"MZ a,176,a,144,a,64\n"
"MZ a,180,a,144,a,80\n"
"MZ a,184,a,144,a,96\n"
"MZ a,188,a,144,a,112\n"
"MA a,176,a,148,a,68\n"
"MA a,180,a,148,a,84\n"
"MA a,184,a,148,a,100\n"
"MA a,188,a,148,a,116\n"
"MA a,176,a,152,a,72\n"
"MA a,180,a,152,a,88\n"
"MA a,184,a,152,a,104\n"
"MA a,188,a,152,a,120\n"
"MA a,176,a,156,a,76\n"
"MA a,180,a,156,a,92\n"
"MA a,184,a,156,a,108\n"
"MA a,188,a,156,a,124\n"
"s_barrier\n"
"s_nop 7\n"
"v_accvgpr_read_b32 v248, a160\n"
"v_max_f32 v244, v244, v248\n"
"v_accvgpr_read_b32 v248, a161\n"
"v_max_f32 v244, v244, v248\n"
"v_accvgpr_read_b32 v248, a162\n"
"v_max_f32 v244, v244, v248\n"
"v_accvgpr_read_b32 v248, a163\n"
"v_max_f32 v244, v244, v248\n"
"v_accvgpr_read_b32 v248, a164\n"
"v_max_f32 v245, v245, v248\n"
"v_accvgpr_read_b32 v248, a165\n"
"v_max_f32 v245, v245, v248\n"
"v_accvgpr_read_b32 v248, a166\n"
"v_max_f32 v245, v245, v248\n"
"v_accvgpr_read_b32 v248, a167\n"
"v_max_f32 v245, v245, v248\n"
"v_accvgpr_read_b32 v248, a168\n"
"v_max_f32 v246, v246, v248\n"
"v_accvgpr_read_b32 v248, a169\n"
"v_max_f32 v246, v246, v248\n"
"v_accvgpr_read_b32 v248, a170\n"
"v_max_f32 v246, v246, v248\n"
"v_accvgpr_read_b32 v248, a171\n"
"v_max_f32 v246, v246, v248\n"
"v_accvgpr_read_b32 v248, a172\n"
"v_max_f32 v247, v247, v248\n"
"v_accvgpr_read_b32 v248, a173\n"
"v_max_f32 v247, v247, v248\n"
"v_accvgpr_read_b32 v248, a174\n"
"v_max_f32 v247, v247, v248\n"
"v_accvgpr_read_b32 v248, a175\n"
"v_max_f32 v247, v247, v248\n"
"v_accvgpr_read_b32 v248, a176\n"
"v_max_f32 v244, v244, v248\n"
"v_accvgpr_read_b32 v248, a177\n"
"v_max_f32 v244, v244, v248\n"
"v_accvgpr_read_b32 v248, a178\n"
"v_max_f32 v244, v244, v248\n"
"v_accvgpr_read_b32 v248, a179\n"
"v_max_f32 v244, v244, v248\n"
"v_accvgpr_read_b32 v248, a180\n"
"v_max_f32 v245, v245, v248\n"
"v_accvgpr_read_b32 v248, a181\n"
"v_max_f32 v245, v245, v248\n"
"v_accvgpr_read_b32 v248, a182\n"
"v_max_f32 v245, v245, v248\n"
"v_accvgpr_read_b32 v248, a183\n"
"v_max_f32 v245, v245, v248\n"
"v_accvgpr_read_b32 v248, a184\n"
"v_max_f32 v246, v246, v248\n"
"v_accvgpr_read_b32 v248, a185\n"
"v_max_f32 v246, v246, v248\n"
"v_accvgpr_read_b32 v248, a186\n"
"v_max_f32 v246, v246, v248\n"
"v_accvgpr_read_b32 v248, a187\n"
"v_max_f32 v246, v246, v248\n"
"v_accvgpr_read_b32 v248, a188\n"
"v_max_f32 v247, v247, v248\n"
"v_accvgpr_read_b32 v248, a189\n"
"v_max_f32 v247, v247, v248\n"
"v_accvgpr_read_b32 v248, a190\n"
"v_max_f32 v247, v247, v248\n"
"v_accvgpr_read_b32 v248, a191\n"
"v_max_f32 v247, v247, v248\n"
"s_add_u32 s80, s80, 8192\n"
"s_sub_u32 s72, s72, 1\n"
"s_cmp_lg_u32 s72, 0\n"
"s_cbranch_scc1 L_SW1_%=\n"
"v_accvgpr_write_b32 a232, v244\n"
"v_accvgpr_write_b32 a233, v245\n"
"v_accvgpr_write_b32 a234, v246\n"
"v_accvgpr_write_b32 a235, v247\n"
"v_subrev_u32 v249, 131072, v249\n"
"v_subrev_u32 v250, 131072, v250\n"
"s_nop 1\n"
"s_branch L_P3_%=\n"
"L_END_%=:\n"
"s_nop 0\n"
".purgem MA\n"
".purgem MZ\n"
".purgem MC\n"
".purgem TR\n"
".purgem RD\n"
".purgem EX\n"
".purgem AD\n"
".purgem CV\n"
".purgem LG\n"
    :
    : [ka] "s"(ka), [bid] "s"(bid), [wave] "s"(wave)
    : FULL_CLOBBER);
}


extern "C" void kernel_launch(void* const* d_in, const int* in_sizes, int n_in, void* d_out, int out_size, void* d_ws, size_t ws_size, hipStream_t stream) {
  (void)in_sizes; (void)n_in; (void)out_size; (void)ws_size;
  (void)hipFuncSetAttribute((const void*)mha_fused, hipFuncAttributeMaxDynamicSharedMemorySize, 163840);


  hipLaunchKernelGGL(mha_fused, dim3(256), dim3(256), 163840, stream,
                     (const char*)d_in[0], (const char*)d_in[1], (const char*)d_in[2], (const char*)d_in[3], (const char*)d_in[4],
                     (const char*)d_in[5], (const char*)d_in[6], (const char*)d_in[7], (const char*)d_in[8], (const char*)d_in[9], (char*)d_out, (char*)d_ws);
}
```
